```python
import torch

def _softmax(x, axis=-1):
    return torch.softmax(x, dim=axis)
def _gelu(x):
    return torch.nn.functional.gelu(x, approximate="tanh")
def _silu(x):
    return torch.nn.functional.silu(x)
def _sigmoid(x):
    return torch.sigmoid(x)
def _layernorm(x, axis=-1, eps=1e-5):
    m = x.mean(dim=axis, keepdim=True)
    v = x.var(dim=axis, keepdim=True, unbiased=False)
    return (x - m) / torch.sqrt(v + eps)
def _scatter_add(tgt, idx, src, axis=0):
    return tgt.clone().index_add_(axis, idx, src)
def _segment_sum(src, seg, n):
    out = torch.zeros((n,) + src.shape[1:], dtype=src.dtype)
    return out.index_add_(0, seg, src)
def _pad(x, pw):
    flat = tuple(v for pair in reversed(pw) for v in pair)
    return torch.nn.functional.pad(x, flat)

kernel_name = 'recurrent_scan__ssm_conv_h3__dense_activation_silu__prompt'

def setup_inputs():

    _w = (lambda a: (lambda t: t.to(torch.bfloat16) if t.is_floating_point() else t)(torch.as_tensor(a)))
    def _A_setup():
        B, S, D = 1, 2048, 640
        ks = 4
        return {
            "q": _w((lambda *s: torch.randn(*s, dtype=torch.bfloat16) * 0.05)(B, S, D)),
            "k": _w((lambda *s: torch.randn(*s, dtype=torch.bfloat16) * 0.8)(B, S, D)),
            "v": _w((lambda *s: torch.randn(*s, dtype=torch.bfloat16) * 0.8)(B, S, D)),
            "shift_k": _w((lambda *s: torch.randn(*s, dtype=torch.bfloat16) * 0.8)(D, ks) * 0.5),
            "ssm_k": _w((lambda *s: torch.randn(*s, dtype=torch.bfloat16) * 0.8)(D, S) * (1.0 / S ** 0.5)),
            "ssm_D": _w((lambda *s: torch.randn(*s, dtype=torch.bfloat16) * 0.8)(D)),
        }
    _A_in = _A_setup()

    def _B_setup():
        B, S, D, F = 1, 2048, 640, 1728
        return {
            "x": _w((lambda *s: torch.randn(*s, dtype=torch.bfloat16) * 0.05)(B, S, D)),
            "w1": _w((lambda *s: torch.randn(*s, dtype=torch.bfloat16) * 0.8)(D, F) * (D ** -0.5)),
            "w2": _w((lambda *s: torch.randn(*s, dtype=torch.bfloat16) * 0.2)(F, D) * (F ** -0.5)),
        }
    _B_in = _B_setup()

    return {
        "A_q": _A_in["q"],
        "A_k": _A_in["k"],
        "A_v": _A_in["v"],
        "A_shift_k": _A_in["shift_k"],
        "A_ssm_k": _A_in["ssm_k"],
        "A_ssm_D": _A_in["ssm_D"],
        "B_x": _B_in["x"],
        "B_w1": _B_in["w1"],
        "B_w2": _B_in["w2"],
    }

def reference(A_q, A_k, A_v, A_shift_k, A_ssm_k, A_ssm_D, B_x, B_w1, B_w2):
    def _A_ref(q, k, v, shift_k, ssm_k, ssm_D):
        B, S, D = q.shape
        ks = shift_k.shape[-1]
        k_pad = _pad(k, ((0, 0), (ks - 1, 0), (0, 0)))
        k_shift = torch.zeros_like(k)
        for t in range(ks):
            k_shift = k_shift + k_pad[:, t:t + S, :] * shift_k[:, ks - 1 - t][None, None, :]
        kv = k_shift * v
        L = 2 * S
        kv_pad = _pad(kv, ((0, 0), (0, S), (0, 0)))
        kf_pad = _pad(ssm_k, ((0, 0), (0, S)))
        kv_f = (lambda x, axes=(-2,-1): torch.fft.rfft2(x.float(), dim=axes))(kv_pad[:, None, :, :], axes=(1, 2))
        kf_f = (lambda x, axes=(-2,-1): torch.fft.rfft2(x.float(), dim=axes))(kf_pad[:, None, :], axes=(1, 2))
        kf_f = (lambda x, *d: x.permute(*d))(kf_f, 1, 2, 0)
        y_f = kv_f * kf_f[None, :, :, :]
        y = (lambda x, s, axes=(-2,-1): torch.fft.irfft2(x, s=s, dim=axes).to(torch.bfloat16))(y_f, (1, L), axes=(1, 2))[:, 0, :S, :]
        y = y + kv * ssm_D[None, None, :]
        return q * y

    def _B_ref(x, w1, w2):
        h = torch.einsum("bsd,df->bsf", x, w1)
        h = _silu(h)
        out = torch.einsum("bsf,fd->bsd", h, w2)
        return out

    x = A_q
    xn = _layernorm(x, axis=-1)
    s = _A_ref(xn, A_k, A_v, A_shift_k, A_ssm_k, A_ssm_D)
    g = _silu(_B_ref(xn, B_w1, B_w2))
    return x + s * g
```

```cpp
#include <hip/hip_runtime.h>
#include <hip/hip_bf16.h>
#include <stdint.h>

constexpr int S = 2048, D = 640, F = 1728, KS = 4;
typedef unsigned short u16;
__device__ __forceinline__ float bf2f(u16 b) { return __builtin_bit_cast(float, (unsigned)b << 16); }
__device__ __forceinline__ u16 f2bf(float f) { __hip_bfloat16 h = __float2bfloat16(f); return __builtin_bit_cast(u16, h); }
__device__ __forceinline__ float rbf(float f) { return bf2f(f2bf(f)); }

__global__ void k_prep(const u16* q, const u16* k, const u16* v, const u16* shk, u16* xn, u16* kv) {
  int t = blockIdx.x; int tid = threadIdx.x;
  float s = 0.f;
  for (int d = tid; d < D; d += 64) s += bf2f(q[t * D + d]);
  for (int o = 32; o; o >>= 1) s += __shfl_xor(s, o);
  float mean = s / D;
  float ss = 0.f;
  for (int d = tid; d < D; d += 64) { float e = bf2f(q[t * D + d]) - mean; ss += e * e; }
  for (int o = 32; o; o >>= 1) ss += __shfl_xor(ss, o);
  float mb = rbf(mean), vb = rbf(ss / D);
  float sd = rbf(sqrtf(rbf(vb + 1e-5f)));
  for (int d = tid; d < D; d += 64) {
    xn[t * D + d] = f2bf(rbf(bf2f(q[t * D + d]) - mb) / sd);
    float ks = 0.f;
    for (int j = 0; j < KS; ++j) {
      int tt = t + j - (KS - 1);
      float kk = tt >= 0 ? bf2f(k[tt * D + d]) : 0.f;
      ks = rbf(ks + rbf(kk * bf2f(shk[d * KS + KS - 1 - j])));
    }
    kv[t * D + d] = f2bf(ks * bf2f(v[t * D + d]));
  }
}
__global__ void k_gemm1(const u16* xn, const u16* w1, u16* h) {
  int f = blockIdx.x * 64 + threadIdx.x; int t = blockIdx.y;
  float acc = 0.f;
  for (int d = 0; d < D; ++d) acc += bf2f(xn[t * D + d]) * bf2f(w1[d * F + f]);
  float a = rbf(acc);
  h[t * F + f] = f2bf(a / (1.f + __expf(-a)));
}
__global__ void k_conv(const u16* kv, const u16* sk, const u16* sD, u16* y) {
  int d = blockIdx.x * 64 + threadIdx.x; int t = blockIdx.y;
  float acc = 0.f;
  for (int u = 0; u <= t; ++u) acc += bf2f(kv[u * D + d]) * bf2f(sk[d * S + (t - u)]);
  y[t * D + d] = f2bf(rbf(acc) + rbf(bf2f(kv[t * D + d]) * bf2f(sD[d])));
}
__global__ void k_gemm2(const u16* h, const u16* w2, const u16* q, const u16* xn, const u16* y, u16* out) {
  int d = blockIdx.x * 64 + threadIdx.x; int t = blockIdx.y;
  float acc = 0.f;
  for (int f = 0; f < F; ++f) acc += bf2f(h[t * F + f]) * bf2f(w2[f * D + d]);
  float o = rbf(acc);
  float g = rbf(o / (1.f + __expf(-o)));
  float s = rbf(bf2f(xn[t * D + d]) * bf2f(y[t * D + d]));
  out[t * D + d] = f2bf(bf2f(q[t * D + d]) + rbf(s * g));
}

extern "C" void kernel_launch(void* const* d_in, const int* in_sizes, int n_in,
                              void* d_out, int out_size, void* d_ws, size_t ws_size,
                              hipStream_t stream) {
  const u16* q = (const u16*)d_in[0]; const u16* k = (const u16*)d_in[1]; const u16* v = (const u16*)d_in[2];
  const u16* shk = (const u16*)d_in[3]; const u16* sk = (const u16*)d_in[4]; const u16* sD = (const u16*)d_in[5];
  const u16* w1 = (const u16*)d_in[7]; const u16* w2 = (const u16*)d_in[8];
  u16* xn = (u16*)d_ws; u16* kv = xn + S * D; u16* y = kv + S * D; u16* h = y + S * D;
  k_prep<<<S, 64, 0, stream>>>(q, k, v, shk, xn, kv);
  k_gemm1<<<dim3(F / 64, S), 64, 0, stream>>>(xn, w1, h);
  k_conv<<<dim3(D / 64, S), 64, 0, stream>>>(kv, sk, sD, y);
  k_gemm2<<<dim3(D / 64, S), 64, 0, stream>>>(h, w2, q, xn, y, (u16*)d_out);
}
```

```cpp
#include <hip/hip_runtime.h>
#include <stdint.h>

typedef unsigned short u16;
typedef unsigned v4u __attribute__((ext_vector_type(4)));
typedef unsigned v2u __attribute__((ext_vector_type(2)));
typedef float f32x4 __attribute__((ext_vector_type(4)));
typedef float f32x2 __attribute__((ext_vector_type(2)));
typedef short bf16x8 __attribute__((ext_vector_type(8)));
typedef short v4s __attribute__((ext_vector_type(4)));
typedef __bf16 v2bf __attribute__((ext_vector_type(2)));
#define LAS __attribute__((address_space(3)))
#define GAS __attribute__((address_space(1)))
#define DEV __device__ __forceinline__
typedef const unsigned char* cptr;

constexpr int S = 2048, D = 640, F = 1728;
constexpr int NWG = 256, NTHR = 768;
constexpr unsigned WS_EPOCH = 0;
constexpr unsigned WS_FP0 = 1024;
constexpr unsigned WS_FH = 4096;
constexpr unsigned WS_FY = 16384;
constexpr unsigned WS_STATS = 32768;
constexpr unsigned KVR_ROW = 4224;
constexpr unsigned WS_KVR = 65536;
constexpr unsigned WS_W2T = WS_KVR + 640 * KVR_ROW;
constexpr unsigned WS_HF = WS_W2T + 8 * 54 * 5 * 1024;
constexpr unsigned WS_YT = WS_HF + 128 * 54 * 1024;
constexpr unsigned WS_END = WS_YT + 640 * 4096;
constexpr unsigned WS_DBG = WS_END;
constexpr unsigned L_E = 131072;
constexpr unsigned L_STATS = 30720;
constexpr unsigned L_CS = 32768 + 30720;
constexpr unsigned WSLOT = 32768;
constexpr unsigned CH_R1 = 0, CH_Z1 = 4096, CH_R0 = 4128, CH_Z0 = 8224, CH_H0 = 8240, CH_PF = 12416, CH_H1 = 12432, CH_BYTES = 16528;
constexpr unsigned L_CONV = 163840 - 2 * CH_BYTES;
constexpr unsigned L_Y = L_E;
constexpr unsigned L_O = 0;
constexpr unsigned OROW = 336;
constexpr unsigned SPIN_CAP = 1u << 16;
constexpr int AUX_SC1 = 16;

DEV float bf_lo(unsigned u) { return __builtin_bit_cast(float, u << 16); }
DEV float bf_hi(unsigned u) { return __builtin_bit_cast(float, u & 0xffff0000u); }
DEV unsigned pk_bf(float a, float b) { v2bf r = {(__bf16)a, (__bf16)b}; return __builtin_bit_cast(unsigned, r); }
DEV float rbf(float a) { return bf_lo(pk_bf(a, a) & 0xffffu); }
DEV f32x4 mfma(v4u a, v4u b, f32x4 c) {
  return __builtin_amdgcn_mfma_f32_16x16x32_bf16(__builtin_bit_cast(bf16x8, a), __builtin_bit_cast(bf16x8, b), c, 0, 0, 0);
}
DEV float silu(float a) { return a * __builtin_amdgcn_rcpf(1.0f + __builtin_amdgcn_exp2f(a * -1.44269504088896f)); }

template <int IMM> DEV void dma16(const void* sbase, unsigned voff, unsigned ldsoff) {
  asm volatile("s_mov_b32 m0, %2\n\ts_nop 0\n\tglobal_load_lds_dwordx4 %0, %1 offset:%3" ::"v"(voff), "s"(sbase), "s"(ldsoff), "i"(IMM) : "memory");
}
template <int IMM> DEV void dma16s(const void* sbase, unsigned voff, unsigned ldsoff) {
  asm volatile("s_mov_b32 m0, %2\n\ts_nop 0\n\tglobal_load_lds_dwordx4 %0, %1 offset:%3 sc1" ::"v"(voff), "s"(sbase), "s"(ldsoff), "i"(IMM) : "memory");
}
DEV void dma4k(const void* sbase, unsigned voff, unsigned ldsoff) {
  asm volatile("s_mov_b32 m0, %2\n\ts_nop 0\n\tglobal_load_lds_dwordx4 %0, %1\n\tglobal_load_lds_dwordx4 %0, %1 offset:1024\n\t"
               "global_load_lds_dwordx4 %0, %1 offset:2048\n\tglobal_load_lds_dwordx4 %0, %1 offset:3072" ::"v"(voff), "s"(sbase), "s"(ldsoff) : "memory");
}
DEV void dma4ks(const void* sbase, unsigned voff, unsigned ldsoff) {
  asm volatile("s_mov_b32 m0, %2\n\ts_nop 0\n\tglobal_load_lds_dwordx4 %0, %1 sc1\n\tglobal_load_lds_dwordx4 %0, %1 offset:1024 sc1\n\t"
               "global_load_lds_dwordx4 %0, %1 offset:2048 sc1\n\tglobal_load_lds_dwordx4 %0, %1 offset:3072 sc1" ::"v"(voff), "s"(sbase), "s"(ldsoff) : "memory");
}
DEV void dma3ks(const void* sbase, unsigned voff, unsigned ldsoff) {
  asm volatile("s_mov_b32 m0, %2\n\ts_nop 0\n\tglobal_load_lds_dwordx4 %0, %1 offset:1024 sc1\n\t"
               "global_load_lds_dwordx4 %0, %1 offset:2048 sc1\n\tglobal_load_lds_dwordx4 %0, %1 offset:3072 sc1" ::"v"(voff), "s"(sbase), "s"(ldsoff) : "memory");
}
DEV v4u ld128a(const void* sbase, unsigned voff) { v4u r; asm volatile("global_load_dwordx4 %0, %1, %2 sc1" : "=v"(r) : "v"(voff), "s"(sbase) : "memory"); return r; }
DEV unsigned ld32a(const void* sbase, unsigned voff) { unsigned r; asm volatile("global_load_dword %0, %1, %2 sc1" : "=v"(r) : "v"(voff), "s"(sbase) : "memory"); return r; }
#define WAITV(n) asm volatile("s_waitcnt vmcnt(" #n ")" ::: "memory")
#define USE(x) asm volatile("" : "+v"(x))
#define BAR0() do { asm volatile("s_waitcnt lgkmcnt(0)" ::: "memory"); __builtin_amdgcn_s_barrier(); asm volatile("" ::: "memory"); } while (0)
#define SGB(m, n) __builtin_amdgcn_sched_group_barrier(m, n, 0)

DEV v4u lds128(unsigned a) { return *(const LAS v4u*)(size_t)a; }
DEV v2u lds64(unsigned a) { return *(const LAS v2u*)(size_t)a; }
DEV v2u lds64v(unsigned a) { return *(const volatile LAS v2u*)(size_t)a; }
DEV unsigned lds32(unsigned a) { return *(const LAS unsigned*)(size_t)a; }
DEV void sts128(unsigned a, v4u v) { *(LAS v4u*)(size_t)a = v; }
DEV void sts64(unsigned a, v2u v) { *(LAS v2u*)(size_t)a = v; }
DEV void sts16(unsigned a, unsigned v) { *(LAS u16*)(size_t)a = (u16)v; }
DEV v2u ldstr(unsigned a) { return __builtin_bit_cast(v2u, __builtin_amdgcn_ds_read_tr16_b64_v4i16((LAS v4s*)(size_t)a)); }

DEV float wave_sum(float v) {
#pragma unroll
  for (int o = 32; o; o >>= 1) v += __shfl_xor(v, o);
  return v;
}

#define BAR() BAR0()
#define STAMPN()

struct Params {
  const u16 *x, *k, *v, *shk, *sk, *sD, *w1, *w2;
  u16* out;
  unsigned char* ws;
};

template <int N> DEV bool wait_eq1(unsigned v, unsigned epoch) {
  unsigned d;
  asm volatile("s_waitcnt vmcnt(%3)\n\tv_xor_b32 %0, %2, %1" : "=v"(d) : "v"(v), "s"(epoch), "i"(N) : "memory");
  return __all(d == 0u);
}
template <int N> DEV bool wait_eq4(v4u v, unsigned epoch) {
  unsigned d, e;
  asm volatile("s_waitcnt vmcnt(%7)\n\tv_xor_b32 %0, %6, %2\n\tv_xor_b32 %1, %6, %3\n\tv_or_b32 %0, %0, %1\n\tv_xor_b32 %1, %6, %4\n\t"
               "v_or_b32 %0, %0, %1\n\tv_xor_b32 %1, %6, %5\n\tv_or_b32 %0, %0, %1"
               : "=&v"(d), "=&v"(e) : "v"(v.x), "v"(v.y), "v"(v.z), "v"(v.w), "s"(epoch), "i"(N) : "memory");
  return __all(d == 0u);
}
DEV void poll1(cptr base, unsigned off, unsigned epoch) {
#pragma unroll 1
  for (unsigned spins = 0; spins < SPIN_CAP; ++spins) {
    unsigned v = ld32a(base, off);
    if (wait_eq1<0>(v, epoch)) break;
    __builtin_amdgcn_s_sleep(1);
  }
}
DEV void poll4(cptr base, unsigned off, unsigned epoch) {
#pragma unroll 1
  for (unsigned spins = 0; spins < SPIN_CAP; ++spins) {
    v4u v = ld128a(base, off);
    if (wait_eq4<0>(v, epoch)) break;
    __builtin_amdgcn_s_sleep(1);
  }
}
template <int N> DEV unsigned epoch_fin(unsigned ev) {
  unsigned e;
  asm volatile("s_waitcnt vmcnt(%2)\n\tv_readfirstlane_b32 %0, %1\n\ts_nop 4" : "=s"(e) : "v"(ev), "i"(N) : "memory");
  return e * 2654435761u + 0x9E3779B9u;
}

DEV void conv_final(__amdgpu_buffer_rsrc_t rs, unsigned db, int ch, int chunk, v4u kvr, float dD) {
  const unsigned a = db + chunk * 32;
  f32x4 p0 = __builtin_bit_cast(f32x4, lds128(a)), p1 = __builtin_bit_cast(f32x4, lds128(a + 16));
  f32x4 q0 = __builtin_bit_cast(f32x4, lds128(a + 8192)), q1 = __builtin_bit_cast(f32x4, lds128(a + 8192 + 16));
  p0 += q0; p1 += q1;
  unsigned y01 = pk_bf(p0[0], p0[1]), y23 = pk_bf(p0[2], p0[3]), y45 = pk_bf(p1[0], p1[1]), y67 = pk_bf(p1[2], p1[3]);
  unsigned d01 = pk_bf(bf_hi(kvr.w) * dD, bf_lo(kvr.w) * dD), d23 = pk_bf(bf_hi(kvr.z) * dD, bf_lo(kvr.z) * dD);
  unsigned d45 = pk_bf(bf_hi(kvr.y) * dD, bf_lo(kvr.y) * dD), d67 = pk_bf(bf_hi(kvr.x) * dD, bf_lo(kvr.x) * dD);
  v4u o;
  o.x = pk_bf(bf_lo(y01) + bf_lo(d01), bf_hi(y01) + bf_hi(d01));
  o.y = pk_bf(bf_lo(y23) + bf_lo(d23), bf_hi(y23) + bf_hi(d23));
  o.z = pk_bf(bf_lo(y45) + bf_lo(d45), bf_hi(y45) + bf_hi(d45));
  o.w = pk_bf(bf_lo(y67) + bf_lo(d67), bf_hi(y67) + bf_hi(d67));
  __builtin_amdgcn_raw_buffer_store_b128(o, rs, WS_YT + ch * 4096 + chunk * 16, 0, AUX_SC1);
}
DEV void conv_pads(unsigned cb, int lane, unsigned h01) {
  if (lane < 3) {
    unsigned a = cb + (lane == 0 ? CH_Z1 : lane == 1 ? CH_Z0 : CH_PF);
    sts128(a, v4u{0u, 0u, 0u, lane == 2 ? h01 : 0u});
  }
}

__global__ void __launch_bounds__(NTHR, 3) fused_kernel(Params p) {
  __shared__ __attribute__((aligned(16))) unsigned char smem[163840];
  const int tid = threadIdx.x, lane = tid & 63;
  const int w = __builtin_amdgcn_readfirstlane(tid >> 6);
  const int b = blockIdx.x, xcd = b & 7, idx = b >> 3;
  const int grp = xcd >> 1, half = xcd & 1;
  const bool special = idx >= 28;
  const int rt = 4 * grp + idx / 7, ct = 7 * half + idx % 7;
  const int tt64 = 8 * grp + (idx >> 2), c80 = 4 * half + (idx & 3);
  const int chA = 448 + 6 * (xcd * 4 + idx - 28);
  const int chC = special ? chA + 4 : 2 * (xcd * 28 + idx);
  const __amdgpu_buffer_rsrc_t rs = __builtin_amdgcn_make_buffer_rsrc((void*)p.ws, 0, (int)WS_END, 0x00020000);
  { unsigned lb = (unsigned)(size_t)(LAS unsigned char*)smem; asm volatile("" ::"s"(lb)); }
  const unsigned l16 = lane * 16;
  unsigned stv = 0; int stn = 0; (void)stv; (void)stn;
  STAMPN();
  const unsigned epv = ld32a(p.ws + WS_EPOCH, 0u);
  unsigned epoch;
#define EPOCH_FIN(n) epoch = epoch_fin<n>(epv)
  const int ftok = tid / 10, fc8 = tid % 10;
  const unsigned fgo = (64 * tt64 + (tid < 640 ? ftok : 0)) * 1280 + c80 * 160 + fc8 * 16;
  v4u fx; v2u fst;

  if (w >= 8) {
    const int L = w - 8;
    cptr w2s = p.ws + WS_W2T + c80 * (54 * 5 * 1024) + (L >> 1) * (27 * 5 * 1024) + (L & 1) * 7168;
    const unsigned w2d = (L >> 1) * 15360 + (L & 1) * 7168;
    auto w2group = [&](int g, int slot) {
      dma4ks(w2s + g * 15360, l16, w2d + slot * WSLOT);
      dma4ks(w2s + g * 15360 + 4096, l16, w2d + slot * WSLOT + 4096);
    };
    cptr hsrc = (cptr)p.sk + (size_t)(chC + (L >> 1)) * 4096 + (L & 1) * 4;
    const unsigned hdst = L_CONV + (L >> 1) * CH_BYTES + ((L & 1) ? CH_H1 : CH_H0);
    cptr rsrc = p.ws + WS_KVR + (size_t)(chC + (L >> 1)) * KVR_ROW + (L & 1) * 2;
    const unsigned rdst = L_CONV + (L >> 1) * CH_BYTES + ((L & 1) ? CH_R0 : CH_R1);
    const float dD0 = bf_lo(p.sD[chC]), dD1 = bf_lo(p.sD[chC + 1]);
    const int chunk = tid - 512;
    if (!special) {
      const unsigned vx = (lane >> 3) * 1280 + (((lane & 7) ^ (lane >> 3)) << 4);
      const int cmask = ct == 13 ? 7 : 15;
      unsigned vw[4];
#pragma unroll
      for (int j = 0; j < 4; ++j) vw[j] = (lane >> 4) * 3456 + ((((lane & 15) ^ (((lane >> 4) << 2) | j)) & cmask) << 4) + j * (4 * 3456);
      cptr xb = (cptr)p.x + (size_t)(128 * rt + 32 * L) * 1280;
      cptr wb = (cptr)p.w1 + (size_t)(16 * L) * 3456 + 256 * ct;
      auto stage = [&](int s) {
        const unsigned reg = ((s + 2) & 3) * 32768 + L * 4096;
        cptr xs = xb + 128 * s;
        cptr wsrc = wb + (size_t)s * (64 * 3456);
#pragma unroll
        for (int j = 0; j < 4; ++j) dma16<0>(xs + j * (8 * 1280), vx, reg + j * 1024);
#pragma unroll
        for (int j = 0; j < 4; ++j) dma16<0>(wsrc, vw[j], reg + 16384 + j * 1024);
      };
#pragma unroll 1
      for (int s = 0; s < 4; ++s) stage(s);
      EPOCH_FIN(32);
      BAR();
      WAITV(24); BAR();
      WAITV(16); BAR();
      stage(4);
      WAITV(16); BAR();
      stage(5); dma4k(hsrc, l16, hdst);
      WAITV(20); BAR();
      stage(6);
      WAITV(20); BAR();
      v4u pa = ld128a(p.ws + WS_FP0, l16);
      stage(7);
      WAITV(21); BAR();
      v4u pb = ld128a(p.ws + WS_FP0, l16);
      stage(8);
      WAITV(18); BAR();
      stage(9);
      const bool oka = wait_eq4<17>(pa, epoch);
      BAR();
      {
        const bool okb = wait_eq4<16>(pb, epoch);
        if (!oka && !okb) poll4(p.ws + WS_FP0, l16, epoch);
      }
      if (L != 0) dma16s<0>(rsrc, l16, rdst);
      else dma16s<0>(p.ws + WS_STATS + rt * 1024, l16, L_STATS);
      dma3ks(rsrc, l16, rdst);
      w2group(0, 0);
      WAITV(20); BAR();
      w2group(1, 1);
      WAITV(20); BAR();
      w2group(2, 2);
      WAITV(24); BAR();
      if (L == 0) dma16s<0>(rsrc, l16, rdst);
      if (b == 0 && L == 1 && lane == 0) __builtin_amdgcn_raw_buffer_store_b32(epoch, rs, WS_EPOCH, 0, AUX_SC1);
      w2group(3, 3);
      WAITV(8);
    } else {
      {
        cptr hs = (cptr)p.sk + (size_t)(chA + L) * 4096;
        dma4k(hs, l16, L * CH_BYTES + CH_H0);
        dma4k(hs + 4, l16, L * CH_BYTES + CH_H1);
      }
      EPOCH_FIN(8);
      const float dA = bf_lo(p.sD[chA + L]);
      BAR();
      BAR();
      dma4k(hsrc, l16, hdst);
      poll4(p.ws + WS_FP0, l16, epoch);
      {
        cptr rsA = p.ws + WS_KVR + (size_t)(chA + L) * KVR_ROW;
        dma4ks(rsA, l16, L * CH_BYTES + CH_R1);
        dma4ks(rsA + 2, l16, L * CH_BYTES + CH_R0);
      }
      dma4ks(rsrc, l16, rdst);
      w2group(3, 3);
      WAITV(12); BAR();
      v4u kv4[4];
#pragma unroll
      for (int i = 0; i < 4; ++i) kv4[i] = lds128(L * CH_BYTES + CH_R1 + (255 - 64 * i - lane) * 16);
      BAR();
      BAR();
#pragma unroll
      for (int i = 0; i < 4; ++i) conv_final(rs, L * 16384, chA + L, 64 * i + lane, kv4[i], dA);
      WAITV(0);
      if (lane < 4) __builtin_amdgcn_raw_buffer_store_b32(epoch, rs, WS_FY + (lane * 640 + chA + L) * 4, 0, AUX_SC1);
    }
    BAR();
    if (special) { w2group(0, 0); w2group(1, 1); w2group(2, 2); }
    const v4u kva = lds128(L_CONV + CH_R1 + (255 - chunk) * 16), kvb = lds128(L_CONV + CH_BYTES + CH_R1 + (255 - chunk) * 16);
    BAR();
    WAITV(0); BAR();
    conv_final(rs, L_E, chC, chunk, kva, dD0);
    conv_final(rs, L_E + 16384, chC + 1, chunk, kvb, dD1);
    BAR(); w2group(4, 4); w2group(5, 0);
    BAR(); w2group(6, 1);
    WAITV(24);
    if (lane < 2) __builtin_amdgcn_raw_buffer_store_b32(epoch, rs, WS_FY + (L * 640 + chC + lane) * 4, 0, AUX_SC1);
    BAR(); w2group(7, 2);
    WAITV(25); BAR(); w2group(8, 3);
    const unsigned fyo = ((tt64 >> 3) * 640 + c80 * 80 + L * 20 + (lane < 20 ? lane : 0)) * 4;
    unsigned py = ld32a(p.ws + WS_FY, fyo);
    fx = *(const GAS v4u*)((cptr)p.x + fgo);
    fst = __builtin_amdgcn_raw_buffer_load_b64(rs, WS_STATS + (64 * tt64 + (tid < 640 ? ftok : 0)) * 8, 0, AUX_SC1);
    WAITV(28); BAR();
    WAITV(19); BAR();
    if (!wait_eq1<2>(py, epoch)) poll1(p.ws + WS_FY, fyo, epoch);
    {
      v4u yv[3];
#pragma unroll
      for (int i = 0; i < 3; ++i) {
        int it = i * 64 + lane; if (it >= 160) it = 159;
        yv[i] = __builtin_amdgcn_raw_buffer_load_b128(rs, WS_YT + (c80 * 80 + L * 20 + (it >> 3)) * 4096 + tt64 * 128 + (it & 7) * 16, 0, AUX_SC1);
      }
      BAR();
      BAR();
#pragma unroll
      for (int i = 0; i < 3; ++i) {
        int it = i * 64 + lane; if (it >= 160) it = 159;
        const unsigned a = L_Y + (it & 7) * (8 * 160) + (L * 20 + (it >> 3)) * 2;
        sts16(a, yv[i].x); sts16(a + 160, yv[i].x >> 16); sts16(a + 320, yv[i].y); sts16(a + 480, yv[i].y >> 16);
        sts16(a + 640, yv[i].z); sts16(a + 800, yv[i].z >> 16); sts16(a + 960, yv[i].w); sts16(a + 1120, yv[i].w >> 16);
      }
    }
  } else {
    const bool kvrole = idx < 20;
    const int kvn = xcd * 20 + idx, kt = kvn / 10, kc = kvn % 10;
    const int wn = xcd * 12 + idx - 20;
    const bool w2role = !kvrole && wn < 90;
    if (kvrole) {
#pragma unroll
      for (int j = 0; j < 2; ++j) {
        const int blk = 2 * w + j;
        const unsigned vo = (lane >> 3) * 1280 + (((lane & 7) ^ (blk & 7)) << 4);
        const size_t go = (size_t)(128 * kt + 8 * blk) * 1280 + 128 * kc;
        dma16<0>((cptr)p.k + go, vo, L_E + blk * 1024);
        dma16<0>((cptr)p.v + go, vo, L_E + 16384 + blk * 1024);
      }
    } else if (w2role) {
#pragma unroll
      for (int j = 0; j < 3; ++j) {
        const int pc = 3 * w + j;
        const unsigned vo = (lane >> 4) * 1280 + (((lane & 15) ^ (((lane >> 4) << 2) | (pc & 3))) << 4);
        dma16<0>((cptr)p.w2 + (size_t)(96 * (wn / 5) + 4 * pc) * 1280 + 256 * (wn % 5), vo, L_E + pc * 1024);
      }
    }
    const int srow = 512 * grp + 256 * half + 8 * idx + w;
    v4u xa = *(const GAS v4u*)((cptr)p.x + (size_t)srow * 1280 + l16);
    v4u xb2 = *(const GAS v4u*)((cptr)p.x + (size_t)srow * 1280 + 1024 + (lane & 15) * 16);
    const int tg8 = (lane & 7) + 8 * (w & 1), cpl = (lane >> 3) + 8 * (w >> 1);
    unsigned halo[3] = {0u, 0u, 0u};
    v4u shk4 = {0u, 0u, 0u, 0u};
    if (kvrole) {
      shk4 = *(const GAS v4u*)((cptr)p.shk + (size_t)(64 * kc + 2 * cpl) * 8);
      if (tg8 == 0 && kt > 0) {
#pragma unroll
        for (int i = 0; i < 3; ++i) halo[i] = *(const GAS unsigned*)((cptr)p.k + (size_t)(128 * kt - 3 + i) * 1280 + 128 * kc + 4 * cpl);
      }
    }
    unsigned h01 = *(const GAS unsigned*)((cptr)p.sk + (size_t)(special ? chA + (w < 6 ? w : 0) : chC + (w & 1)) * 4096);
    USE(h01); USE(xa); USE(xb2); USE(shk4); USE(halo[0]); USE(halo[1]); USE(halo[2]);
    EPOCH_FIN(0);
    BAR();
    if (kvrole) {
      const unsigned ka = L_E + tg8 * 1024 + ((((cpl >> 2) ^ tg8) & 7) << 4) + (cpl & 3) * 4;
      const unsigned kp = tg8 ? L_E + (tg8 - 1) * 1024 + ((((cpl >> 2) ^ (tg8 - 1)) & 7) << 4) + (cpl & 3) * 4 : ka;
      unsigned kk[11], vv[8];
#pragma unroll
      for (int i = 0; i < 3; ++i) { unsigned t = lds32(kp + (5 + i) * 128); kk[i] = tg8 ? t : halo[i]; }
#pragma unroll
      for (int i = 0; i < 8; ++i) { kk[3 + i] = lds32(ka + i * 128); vv[i] = lds32(ka + 16384 + i * 128); }
      const float a0 = bf_lo(shk4.x), a1 = bf_hi(shk4.x), a2 = bf_lo(shk4.y), a3 = bf_hi(shk4.y);
      const float b0 = bf_lo(shk4.z), b1 = bf_hi(shk4.z), b2 = bf_lo(shk4.w), b3 = bf_hi(shk4.w);
      float oa[8], ob[8];
#pragma unroll
      for (int i = 0; i < 8; ++i) {
        float sa = bf_lo(kk[i]) * a3 + bf_lo(kk[i + 1]) * a2 + bf_lo(kk[i + 2]) * a1 + bf_lo(kk[i + 3]) * a0;
        float sb = bf_hi(kk[i]) * b3 + bf_hi(kk[i + 1]) * b2 + bf_hi(kk[i + 2]) * b1 + bf_hi(kk[i + 3]) * b0;
        unsigned r = pk_bf(sa, sb);
        oa[i] = bf_lo(r) * bf_lo(vv[i]); ob[i] = bf_hi(r) * bf_hi(vv[i]);
      }
      v4u ra = {pk_bf(oa[7], oa[6]), pk_bf(oa[5], oa[4]), pk_bf(oa[3], oa[2]), pk_bf(oa[1], oa[0])};
      v4u rb = {pk_bf(ob[7], ob[6]), pk_bf(ob[5], ob[4]), pk_bf(ob[3], ob[2]), pk_bf(ob[1], ob[0])};
      const unsigned o = WS_KVR + (64 * kc + 2 * cpl) * KVR_ROW + (2040 - 128 * kt - 8 * tg8) * 2;
      __builtin_amdgcn_raw_buffer_store_b128(ra, rs, o, 0, AUX_SC1);
      __builtin_amdgcn_raw_buffer_store_b128(rb, rs, o + KVR_ROW, 0, AUX_SC1);
      if (kt == 0 && tg8 == 0) {
        __builtin_amdgcn_raw_buffer_store_b128(v4u{0u, 0u, 0u, 0u}, rs, o + 16, 0, AUX_SC1);
        __builtin_amdgcn_raw_buffer_store_b128(v4u{0u, 0u, 0u, 0u}, rs, o + KVR_ROW + 16, 0, AUX_SC1);
      }
    } else if (w2role) {
      const int g = lane >> 4, q = (lane & 15) >> 2, pp = lane & 3;
      const int dt = 8 * (wn % 5) + w;
#pragma unroll
      for (int u = 0; u < 3; ++u) {
        v2u h2[2];
#pragma unroll
        for (int t = 0; t < 2; ++t) {
          const int row = 32 * u + 16 * t + 4 * g + q;
          const int swz = (q << 2) | (g & 3);
          h2[t] = ldstr(L_E + row * 256 + (((2 * w + (pp >> 1)) ^ swz) << 4) + 8 * (pp & 1));
        }
        const int ks = 3 * (wn / 5) + u;
        __builtin_amdgcn_raw_buffer_store_b128(v4u{h2[0].x, h2[0].y, h2[1].x, h2[1].y}, rs, WS_W2T + (((dt / 5) * 54 + ks) * 5 + dt % 5) * 1024 + l16, 0, AUX_SC1);
      }
    }
    {
      float s = bf_lo(xa.x) + bf_hi(xa.x) + bf_lo(xa.y) + bf_hi(xa.y) + bf_lo(xa.z) + bf_hi(xa.z) + bf_lo(xa.w) + bf_hi(xa.w);
      float s2 = bf_lo(xb2.x) + bf_hi(xb2.x) + bf_lo(xb2.y) + bf_hi(xb2.y) + bf_lo(xb2.z) + bf_hi(xb2.z) + bf_lo(xb2.w) + bf_hi(xb2.w);
      if (lane < 16) s += s2;
      const float mean = wave_sum(s) * (1.0f / D);
      float q = 0.f, q2 = 0.f, e;
#define SQ(acc, u) e = bf_lo(u) - mean; acc += e * e; e = bf_hi(u) - mean; acc += e * e;
      SQ(q, xa.x) SQ(q, xa.y) SQ(q, xa.z) SQ(q, xa.w) SQ(q2, xb2.x) SQ(q2, xb2.y) SQ(q2, xb2.z) SQ(q2, xb2.w)
#undef SQ
      if (lane < 16) q += q2;
      const float var = wave_sum(q) * (1.0f / D);
      const float sd = rbf(__builtin_amdgcn_sqrtf(rbf(rbf(var) + 1e-5f)));
      if (lane == 0) __builtin_amdgcn_raw_buffer_store_b64(__builtin_bit_cast(v2u, f32x2{rbf(mean), __builtin_amdgcn_rcpf(sd)}), rs, WS_STATS + srow * 8, 0, AUX_SC1);
    }
    bool hflag = false;
    unsigned hfo = 0;
    if (!special) {
      const int tg = w & 3, fg = w >> 2;
      const int n = lane & 15, g = lane >> 4, q = (lane & 15) >> 2, pp = lane & 3;
      unsigned ax[2], aw[4][2];
#pragma unroll
      for (int u = 0; u < 2; ++u) ax[u] = 65536 + (32 * tg + n) * 128 + ((((4 * u + g) ^ n) & 7) << 4);
#pragma unroll
      for (int mt = 0; mt < 4; ++mt)
#pragma unroll
        for (int t = 0; t < 2; ++t)
          aw[mt][t] = 65536 + 16384 + (8 * g + 4 * t + q) * 256 + (((2 * (4 * fg + mt) + (pp >> 1)) ^ ((q << 2) | ((2 * g + t) & 3))) << 4) + 8 * (pp & 1);
      f32x4 acc[4][2], cs[4];
#pragma unroll
      for (int mt = 0; mt < 4; ++mt) { acc[mt][0] = acc[mt][1] = cs[mt] = f32x4{0.f, 0.f, 0.f, 0.f}; }
      v4u ones = {0x3f803f80u, 0x3f803f80u, 0x3f803f80u, 0x3f803f80u};
      USE(ones);
      v4u xf0[2], wf0[4], xf1[2], wf1[4];
#pragma unroll
      for (int i = 0; i < 2; ++i) xf1[i] = v4u{0u, 0u, 0u, 0u};
#pragma unroll
      for (int i = 0; i < 4; ++i) wf1[i] = v4u{0u, 0u, 0u, 0u};
#define P1_READ(XF, WF, U) do { \
        XF[0] = lds128(ax[U]); XF[1] = lds128(ax[U] + 2048); \
        _Pragma("unroll") for (int mt = 0; mt < 4; ++mt) { v2u lo = ldstr(aw[mt][0] + 8192 * U), hi = ldstr(aw[mt][1] + 8192 * U); WF[mt] = v4u{lo.x, lo.y, hi.x, hi.y}; } \
      } while (0)
#define P1_MMA(XF, WF) do { \
        _Pragma("unroll") for (int mt = 0; mt < 4; ++mt) { acc[mt][0] = mfma(WF[mt], XF[0], acc[mt][0]); acc[mt][1] = mfma(WF[mt], XF[1], acc[mt][1]); } \
      } while (0)
#define P1_CS(WF, KS) do { \
        if (((KS) & 3) == tg) { _Pragma("unroll") for (int mt = 0; mt < 4; ++mt) cs[mt] = mfma(WF[mt], ones, cs[mt]); } \
      } while (0)
#define P1_ILV() do { SGB(0x100, 2); SGB(0x8, 1); SGB(0x100, 2); SGB(0x8, 1); SGB(0x100, 1); SGB(0x8, 1); SGB(0x100, 1); SGB(0x8, 1); \
        SGB(0x100, 1); SGB(0x8, 1); SGB(0x100, 1); SGB(0x8, 1); SGB(0x100, 1); SGB(0x8, 1); SGB(0x100, 1); SGB(0x8, 1); } while (0)
#pragma unroll 1
      for (int s = 0; s < 10; ++s) {
        if (s == 2) WAITV(0);
        BAR();
        if (s == 2) {
          if (w == 0 && lane == 0) __builtin_amdgcn_raw_buffer_store_b32(epoch, rs, WS_FP0 + b * 4, 0, AUX_SC1);
          if (w < 2) conv_pads(L_CONV + w * CH_BYTES, lane, h01);
        }
        __builtin_amdgcn_sched_barrier(0);
        P1_READ(xf0, wf0, 0);
        P1_MMA(xf1, wf1);
        P1_ILV();
        __builtin_amdgcn_sched_barrier(0);
        P1_CS(wf1, 2 * s - 1);
        __builtin_amdgcn_sched_barrier(0);
        P1_READ(xf1, wf1, 1);
        P1_MMA(xf0, wf0);
        P1_ILV();
        __builtin_amdgcn_sched_barrier(0);
        P1_CS(wf0, 2 * s);
        const int dlt = ((s + 2) & 3) == 3 ? -98304 : 32768;
#pragma unroll
        for (int u = 0; u < 2; ++u) ax[u] += dlt;
#pragma unroll
        for (int mt = 0; mt < 4; ++mt) { aw[mt][0] += dlt; aw[mt][1] += dlt; }
      }
      P1_MMA(xf1, wf1);
      P1_CS(wf1, 19);
      if (n == 0) {
#pragma unroll
        for (int mt = 0; mt < 4; ++mt) sts128(L_CS + ((fg * 4 + tg) * 64 + mt * 16 + g * 4) * 4, __builtin_bit_cast(v4u, cs[mt]));
      }
      BAR();
      {
#pragma unroll
        for (int mt = 0; mt < 4; ++mt) {
          f32x4 c = {0.f, 0.f, 0.f, 0.f};
#pragma unroll
          for (int t4 = 0; t4 < 4; ++t4) c += __builtin_bit_cast(f32x4, lds128(L_CS + ((fg * 4 + t4) * 64 + mt * 16 + g * 4) * 4));
          cs[mt] = c;
        }
        if (!(ct == 13 && fg == 1)) {
#pragma unroll
          for (int T = 0; T < 2; ++T) {
            const f32x2 st = __builtin_bit_cast(f32x2, lds64(L_STATS + (32 * tg + 16 * T + n) * 8));
#pragma unroll
            for (int pr = 0; pr < 2; ++pr) {
              float hv[8];
#pragma unroll
              for (int j = 0; j < 8; ++j) {
                const int mt = 2 * pr + (j >> 2);
                hv[j] = silu((acc[mt][T][j & 3] - st[0] * cs[mt][j & 3]) * st[1]);
              }
              v4u o = {pk_bf(hv[0], hv[1]), pk_bf(hv[2], hv[3]), pk_bf(hv[4], hv[5]), pk_bf(hv[6], hv[7])};
              __builtin_amdgcn_raw_buffer_store_b128(o, rs, WS_HF + ((8 * rt + 2 * tg + T) * 54 + 4 * ct + 2 * fg + pr) * 1024 + l16, 0, AUX_SC1);
            }
          }
          hflag = true;
          hfo = WS_FH + ((4 * rt + tg) * 32 + 2 * ct + fg) * 4;
        }
      }
    } else {
      WAITV(0); BAR();
      if (w == 0 && lane == 0) __builtin_amdgcn_raw_buffer_store_b32(epoch, rs, WS_FP0 + b * 4, 0, AUX_SC1);
      if (w < 6) conv_pads(w < 4 ? w * CH_BYTES : L_CONV + (w - 4) * CH_BYTES, lane, h01);
    }
    const int tt = w & 3, kh = w >> 2;
    v4u hf[27];
    const unsigned hoff = WS_HF + ((4 * tt64 + tt) * 54 + 27 * kh) * 1024 + l16;
    const unsigned fho = ((2 * tt64 + (tt >> 1)) * 32 + 13 * kh + (lane < 14 ? lane : 0)) * 4;
#define H_FLAG() do { if (hflag) { WAITV(0); if (lane == 0) __builtin_amdgcn_raw_buffer_store_b32(epoch, rs, hfo, 0, AUX_SC1); hflag = false; } } while (0)
#pragma unroll 1
    for (int round = special ? 0 : 1; round < (w < 4 ? 2 : 1); ++round) {
      BAR();
      {
        const unsigned cb = (round ? L_CONV : 0u) + (w >> 1) * CH_BYTES;
        const int rh = w & 1;
        const int i16 = lane & 15, kg = lane >> 4;
        const unsigned rbase = cb + ((i16 & 1) ? CH_R1 : CH_R0);
        unsigned va = rbase + 4032 - 3584 - 64 * (i16 >> 1) + 16 * kg + 256 * rh;
        const unsigned vamax = rbase + 4096;
        const unsigned hbase = cb + ((i16 & 1) ? CH_H0 : CH_H1);
        unsigned vb = hbase + 4 * i16 - ((i16 & 1) ? 60 : 64) + 16 * kg + 256 * rh;
        const unsigned vbmin = hbase - 16;
        f32x4 acc[8];
#pragma unroll
        for (int i = 0; i < 8; ++i) acc[i] = f32x4{0.f, 0.f, 0.f, 0.f};
#define LDA_(i) lds128(va + 3584 - 512 * (i))
#define LDB_(dst, i) do { v2u lo = lds64v(vb + 512 * (i)), hi = lds64v(vb + 512 * (i) + 8); dst = v4u{lo.x, lo.y, hi.x, hi.y}; } while (0)
        v4u A[8], B[8];
        {
          A[0] = lds128(min(va + 3584, vamax));
          v2u lo = lds64v(max(vb, vbmin)), hi = lds64v(max(vb + 8, vbmin));
          B[0] = v4u{lo.x, lo.y, hi.x, hi.y};
          A[1] = LDA_(1); LDB_(B[1], 1);
        }
#pragma unroll 1
        for (int r = 0; r < 4; ++r) {
          if (r == 2) H_FLAG();
          v4u nA0, nB0, nA1, nB1;
#pragma unroll
          for (int m = 0; m < 8; ++m) {
            if (m < 6) { A[m + 2] = LDA_(m + 2); LDB_(B[m + 2], m + 2); }
            else if (m == 6) { va += 64; vb += 64; nA0 = lds128(min(va + 3584, vamax)); LDB_(nB0, 0); }
            else { nA1 = LDA_(1); LDB_(nB1, 1); }
#pragma unroll
            for (int o = 0; o < m; ++o)
              if (m + o <= 7) { acc[m + o] = mfma(A[m], B[o], acc[m + o]); acc[m + o] = mfma(A[o], B[m], acc[m + o]); }
            if (2 * m <= 7) acc[2 * m] = mfma(A[m], B[m], acc[2 * m]);
            __builtin_amdgcn_sched_barrier(0);
          }
          A[0] = nA0; B[0] = nB0; A[1] = nA1; B[1] = nB1;
        }
        BAR();
        {
          const unsigned a = (round ? L_E : 0u) + w * 8192 + (64 * (lane >> 4) + 2 * (lane & 15)) * 4;
#pragma unroll
          for (int i = 0; i < 8; ++i) {
            sts64(a + 1024 * i, __builtin_bit_cast(v2u, f32x2{acc[i][0], acc[i][1]}));
            sts64(a + 1024 * i + 128, __builtin_bit_cast(v2u, f32x2{acc[i][2], acc[i][3]}));
          }
        }
        if (round == 0) { BAR(); }
      }
    }
    if (w >= 4) { BAR(); H_FLAG(); }
    poll1(p.ws + WS_FH, fho, epoch);
#pragma unroll
    for (int i = 0; i < 27; ++i) hf[i] = __builtin_amdgcn_raw_buffer_load_b128(rs, hoff + i * 1024, 0, AUX_SC1);
    if (w >= 4) { BAR(); }
    fx = *(const GAS v4u*)((cptr)p.x + fgo);
    fst = __builtin_amdgcn_raw_buffer_load_b64(rs, WS_STATS + (64 * tt64 + ftok) * 8, 0, AUX_SC1);
    f32x4 acc[5];
#pragma unroll
    for (int i = 0; i < 5; ++i) acc[i] = f32x4{0.f, 0.f, 0.f, 0.f};
    const unsigned wa = kh * 15360 + l16;
    v4u a0[5], a1[5];
#define P3_RD(A, ks) do { _Pragma("unroll") for (int i = 0; i < 5; ++i) A[i] = lds128(wa + (((ks) / 3) % 5) * WSLOT + (((ks) % 3) * 5 + i) * 1024); } while (0)
#define P3_MM(A, ks) do { _Pragma("unroll") for (int i = 0; i < 5; ++i) acc[i] = mfma(A[i], hf[ks], acc[i]); } while (0)
#define P3_ILV() do { _Pragma("unroll") for (int i = 0; i < 5; ++i) { SGB(0x100, 1); SGB(0x8, 1); } __builtin_amdgcn_sched_barrier(0); } while (0)
    BAR();
    P3_RD(a0, 0);
    __builtin_amdgcn_sched_barrier(0);
#pragma unroll
    for (int ks = 0; ks < 26; ks += 2) {
      if ((ks + 1) % 3 == 0) { BAR(); __builtin_amdgcn_sched_barrier(0); }
      P3_RD(a1, ks + 1); P3_MM(a0, ks); P3_ILV();
      if ((ks + 2) % 3 == 0) { BAR(); __builtin_amdgcn_sched_barrier(0); }
      P3_RD(a0, ks + 2); P3_MM(a1, ks + 1); P3_ILV();
    }
    P3_MM(a0, 26);
    {
      const unsigned oa = L_O + kh * (64 * OROW) + (16 * tt + (lane & 15)) * OROW + (lane >> 4) * 16;
#pragma unroll
      for (int i = 0; i < 5; ++i) sts128(oa + i * 64, __builtin_bit_cast(v4u, acc[i]));
    }
  }
  BAR();
  if (tid < 640) {
    const f32x2 st = __builtin_bit_cast(f32x2, fst);
    const unsigned oa = L_O + ftok * OROW + fc8 * 32;
    f32x4 o0 = __builtin_bit_cast(f32x4, lds128(oa)) + __builtin_bit_cast(f32x4, lds128(oa + 64 * OROW));
    f32x4 o1 = __builtin_bit_cast(f32x4, lds128(oa + 16)) + __builtin_bit_cast(f32x4, lds128(oa + 64 * OROW + 16));
    const v4u yv = lds128(L_Y + ftok * 160 + fc8 * 16);
    float o[8] = {o0[0], o0[1], o0[2], o0[3], o1[0], o1[1], o1[2], o1[3]};
    unsigned xw[4] = {fx.x, fx.y, fx.z, fx.w}, yw[4] = {yv.x, yv.y, yv.z, yv.w}, rw[4];
#pragma unroll
    for (int j = 0; j < 4; ++j) {
      const unsigned ob = pk_bf(o[2 * j], o[2 * j + 1]);
      const unsigned gb = pk_bf(silu(bf_lo(ob)), silu(bf_hi(ob)));
      const unsigned xm = pk_bf(bf_lo(xw[j]) - st[0], bf_hi(xw[j]) - st[0]);
      const unsigned xn = pk_bf(bf_lo(xm) * st[1], bf_hi(xm) * st[1]);
      const unsigned sb = pk_bf(bf_lo(xn) * bf_lo(yw[j]), bf_hi(xn) * bf_hi(yw[j]));
      const unsigned sg = pk_bf(bf_lo(sb) * bf_lo(gb), bf_hi(sb) * bf_hi(gb));
      rw[j] = pk_bf(bf_lo(xw[j]) + bf_lo(sg), bf_hi(xw[j]) + bf_hi(sg));
    }
    *(GAS v4u*)((unsigned char*)p.out + fgo) = v4u{rw[0], rw[1], rw[2], rw[3]};
  }
}

extern "C" void kernel_launch(void* const* d_in, const int* in_sizes, int n_in, void* d_out, int out_size, void* d_ws, size_t ws_size,
                              hipStream_t stream) {
  Params p;
  p.x = (const u16*)d_in[0]; p.k = (const u16*)d_in[1]; p.v = (const u16*)d_in[2];
  p.shk = (const u16*)d_in[3]; p.sk = (const u16*)d_in[4]; p.sD = (const u16*)d_in[5];
  p.w1 = (const u16*)d_in[7]; p.w2 = (const u16*)d_in[8];
  p.out = (u16*)d_out; p.ws = (unsigned char*)d_ws;
  fused_kernel<<<NWG, NTHR, 0, stream>>>(p);
}
```

```cpp
#include <hip/hip_runtime.h>
#include <stdint.h>

typedef unsigned short u16;
typedef unsigned v4u __attribute__((ext_vector_type(4)));
typedef unsigned v2u __attribute__((ext_vector_type(2)));
typedef float f32x4 __attribute__((ext_vector_type(4)));
typedef float f32x2 __attribute__((ext_vector_type(2)));
typedef short bf16x8 __attribute__((ext_vector_type(8)));
typedef short v4s __attribute__((ext_vector_type(4)));
typedef __bf16 v2bf __attribute__((ext_vector_type(2)));
#define LAS __attribute__((address_space(3)))
#define GAS __attribute__((address_space(1)))
#define DEV __device__ __forceinline__
typedef const unsigned char* cptr;
typedef const __attribute__((address_space(4))) unsigned* kptr;

constexpr int S = 2048, D = 640, F = 1728;
constexpr int NWG = 256, NTHR = 768;
constexpr unsigned WS_EPOCH = 0;
constexpr unsigned WS_FP0 = 1024;
constexpr unsigned WS_FH = 4096;
constexpr unsigned WS_FY = 16384;
constexpr unsigned WS_STATS = 32768;
constexpr unsigned KVR_ROW = 4224;
constexpr unsigned WS_KVR = 65536;
constexpr unsigned WS_W2T = WS_KVR + 640 * KVR_ROW;
constexpr unsigned WS_HF = WS_W2T + 8 * 54 * 5 * 1024;
constexpr unsigned WS_YT = WS_HF + 128 * 54 * 1024;
constexpr unsigned WS_CSP = WS_YT + 640 * 4096;
constexpr unsigned WS_END = WS_CSP + 10 * 1792 * 8;
constexpr unsigned WS_DBG = WS_YT + 640 * 4096 + 262144;
constexpr unsigned L_E = 131072;
constexpr unsigned L_STATS = 65536 + 30720;
constexpr unsigned L_CS = 30720;
constexpr unsigned CH_R1 = 0, CH_Z1 = 4096, CH_R0 = 4128, CH_Z0 = 8224, CH_H0 = 8240, CH_PF = 12416, CH_H1 = 12432, CH_BYTES = 16528;
constexpr unsigned L_AREA0 = 163840 - 4 * CH_BYTES;
constexpr unsigned L_Y = L_E;
constexpr unsigned L_X = L_E + 12288;
constexpr unsigned L_O = 0;
constexpr unsigned OROW = 336;
constexpr unsigned SPIN_CAP = 1u << 15;
constexpr int AUX_SC1 = 16;
constexpr unsigned wslot(int g) { return (g % 5) * 32768u; }

DEV float bf_lo(unsigned u) { return __builtin_bit_cast(float, u << 16); }
DEV float bf_hi(unsigned u) { return __builtin_bit_cast(float, u & 0xffff0000u); }
DEV f32x2 bf2(unsigned u) { return f32x2{bf_lo(u), bf_hi(u)}; }
DEV unsigned pk_bf(float a, float b) { v2bf r = {(__bf16)a, (__bf16)b}; return __builtin_bit_cast(unsigned, r); }
DEV unsigned pk_bf(f32x2 a) { return pk_bf(a[0], a[1]); }
DEV float rbf(float a) { return bf_lo(pk_bf(a, a) & 0xffffu); }
DEV f32x4 mfma(v4u a, v4u b, f32x4 c) {
  return __builtin_amdgcn_mfma_f32_16x16x32_bf16(__builtin_bit_cast(bf16x8, a), __builtin_bit_cast(bf16x8, b), c, 0, 0, 0);
}
constexpr float LOG2E = 1.44269504088896f;
DEV f32x2 silu_z(f32x2 z) {
  f32x2 e = {__builtin_amdgcn_exp2f(z[0]), __builtin_amdgcn_exp2f(z[1])};
  f32x2 d = e * -LOG2E - LOG2E;
  f32x2 r = {__builtin_amdgcn_rcpf(d[0]), __builtin_amdgcn_rcpf(d[1])};
  return z * r;
}
DEV float dot2ones(unsigned u, float acc) {
  return __builtin_amdgcn_fdot2_f32_bf16(__builtin_bit_cast(v2bf, u), __builtin_bit_cast(v2bf, 0x3f803f80u), acc, false);
}

DEV void dma16(const void* sbase, unsigned voff, unsigned ldsoff) {
  asm volatile("s_mov_b32 m0, %2\n\ts_nop 0\n\tglobal_load_lds_dwordx4 %0, %1" ::"v"(voff), "s"(sbase), "s"(ldsoff) : "memory");
}
DEV void dma16s(const void* sbase, unsigned voff, unsigned ldsoff) {
  asm volatile("s_mov_b32 m0, %2\n\ts_nop 0\n\tglobal_load_lds_dwordx4 %0, %1 sc1" ::"v"(voff), "s"(sbase), "s"(ldsoff) : "memory");
}
DEV void dma4k(const void* sbase, unsigned voff, unsigned ldsoff) {
  asm volatile("s_mov_b32 m0, %2\n\ts_nop 0\n\tglobal_load_lds_dwordx4 %0, %1\n\tglobal_load_lds_dwordx4 %0, %1 offset:1024\n\t"
               "global_load_lds_dwordx4 %0, %1 offset:2048\n\tglobal_load_lds_dwordx4 %0, %1 offset:3072" ::"v"(voff), "s"(sbase), "s"(ldsoff) : "memory");
}
DEV void dma4ks(const void* sbase, unsigned voff, unsigned ldsoff) {
  asm volatile("s_mov_b32 m0, %2\n\ts_nop 0\n\tglobal_load_lds_dwordx4 %0, %1 sc1\n\tglobal_load_lds_dwordx4 %0, %1 offset:1024 sc1\n\t"
               "global_load_lds_dwordx4 %0, %1 offset:2048 sc1\n\tglobal_load_lds_dwordx4 %0, %1 offset:3072 sc1" ::"v"(voff), "s"(sbase), "s"(ldsoff) : "memory");
}
DEV void dma4v(const void* sbase, const unsigned (&vo)[4], unsigned ldsoff) {
  asm volatile("s_mov_b32 m0, %5\n\ts_nop 0\n\tglobal_load_lds_dwordx4 %0, %4\n\tglobal_load_lds_dwordx4 %1, %4 offset:1024\n\t"
               "global_load_lds_dwordx4 %2, %4 offset:2048\n\tglobal_load_lds_dwordx4 %3, %4 offset:3072"
               ::"v"(vo[0]), "v"(vo[1]), "v"(vo[2]), "v"(vo[3]), "s"(sbase), "s"(ldsoff) : "memory");
}
DEV v4u ld128a(const void* sbase, unsigned voff) { v4u r; asm volatile("global_load_dwordx4 %0, %1, %2 sc1" : "=v"(r) : "v"(voff), "s"(sbase) : "memory"); return r; }
DEV v2u ld64a(const void* sbase, unsigned voff) { v2u r; asm volatile("global_load_dwordx2 %0, %1, %2 sc1" : "=v"(r) : "v"(voff), "s"(sbase) : "memory"); return r; }
DEV unsigned atom_rd(const void* sbase, unsigned voff, unsigned long long mask, unsigned dflt) {
  unsigned r; unsigned long long sv;
  asm volatile("v_mov_b32 %0, %6\n\ts_mov_b64 %1, exec\n\ts_mov_b64 exec, %5\n\tglobal_atomic_or %0, %2, %3, %4 sc0 sc1\n\ts_mov_b64 exec, %1"
               : "=&v"(r), "=&s"(sv) : "v"(voff), "v"(0u), "s"(sbase), "s"(mask), "s"(dflt) : "memory");
  return r;
}
DEV unsigned ld32a(const void* sbase, unsigned voff) { unsigned r; asm volatile("global_load_dword %0, %1, %2 sc1" : "=v"(r) : "v"(voff), "s"(sbase) : "memory"); return r; }
#define WAITV(n) asm volatile("s_waitcnt vmcnt(" #n ")" ::: "memory")
#define USE(x) asm volatile("" : "+v"(x))
#define BAR0() do { asm volatile("s_waitcnt lgkmcnt(0)" ::: "memory"); __builtin_amdgcn_s_barrier(); asm volatile("" ::: "memory"); } while (0)
#define SGB(m, n) __builtin_amdgcn_sched_group_barrier(m, n, 0)
#define SB0() __builtin_amdgcn_sched_barrier(0)

DEV v4u lds128(unsigned a) { return *(const LAS v4u*)(size_t)a; }
DEV v2u lds64(unsigned a) { return *(const LAS v2u*)(size_t)a; }
DEV v2u lds64v(unsigned a) { return *(const volatile LAS v2u*)(size_t)a; }
DEV unsigned lds32(unsigned a) { return *(const LAS unsigned*)(size_t)a; }
DEV void sts128(unsigned a, v4u v) { *(LAS v4u*)(size_t)a = v; }
DEV void sts64(unsigned a, v2u v) { *(LAS v2u*)(size_t)a = v; }
DEV void sts32(unsigned a, unsigned v) { *(LAS unsigned*)(size_t)a = v; }
DEV void sts16(unsigned a, unsigned v) { *(LAS u16*)(size_t)a = (u16)v; }
DEV v2u ldstr(unsigned a) { return __builtin_bit_cast(v2u, __builtin_amdgcn_ds_read_tr16_b64_v4i16((LAS v4s*)(size_t)a)); }

template <int CTRL, int RM> DEV float dpp_add(float v) {
  return v + __builtin_bit_cast(float, __builtin_amdgcn_update_dpp(0, __builtin_bit_cast(int, v), CTRL, RM, 0xf, false));
}
DEV float wave_sum(float v) {
  v = dpp_add<0x111, 0xf>(v); v = dpp_add<0x112, 0xf>(v); v = dpp_add<0x114, 0xf>(v); v = dpp_add<0x118, 0xf>(v);
  v = dpp_add<0x142, 0xa>(v); v = dpp_add<0x143, 0xc>(v);
  return __builtin_bit_cast(float, __builtin_amdgcn_readlane(__builtin_bit_cast(int, v), 63));
}

#define BAR() BAR0()
#define STAMP(i)

struct Params {
  const u16 *x, *k, *v, *shk, *sk, *sD, *w1, *w2;
  u16* out;
  unsigned char* ws;
};

template <int N> DEV bool wait_eq1(unsigned v, unsigned epoch) {
  unsigned d;
  asm volatile("s_waitcnt vmcnt(%3)\n\tv_xor_b32 %0, %2, %1" : "=v"(d) : "v"(v), "s"(epoch), "i"(N) : "memory");
  return __all(d == 0u);
}
template <int N> DEV bool wait_eq4(v4u v, unsigned epoch) {
  unsigned d, e;
  asm volatile("s_waitcnt vmcnt(%7)\n\tv_xor_b32 %0, %6, %2\n\tv_xor_b32 %1, %6, %3\n\tv_or_b32 %0, %0, %1\n\tv_xor_b32 %1, %6, %4\n\t"
               "v_or_b32 %0, %0, %1\n\tv_xor_b32 %1, %6, %5\n\tv_or_b32 %0, %0, %1"
               : "=&v"(d), "=&v"(e) : "v"(v.x), "v"(v.y), "v"(v.z), "v"(v.w), "s"(epoch), "i"(N) : "memory");
  return __all(d == 0u);
}
DEV void poll1(cptr base, unsigned off, unsigned epoch) {
#pragma unroll 1
  for (unsigned spins = 0; spins < SPIN_CAP; ++spins) {
    unsigned v = ld32a(base, off);
    if (wait_eq1<0>(v, epoch)) break;
    __builtin_amdgcn_s_sleep(6);
  }
}
DEV void poll4(cptr base, unsigned off, unsigned epoch) {
#pragma unroll 1
  for (unsigned spins = 0; spins < SPIN_CAP; ++spins) {
    v4u v = ld128a(base, off);
    if (wait_eq4<0>(v, epoch)) break;
    __builtin_amdgcn_s_sleep(6);
  }
}
DEV void flag_set(const void* sbase, unsigned voff, unsigned epoch) {
  asm volatile("global_atomic_swap %0, %1, %2 sc1" ::"v"(voff), "v"(epoch), "s"(sbase) : "memory");
}
DEV void poll_atomic(cptr base, unsigned off, unsigned long long mask, unsigned epoch) {
#pragma unroll 1
  for (unsigned spins = 0; spins < SPIN_CAP; ++spins) {
    const unsigned v = atom_rd(base, off, mask, epoch);
    if (wait_eq1<0>(v, epoch)) break;
    __builtin_amdgcn_s_sleep(2);
  }
}
template <int N> DEV unsigned epoch_fin(unsigned ev) {
  unsigned e;
  asm volatile("s_waitcnt vmcnt(%2)\n\tv_readfirstlane_b32 %0, %1\n\ts_nop 4" : "=s"(e) : "v"(ev), "i"(N) : "memory");
  return e * 2654435761u + 0x9E3779B9u;
}

DEV void conv_final(__amdgpu_buffer_rsrc_t rs, unsigned db, int ch, int chunk, v4u kvr, float dD) {
  const unsigned a = db + chunk * 32;
  f32x4 p0 = __builtin_bit_cast(f32x4, lds128(a)), p1 = __builtin_bit_cast(f32x4, lds128(a + 16));
  f32x4 q0 = __builtin_bit_cast(f32x4, lds128(a + 8192)), q1 = __builtin_bit_cast(f32x4, lds128(a + 8192 + 16));
  p0 += q0; p1 += q1;
  unsigned y01 = pk_bf(p0[0], p0[1]), y23 = pk_bf(p0[2], p0[3]), y45 = pk_bf(p1[0], p1[1]), y67 = pk_bf(p1[2], p1[3]);
  unsigned d01 = pk_bf(bf_hi(kvr.w) * dD, bf_lo(kvr.w) * dD), d23 = pk_bf(bf_hi(kvr.z) * dD, bf_lo(kvr.z) * dD);
  unsigned d45 = pk_bf(bf_hi(kvr.y) * dD, bf_lo(kvr.y) * dD), d67 = pk_bf(bf_hi(kvr.x) * dD, bf_lo(kvr.x) * dD);
  v4u o = {pk_bf(bf2(y01) + bf2(d01)), pk_bf(bf2(y23) + bf2(d23)), pk_bf(bf2(y45) + bf2(d45)), pk_bf(bf2(y67) + bf2(d67))};
  __builtin_amdgcn_raw_buffer_store_b128(o, rs, WS_YT + ch * 4096 + chunk * 16, 0, AUX_SC1);
}

__global__ void __launch_bounds__(NTHR, 3) fused_kernel(Params p) {
  __shared__ __attribute__((aligned(16))) unsigned char smem[163840];
  const int tid = threadIdx.x, lane = tid & 63;
  const int w = __builtin_amdgcn_readfirstlane(tid >> 6);
  const int b = blockIdx.x, xcd = b & 7, idx = b >> 3;
  const int half = xcd & 1, cti = idx & 7;
  const int rt = 4 * (xcd >> 1) + (idx >> 3);
  const int nfull = 6 - half;
  const bool isfull = cti < nfull;
  const int u0 = 14 * half + (isfull ? 2 * cti : nfull + cti);
  const unsigned l16 = lane * 16;
  { unsigned lb = (unsigned)(size_t)(LAS unsigned char*)smem; asm volatile("" ::"s"(lb)); }
  unsigned stv = 0; int stn = 32; (void)stv; (void)stn;
  STAMP(0);
  unsigned epoch, epraw;
  asm volatile("s_load_dword %0, %1, 0x0" : "=s"(epraw) : "s"(p.ws + WS_EPOCH) : "memory");
#define EPOCH_FIN() asm volatile("s_waitcnt lgkmcnt(0)\n\ts_mul_i32 %0, %1, 0x9E3779B1\n\ts_add_u32 %0, %0, 0x9E3779B9" : "=s"(epoch) : "s"(epraw) : "memory", "scc")
  const bool kvrole = idx < 20;
  const int kvn = xcd * 20 + idx, kt = kvn / 10, kc = kvn % 10;
  const int wn = xcd * 12 + idx - 20;
#define LATE_CONSTS() \
  int rt_ = rt; asm volatile("" : "+s"(rt_)); \
  const __amdgpu_buffer_rsrc_t rs = __builtin_amdgcn_make_buffer_rsrc((void*)p.ws, 0, (int)WS_END, 0x00020000); \
  const int tt64 = 2 * rt_ + (cti >> 2), c80 = 4 * half + (cti & 3); \
  const int hn = rt_ * 5 + half * 2 + cti - nfull; \
  const int area = w >= 8 ? 2 + ((w >> 1) & 1) : (w >> 1) & 1; \
  const bool convw = isfull ? w >= 8 : (hn < 72 && (w < 4 || w >= 8)); \
  const int chb = isfull ? 2 * (rt_ * 11 + half * 6 + cti) - 2 : (hn < 72 ? 352 + 4 * hn : 0); \
  const int cch = (isfull && area < 2) ? chb + 2 : chb + area; \
  const unsigned cb = L_AREA0 + area * CH_BYTES; \
  cptr w2s = p.ws + WS_W2T + c80 * (54 * 5 * 1024) + ((w >> 1) & 1) * (27 * 5 * 1024) + (w & 1) * 7168; \
  const unsigned w2d = ((w >> 1) & 1) * 15360 + (w & 1) * 7168; \
  (void)rs; (void)tt64; (void)convw; (void)cch; (void)cb; (void)w2s; (void)w2d; (void)chb;
#define H_DMA() dma4k((cptr)p.sk + (size_t)cch * 4096 + (w & 1) * 4, l16, cb + ((w & 1) ? CH_H1 : CH_H0))
#define R_DMA() dma4ks(p.ws + WS_KVR + (size_t)cch * KVR_ROW + (w & 1) * 2, l16, cb + ((w & 1) ? CH_R0 : CH_R1))
#define CONV_PADS() do { const unsigned h01 = *(kptr)((cptr)p.sk + (size_t)cch * 4096); \
    if (!(w & 1) && lane < 3) sts128(cb + (lane == 0 ? CH_Z1 : lane == 1 ? CH_Z0 : CH_PF), v4u{0u, 0u, 0u, lane == 2 ? h01 : 0u}); } while (0)
#define W2GROUP(g) do { dma4ks(w2s + (g) * 15360, l16, w2d + wslot(g)); dma4ks(w2s + (g) * 15360 + 4096, l16, w2d + wslot(g) + 4096); } while (0)

  if (w < 4) {
    const int L = w;
    const unsigned bx = (lane >> 3) * 1280, cx = lane & 7;
    const unsigned cwz = (lane & 15) ^ ((lane >> 4) << 2);
    const int cmask = isfull ? 15 : 7;
    unsigned vx[4], vw[4];
#pragma unroll
    for (int j = 0; j < 4; ++j) {
      vx[j] = bx + ((cx ^ (lane >> 3)) << 4) + j * (8 * 1280 - 1024);
      vw[j] = (lane >> 4) * 3456 + (((cwz ^ j) & cmask) << 4) + j * (4 * 3456 - 1024);
    }
    int chunk = rt < 10 ? rt : rt - 10;
    cptr xb0 = (cptr)p.x + (size_t)(128 * rt + 32 * L) * 1280;
    cptr wb0 = (cptr)p.w1 + (size_t)(16 * L) * 3456 + 128 * u0;
    unsigned reg = L * 4096;
#define STAGE(X) do { dma4v(xb0 + 128 * chunk, vx, reg); dma4v(wb0 + (size_t)chunk * (64 * 3456), vw, reg + 16384); chunk = chunk == 9 ? 0 : chunk + 1; reg ^= (X); } while (0)
    STAGE(32768);
    if (kvrole) {
      unsigned vo[4];
#pragma unroll
      for (int j = 0; j < 4; ++j) vo[j] = bx + ((cx ^ ((4 * L + j) & 7)) << 4) + j * (8 * 1280 - 1024);
      const size_t go = (size_t)(128 * kt + 32 * L) * 1280 + 128 * kc;
      dma4v((cptr)p.k + go, vo, L_E + L * 4096);
      dma4v((cptr)p.v + go, vo, L_E + 16384 + L * 4096);
    } else {
      unsigned vo[4];
#pragma unroll
      for (int j = 0; j < 4; ++j) vo[j] = (lane >> 4) * 1280 + ((cwz ^ j) << 4) + j * (4 * 1280 - 1024);
      const int wnc = wn < 90 ? wn : 0;
      cptr src = (cptr)p.w2 + (size_t)(96 * (wnc / 5)) * 1280 + 256 * (wnc % 5);
      const int g0 = L < 3 ? 2 * L : 4;
      dma4v(src + (size_t)g0 * (16 * 1280), vo, L_E + g0 * 4096);
      dma4v(src + (size_t)(g0 + 1) * (16 * 1280), vo, L_E + g0 * 4096 + 4096);
    }
    const int srow = 128 * rt + 8 * (half * 8 + cti) + 2 * L;
    v4u xa, xc; unsigned xb, xd;
    asm volatile("global_load_dwordx4 %0, %4, %6\n\tglobal_load_dword %1, %5, %6 offset:1024\n\tglobal_load_dwordx4 %2, %4, %6 offset:1280\n\t"
                 "global_load_dword %3, %5, %6 offset:2304"
                 : "=&v"(xa), "=&v"(xb), "=&v"(xc), "=&v"(xd) : "v"(l16), "v"(lane * 4), "s"((cptr)p.x + (size_t)srow * 1280) : "memory");
    STAGE(65536);
    EPOCH_FIN();
    LATE_CONSTS();
    STAMP(1);
    WAITV(12); BAR();
    STAMP(2);
    STAGE(32768); STAGE(65536);
    if (rt < 10) {
      const int g = lane >> 4, q = (lane & 15) >> 2, pp = lane & 3;
      float cs0 = 0.f, cs1 = 0.f;
#pragma unroll
      for (int t = 0; t < 2; ++t) {
        const unsigned rowb = 16384 + (8 * g + 4 * t + q) * 256 + 8 * (pp & 1), swz = (q << 2) | ((2 * g + t) & 3);
        const unsigned a0 = rowb + (((4 * L + (pp >> 1)) ^ swz) << 4), a1 = rowb + (((4 * L + 2 + (pp >> 1)) ^ swz) << 4);
#pragma unroll
        for (int u = 0; u < 2; ++u) {
          v2u r0 = ldstr(a0 + 8192 * u), r1 = ldstr(a1 + 8192 * u);
          cs0 = dot2ones(r0.x, cs0); cs1 = dot2ones(r1.x, cs1); cs0 = dot2ones(r0.y, cs0); cs1 = dot2ones(r1.y, cs1);
        }
      }
      cs0 += __shfl_xor(cs0, 16); cs1 += __shfl_xor(cs1, 16);
      cs0 += __shfl_xor(cs0, 32); cs1 += __shfl_xor(cs1, 32);
      if (lane < 32 && (isfull || L < 2))
        __builtin_amdgcn_raw_buffer_store_b64(v2u{__builtin_bit_cast(unsigned, lane < 16 ? cs0 : cs1), epoch}, rs, WS_CSP + (rt * 1792 + 64 * u0 + 32 * L + lane) * 8, 0, AUX_SC1);
    }
    asm volatile("s_waitcnt vmcnt(16)" : "+v"(xa), "+v"(xb), "+v"(xc), "+v"(xd)::"memory");
    BAR();
    STAGE(32768);
    {
      f32x2 a[5] = {bf2(xa.x), bf2(xa.y), bf2(xa.z), bf2(xa.w), bf2(xb)}, c[5] = {bf2(xc.x), bf2(xc.y), bf2(xc.z), bf2(xc.w), bf2(xd)};
      f32x2 sa = (a[0] + a[1]) + (a[2] + a[3]) + a[4], sc = (c[0] + c[1]) + (c[2] + c[3]) + c[4];
      const float ma = wave_sum(sa[0] + sa[1]) * (1.0f / D), mc = wave_sum(sc[0] + sc[1]) * (1.0f / D);
      f32x2 qa = {0.f, 0.f}, qc = {0.f, 0.f};
#pragma unroll
      for (int i = 0; i < 5; ++i) { f32x2 e = a[i] - ma; qa += e * e; f32x2 f = c[i] - mc; qc += f * f; }
      const float va = wave_sum(qa[0] + qa[1]) * (1.0f / D), vc = wave_sum(qc[0] + qc[1]) * (1.0f / D);
      const unsigned vb = pk_bf(bf2(pk_bf(va, vc)) + 1e-5f);
      const v4u o = {pk_bf(ma, __builtin_amdgcn_sqrtf(bf_lo(vb))), epoch, pk_bf(mc, __builtin_amdgcn_sqrtf(bf_hi(vb))), epoch};
      if (lane < 2) __builtin_amdgcn_raw_buffer_store_b64(lane ? v2u{o.z, o.w} : v2u{o.x, o.y}, rs, WS_STATS + (srow + lane) * 8, 0, AUX_SC1);
    }
    WAITV(17); BAR();
    STAGE(65536);
    WAITV(17); BAR();
    STAGE(32768);
    WAITV(17); BAR();
    STAGE(65536);
    WAITV(16); BAR();
    STAMP(3);
    STAGE(32768);
    WAITV(16); BAR();
    STAGE(65536);
    WAITV(16); BAR();
    STAMP(4);
    if (isfull) { W2GROUP(3); WAITV(16); } else WAITV(8);
    BAR();
    if (!isfull) { H_DMA(); R_DMA(); CONV_PADS(); }
    W2GROUP(2);
    WAITV(16); BAR();
    W2GROUP(0);
    WAITV(16); BAR();
    STAMP(5);
    W2GROUP(1);
    if (b == 0 && w == 3) {
#pragma unroll 1
      for (unsigned spins = 0; spins < SPIN_CAP; ++spins) {
        bool ok = true;
#pragma unroll 1
        for (int i = 0; i < 4; ++i) {
          const int bb = lane + 64 * i, x2 = bb & 7, i2 = bb >> 3, kn = x2 * 20 + i2, w2n = x2 * 12 + i2 - 20;
          const unsigned v = ld32a(p.ws + WS_FP0, (i2 < 20 ? (kn % 10) * 16 + kn / 10 : (w2n < 90 ? 160 + (w2n % 5) * 32 + w2n / 5 : 88 + w2n)) * 4);
          ok = wait_eq1<0>(v, epoch) && ok;
        }
        if (ok) break;
      }
      if (lane == 0) __builtin_amdgcn_raw_buffer_store_b32(epoch, rs, WS_EPOCH, 0, AUX_SC1);
    }
  } else if (w < 8) {
    const int cw = w - 4;
    const int n = lane & 15, g = lane >> 4, q = (lane & 15) >> 2, pp = lane & 3;
    const int tok0 = isfull ? 64 * (cw & 1) : 32 * cw, fg = isfull ? cw >> 1 : 0;
    unsigned ax[2], aw[4][2];
#pragma unroll
    for (int u = 0; u < 2; ++u) ax[u] = (tok0 + n) * 128 + ((((4 * u + g) ^ n) & 7) << 4);
#pragma unroll
    for (int mt = 0; mt < 4; ++mt)
#pragma unroll
      for (int t = 0; t < 2; ++t)
        aw[mt][t] = 16384 + (8 * g + 4 * t + q) * 256 + (((2 * (4 * fg + mt) + (pp >> 1)) ^ ((q << 2) | ((2 * g + t) & 3))) << 4) + 8 * (pp & 1);
    f32x4 acc[4][4];
#pragma unroll
    for (int mt = 0; mt < 4; ++mt)
#pragma unroll
      for (int T = 0; T < 4; ++T) acc[mt][T] = f32x4{0.f, 0.f, 0.f, 0.f};
    v4u xf0[4], wf0[4], xf1[4], wf1[4];
#pragma unroll
    for (int i = 0; i < 4; ++i) { xf1[i] = v4u{0u, 0u, 0u, 0u}; wf1[i] = v4u{0u, 0u, 0u, 0u}; }
    EPOCH_FIN();
    LATE_CONSTS();
#define P1_READ(NT, XF, WF, U) do { \
      { v2u lo = ldstr(aw[0][0] + 8192 * U), hi = ldstr(aw[0][1] + 8192 * U); WF[0] = v4u{lo.x, lo.y, hi.x, hi.y}; } \
      _Pragma("unroll") for (int T = 0; T < NT; ++T) XF[T] = lds128(ax[U] + 2048 * T); \
      _Pragma("unroll") for (int mt = 1; mt < 4; ++mt) { v2u lo = ldstr(aw[mt][0] + 8192 * U), hi = ldstr(aw[mt][1] + 8192 * U); WF[mt] = v4u{lo.x, lo.y, hi.x, hi.y}; } \
    } while (0)
#define P1_MMA(NT, XF, WF) do { \
      _Pragma("unroll") for (int mt = 0; mt < 4; ++mt) _Pragma("unroll") for (int T = 0; T < NT; ++T) acc[mt][T] = mfma(WF[mt], XF[T], acc[mt][T]); \
    } while (0)
#define P1_ILV(NT) do { _Pragma("unroll") for (int i = 0; i < 4 * NT; ++i) { SGB(0x8, 1); SGB(0x100, NT == 4 ? 1 : 2); } SB0(); } while (0)
#define P1_LOOP(NT) do { \
      _Pragma("unroll 1") for (int s = 0; s < 10; ++s) { \
        BAR(); SB0(); \
        P1_READ(NT, xf0, wf0, 0); P1_MMA(NT, xf1, wf1); P1_ILV(NT); \
        P1_READ(NT, xf1, wf1, 1); P1_MMA(NT, xf0, wf0); P1_ILV(NT); \
        const unsigned dx = (s & 1) ? 65536u : 32768u; \
        ax[0] ^= dx; ax[1] ^= dx; \
        _Pragma("unroll") for (int mt = 0; mt < 4; ++mt) { aw[mt][0] ^= dx; aw[mt][1] ^= dx; } \
      } \
      BAR();   \
      P1_MMA(NT, xf1, wf1); \
    } while (0)
    STAMP(1);
    __builtin_amdgcn_s_setprio(2);
    if (isfull) P1_LOOP(4); else P1_LOOP(2);
    STAMP(5);
    f32x4 cs[4];
#pragma unroll
    for (int mt = 0; mt < 4; ++mt) cs[mt] = __builtin_bit_cast(f32x4, lds128(L_CS + (fg * 64 + mt * 16 + g * 4) * 4));
    const unsigned ho = WS_HF + ((8 * rt + (tok0 >> 4)) * 54 + 2 * (u0 + fg)) * 1024 + l16;
#pragma unroll
    for (int T = 0; T < 4; ++T) {
      if (T < 2 || isfull) {
        const f32x2 st = __builtin_bit_cast(f32x2, lds64(L_STATS + (tok0 + 16 * T + n) * 8));
#pragma unroll
        for (int pr = 0; pr < 2; ++pr) {
          unsigned o[4];
#pragma unroll
          for (int j = 0; j < 4; ++j) {
            const int mt = 2 * pr + (j >> 1), e = 2 * (j & 1);
            const f32x2 av = {acc[mt][T][e], acc[mt][T][e + 1]}, cv = {cs[mt][e], cs[mt][e + 1]};
            o[j] = pk_bf(silu_z(av * st[0] + cv * st[1]));
          }
          __builtin_amdgcn_raw_buffer_store_b128(v4u{o[0], o[1], o[2], o[3]}, rs, ho + T * (54 * 1024) + pr * 1024, 0, AUX_SC1);
          SB0();
        }
      }
    }
    __builtin_amdgcn_s_setprio(0);
    STAMP(6);
    WAITV(0);
    if (lane < (isfull ? 2 : 1)) flag_set(p.ws + WS_FH, ((4 * rt + (tok0 >> 5) + lane) * 32 + u0 + fg) * 4, epoch);
    STAMP(7);
    if (cw == 0) poll_atomic(p.ws + WS_FH, tt64 * 256 + lane * 4, 0x07ffffff07ffffffull, epoch);
  } else {
    const int aw = w - 8;
    const int tg8 = (lane & 7) + 8 * (aw & 1), cpl = (lane >> 3) + 8 * (aw >> 1);
    unsigned halo[2][3] = {{0u, 0u, 0u}, {0u, 0u, 0u}};
    v4u shk4[2] = {{0u, 0u, 0u, 0u}, {0u, 0u, 0u, 0u}};
    if (kvrole) {
#pragma unroll
      for (int it = 0; it < 2; ++it) shk4[it] = *(const GAS v4u*)((cptr)p.shk + (size_t)(64 * kc + 2 * cpl + 32 * it) * 8);
      if (tg8 == 0 && kt > 0) {
#pragma unroll
        for (int it = 0; it < 2; ++it)
#pragma unroll
          for (int i = 0; i < 3; ++i) halo[it][i] = *(const GAS unsigned*)((cptr)p.k + (size_t)(128 * kt - 3 + i) * 1280 + 128 * kc + 4 * cpl + 64 * it);
      }
    }
    EPOCH_FIN();
    LATE_CONSTS();
    unsigned ka = L_E + tg8 * 1024 + ((((cpl >> 2) ^ tg8) & 7) << 4) + (cpl & 3) * 4;
    unsigned kp = tg8 ? L_E + (tg8 - 1) * 1024 + ((((cpl >> 2) ^ (tg8 - 1)) & 7) << 4) + (cpl & 3) * 4 : ka;
    unsigned kvo = WS_KVR + (64 * kc + 2 * cpl) * KVR_ROW + (2040 - 128 * kt - 8 * tg8) * 2;
    USE(ka); USE(kp); USE(kvo);
    USE(shk4[0]); USE(shk4[1]);
#pragma unroll
    for (int i = 0; i < 3; ++i) { USE(halo[0][i]); USE(halo[1][i]); }
    STAMP(1);
#pragma unroll 1
    for (int it = 0; it < 2; ++it) {
      BAR();
      if (kvrole) {
        unsigned kk[11], vv[8];
#pragma unroll
        for (int i = 0; i < 3; ++i) { unsigned t = lds32(kp + (5 + i) * 128); kk[i] = tg8 ? t : halo[0][i]; }
#pragma unroll
        for (int i = 0; i < 8; ++i) { kk[3 + i] = lds32(ka + i * 128); vv[i] = lds32(ka + 16384 + i * 128); }
        USE(kk[10]); USE(vv[7]); if (it == 0) STAMP(3);
        const v4u sh = shk4[0];
        const f32x2 s0 = {bf_lo(sh.x), bf_lo(sh.z)}, s1 = {bf_hi(sh.x), bf_hi(sh.z)}, s2 = {bf_lo(sh.y), bf_lo(sh.w)}, s3 = {bf_hi(sh.y), bf_hi(sh.w)};
        f32x2 kf[11], o[8];
#pragma unroll
        for (int i = 0; i < 11; ++i) kf[i] = bf2(kk[i]);
#pragma unroll
        for (int i = 0; i < 8; ++i) {
          f32x2 s = kf[i] * s3 + kf[i + 1] * s2 + kf[i + 2] * s1 + kf[i + 3] * s0;
          o[i] = bf2(pk_bf(s)) * bf2(vv[i]);
        }
        v4u ra = {pk_bf(o[7][0], o[6][0]), pk_bf(o[5][0], o[4][0]), pk_bf(o[3][0], o[2][0]), pk_bf(o[1][0], o[0][0])};
        v4u rb = {pk_bf(o[7][1], o[6][1]), pk_bf(o[5][1], o[4][1]), pk_bf(o[3][1], o[2][1]), pk_bf(o[1][1], o[0][1])};
        USE(ra); USE(rb); if (it == 0) STAMP(6);
        __builtin_amdgcn_raw_buffer_store_b128(ra, rs, kvo, 0, AUX_SC1);
        __builtin_amdgcn_raw_buffer_store_b128(rb, rs, kvo + KVR_ROW, 0, AUX_SC1);
        if (kt == 0 && tg8 == 0) {
          __builtin_amdgcn_raw_buffer_store_b128(v4u{0u, 0u, 0u, 0u}, rs, kvo + 16, 0, AUX_SC1);
          __builtin_amdgcn_raw_buffer_store_b128(v4u{0u, 0u, 0u, 0u}, rs, kvo + KVR_ROW + 16, 0, AUX_SC1);
        }
        if (it == 0) STAMP(7);
        ka ^= 64; kp ^= 64; kvo += 32 * KVR_ROW; shk4[0] = shk4[1];
#pragma unroll
        for (int i = 0; i < 3; ++i) halo[0][i] = halo[1][i];
      } else if (wn < 90) {
        const int g = lane >> 4, q = (lane & 15) >> 2, pp = lane & 3;
        const int dl = aw + 4 * it, dt = 8 * (wn % 5) + dl;
#pragma unroll
        for (int u = 0; u < 3; ++u) {
          v2u h2[2];
#pragma unroll
          for (int t = 0; t < 2; ++t) {
            const int row = 32 * u + 16 * t + 4 * g + q;
            const int swz = (q << 2) | (g & 3);
            h2[t] = ldstr(L_E + row * 256 + (((2 * dl + (pp >> 1)) ^ swz) << 4) + 8 * (pp & 1));
          }
          const int ks = 3 * (wn / 5) + u;
          __builtin_amdgcn_raw_buffer_store_b128(v4u{h2[0].x, h2[0].y, h2[1].x, h2[1].y}, rs, WS_W2T + (((dt / 5) * 54 + ks) * 5 + dt % 5) * 1024 + l16, 0, AUX_SC1);
        }
      }
    }
    STAMP(2);
    BAR();
    WAITV(0); BAR();
    if (aw == 0 && lane == 0) flag_set(p.ws + WS_FP0, (kvrole ? kc * 16 + kt : (wn < 90 ? 160 + (wn % 5) * 32 + wn / 5 : 88 + wn)) * 4, epoch);
    H_DMA(); CONV_PADS();
    BAR();
    BAR();
    const int blk0 = (80 * c80) >> 7, blk1 = (80 * c80 + 79) >> 7;
    const unsigned fo = (aw == 0 ? ((chb + 3) >> 6) * 16 + lane : 160 + blk0 * 32 + lane) * 4;
    const unsigned long long fm = aw == 0 ? 0xffffull : (blk1 != blk0 ? 0x0003ffff0003ffffull : 0x3ffffull);
    unsigned pf = 0;
    if (aw < 2) pf = atom_rd(p.ws + WS_FP0, fo, fm, epoch);
    BAR();
    if (aw < 2) { if (!wait_eq1<0>(pf, epoch)) poll_atomic(p.ws + WS_FP0, fo, fm, epoch); }
    BAR();
    STAMP(4);
    R_DMA();
    v2u gr[10];
    cptr gb = aw < 2 ? p.ws + WS_STATS + (128 * rt + 64 * aw) * 8 : p.ws + WS_CSP + (64 * u0 + (isfull ? 64 * (aw - 2) : 0)) * 8;
    const int gstep = aw < 2 ? 0 : 1792 * 8;
#pragma unroll 1
    for (unsigned spins = 0; spins < SPIN_CAP; ++spins) {
#pragma unroll
      for (int i = 0; i < 10; ++i) gr[i] = __builtin_amdgcn_raw_buffer_load_b64(rs, (unsigned)(gb - p.ws) + i * gstep + lane * 8, 0, AUX_SC1);
      if (spins == 0) { BAR(); BAR(); }
      unsigned bad = 0;
#pragma unroll
      for (int i = 0; i < 10; ++i) bad |= gr[i].y ^ epoch;
      if (__all(bad == 0u)) break;
    }
    if (aw < 2) {
      const float ri = __builtin_amdgcn_rcpf(bf_hi(gr[0].x)) * LOG2E;
      sts64(L_STATS + (64 * aw + lane) * 8, __builtin_bit_cast(v2u, f32x2{-ri, bf_lo(gr[0].x) * ri}));
    } else {
      float c = 0.f;
#pragma unroll
      for (int i = 0; i < 10; ++i) c += __builtin_bit_cast(float, gr[i].x);
      sts32(L_CS + (64 * (aw - 2) + lane) * 4, __builtin_bit_cast(unsigned, c));
    }
    WAITV(0); BAR();
    STAMP(5);
  }
  LATE_CONSTS();
  STAMP(8);
  if (convw) {
    const int rh = w & 1;
    const int i16 = lane & 15, kg = lane >> 4;
    const unsigned rbase = cb + ((i16 & 1) ? CH_R1 : CH_R0);
    unsigned va = rbase + 4032 - 3584 - 64 * (i16 >> 1) + 16 * kg + 256 * rh;
    const unsigned vamax = rbase + 4096;
    const unsigned hbase = cb + ((i16 & 1) ? CH_H0 : CH_H1);
    unsigned vb = hbase + 4 * i16 - ((i16 & 1) ? 60 : 64) + 16 * kg + 256 * rh;
    const unsigned vbmin = hbase - 16;
    USE(va); USE(vb);
    f32x4 acc[8];
#pragma unroll
    for (int i = 0; i < 8; ++i) acc[i] = f32x4{0.f, 0.f, 0.f, 0.f};
#define LDA_(i, o) lds128(va + 3584 - 512 * (i) + (o))
#define LDB_(dst, i, o) do { v2u lo = lds64v(vb + 512 * (i) + (o)), hi = lds64v(vb + 512 * (i) + (o) + 8); dst = v4u{lo.x, lo.y, hi.x, hi.y}; } while (0)
#define LDP(i, o) do { A[i] = LDA_(i, o); LDB_(B[i], i, o); } while (0)
#define STEP(m) do { _Pragma("unroll") for (int o = 0; o < (m); ++o) if ((m) + o <= 7) acc[(m) + o] = mfma(A[m], B[o], acc[(m) + o]); \
      if (2 * (m) <= 7) acc[2 * (m)] = mfma(A[m], B[m], acc[2 * (m)]); \
      _Pragma("unroll") for (int o = 0; o < (m); ++o) if ((m) + o <= 7) acc[(m) + o] = mfma(A[o], B[m], acc[(m) + o]); } while (0)
#define ILV(n) do { _Pragma("unroll") for (int i_ = 0; i_ < (n); ++i_) { SGB(0x8, 1); SGB(0x100, 1); } SGB(0x8, 16); SB0(); } while (0)
    v4u A[8], B[8];
    {
      A[0] = lds128(min(va + 3584, vamax));
      v2u lo = lds64v(max(vb, vbmin)), hi = lds64v(max(vb + 8, vbmin));
      B[0] = v4u{lo.x, lo.y, hi.x, hi.y};
      LDP(1, 0); LDP(2, 0); LDP(3, 0);
    }
    SB0();
#pragma unroll 1
    for (int r = 0; r < 4; ++r) {
      v4u nA0, nB0, nA1, nB1;
      LDP(4, 0); STEP(0); STEP(1); ILV(3);
      LDP(5, 0); STEP(2); ILV(3);
      LDP(6, 0); LDP(7, 0); STEP(3); ILV(6);
      nA0 = lds128(min(va + 3584 + 64, vamax)); LDB_(nB0, 0, 64); nA1 = LDA_(1, 64); LDB_(nB1, 1, 64); STEP(4); ILV(6);
      LDP(3, 64); STEP(5); ILV(3);
      LDP(2, 64); STEP(6); STEP(7); ILV(3);
      A[0] = nA0; B[0] = nB0; A[1] = nA1; B[1] = nB1;
      va += 64; vb += 64; USE(va); USE(vb);
    }
    const int chunk = 128 * rh + lane;
    const v4u kva = lds128(cb + CH_R1 + (255 - chunk) * 16), kvb = lds128(cb + CH_R1 + (191 - chunk) * 16);
    const float dD = bf_lo(*(kptr)((cptr)p.sD + (cch >> 1) * 4) >> ((cch & 1) * 16));
    STAMP(9);
    BAR();
    {
      const unsigned a = cb + rh * 8192 + (64 * (lane >> 4) + 2 * (lane & 15)) * 4;
#pragma unroll
      for (int i = 0; i < 8; ++i) {
        sts64(a + 1024 * i, __builtin_bit_cast(v2u, f32x2{acc[i][0], acc[i][1]}));
        sts64(a + 1024 * i + 128, __builtin_bit_cast(v2u, f32x2{acc[i][2], acc[i][3]}));
      }
    }
    WAITV(0); BAR();
    conv_final(rs, cb, cch, chunk, kva, dD);
    conv_final(rs, cb, cch, chunk + 64, kvb, dD);
  } else {
    BAR();
  }
  STAMP(10);
#define Y_FLAG() do { if (convw && lane == 0) flag_set(p.ws + WS_FY, ((w & 1) * 640 + cch) * 4, epoch); } while (0)
  if (w < 4) {
    if (!convw) { WAITV(0); BAR(); }
    BAR();
    if (!isfull) W2GROUP(3);
    W2GROUP(4); W2GROUP(5);
    BAR(); W2GROUP(6);
    WAITV(24); Y_FLAG();
    BAR(); W2GROUP(7);
    WAITV(24); BAR(); W2GROUP(8);
    WAITV(24); BAR();
    WAITV(16); BAR();
    WAITV(8); BAR();
    WAITV(0); BAR();
  } else if (w < 8) {
    const int cw = w - 4, tp = cw & 1, kh = cw >> 1;
    const unsigned hoff = WS_HF + ((4 * tt64 + 2 * tp) * 54 + 27 * kh) * 1024 + l16;
    constexpr int HW = 8;
    v4u hf[27][2];
#define H_LD(i) do { hf[i][0] = __builtin_amdgcn_raw_buffer_load_b128(rs, hoff + (i) * 1024, 0, AUX_SC1); \
      hf[i][1] = __builtin_amdgcn_raw_buffer_load_b128(rs, hoff + (i) * 1024 + 54 * 1024, 0, AUX_SC1); } while (0)
#pragma unroll
    for (int i = 0; i < HW; ++i) H_LD(i);
    BAR();
    STAMP(11);
    f32x4 acc[2][5];
#pragma unroll
    for (int i = 0; i < 5; ++i) acc[0][i] = acc[1][i] = f32x4{0.f, 0.f, 0.f, 0.f};
    unsigned wa[3] = {kh * 15360 + l16, kh * 15360 + l16 + 65536, kh * 15360 + l16 + 131072};
    USE(wa[0]); USE(wa[1]); USE(wa[2]);
    v4u a0[5], a1[5];
#define P3_RD(A, ks) do { _Pragma("unroll") for (int i = 0; i < 5; ++i) A[i] = lds128(wa[wslot((ks) / 3) >> 16] + (wslot((ks) / 3) & 65535) + (((ks) % 3) * 5 + i) * 1024); } while (0)
#define P3_MM(A, ks) do { \
      _Pragma("unroll") for (int i = 0; i < 5; ++i) { acc[0][i] = mfma(A[i], hf[ks][0], acc[0][i]); acc[1][i] = mfma(A[i], hf[ks][1], acc[1][i]); } \
      if ((ks) + HW < 27) H_LD((ks) + HW); } while (0)
#define P3_ILV() do { _Pragma("unroll") for (int i = 0; i < 5; ++i) { SGB(0x8, 1); SGB(0x100, 1); } SGB(0x8, 5); SB0(); } while (0)
    __builtin_amdgcn_s_setprio(2);
    P3_RD(a0, 0);
    SB0();
#pragma unroll
    for (int ks = 0; ks < 26; ks += 2) {
      if ((ks + 1) % 3 == 0) { BAR(); SB0(); }
      P3_RD(a1, ks + 1); P3_MM(a0, ks); P3_ILV();
      if ((ks + 2) % 3 == 0) { BAR(); SB0(); }
      P3_RD(a0, ks + 2); P3_MM(a1, ks + 1); P3_ILV();
      if (ks == 2) STAMP(12);
      if (ks == 12) STAMP(13);
    }
    P3_MM(a0, 26);
    STAMP(14);
    {
      const unsigned oa = L_O + kh * (64 * OROW) + (32 * tp + (lane & 15)) * OROW + (lane >> 4) * 16;
#pragma unroll
      for (int i = 0; i < 5; ++i) { sts128(oa + i * 64, __builtin_bit_cast(v4u, acc[0][i])); sts128(oa + 16 * OROW + i * 64, __builtin_bit_cast(v4u, acc[1][i])); }
    }
    __builtin_amdgcn_s_setprio(0);
  } else {
    const int aw = w - 8, at = tid - 512;
    if (!convw) { BAR(); }
    BAR();
    WAITV(0); Y_FLAG();
    v4u fx[3]; v2u fs[3];
#pragma unroll
    for (int i = 0; i < 3; ++i) {
      int it = at + 256 * i; if (it >= 640) it = 639;
      const int tok = it / 10, c8 = it % 10;
      fx[i] = *(const GAS v4u*)((cptr)p.x + (size_t)(64 * tt64 + tok) * 1280 + c80 * 160 + c8 * 16);
      fs[i] = __builtin_amdgcn_raw_buffer_load_b64(rs, WS_STATS + (64 * tt64 + tok) * 8, 0, AUX_SC1);
    }
    BAR();
    BAR();
    const unsigned fyo = ((tt64 >> 4) * 640 + c80 * 80 + aw * 20 + lane) * 4;
    const unsigned py = atom_rd(p.ws + WS_FY, fyo, 0xfffffull, epoch);
    BAR();
    if (!wait_eq1<0>(py, epoch)) poll_atomic(p.ws + WS_FY, fyo, 0xfffffull, epoch);
    v4u yv[3];
#pragma unroll
    for (int i = 0; i < 3; ++i) {
      int it = i * 64 + lane; if (it >= 160) it = 159;
      yv[i] = __builtin_amdgcn_raw_buffer_load_b128(rs, WS_YT + (c80 * 80 + aw * 20 + (it >> 3)) * 4096 + tt64 * 128 + (it & 7) * 16, 0, AUX_SC1);
    }
    BAR();
    BAR();
#pragma unroll
    for (int i = 0; i < 3; ++i) {
      int it = i * 64 + lane; if (it >= 160) it = 159;
      const unsigned a = L_Y + (it & 7) * (8 * 160 + 16) + (aw * 20 + (it >> 3)) * 2;
      sts16(a, yv[i].x); sts16(a + 160, yv[i].x >> 16); sts16(a + 320, yv[i].y); sts16(a + 480, yv[i].y >> 16);
      sts16(a + 640, yv[i].z); sts16(a + 800, yv[i].z >> 16); sts16(a + 960, yv[i].w); sts16(a + 1120, yv[i].w >> 16);
    }
    BAR();
#pragma unroll
    for (int i = 0; i < 3; ++i) {
      if (i == 2) { BAR(); }
      const int it = at + 256 * i;
      if (it < 640) {
        const int tok = it / 10, c8 = it % 10;
        const unsigned a = L_Y + tok * 160 + (tok >> 3) * 16 + c8 * 16;
        const v4u yv4 = lds128(a);
        const float m = bf_lo(fs[i].x), ri = __builtin_amdgcn_rcpf(bf_hi(fs[i].x));
        const unsigned xw[4] = {fx[i].x, fx[i].y, fx[i].z, fx[i].w}, yw[4] = {yv4.x, yv4.y, yv4.z, yv4.w};
        unsigned sw[4];
#pragma unroll
        for (int j = 0; j < 4; ++j) sw[j] = pk_bf(bf2(pk_bf(bf2(pk_bf(bf2(xw[j]) - m)) * ri)) * bf2(yw[j]));
        sts128(a, v4u{sw[0], sw[1], sw[2], sw[3]});
        sts128(L_X + it * 16, fx[i]);
      }
    }
  }
  STAMP(15);
  BAR();
  STAMP(16);
  if (tid < 640) {
    const int ftok = tid / 10, fc8 = tid % 10;
    const unsigned oa = L_O + ftok * OROW + fc8 * 32;
    f32x4 o0 = __builtin_bit_cast(f32x4, lds128(oa)) + __builtin_bit_cast(f32x4, lds128(oa + 64 * OROW));
    f32x4 o1 = __builtin_bit_cast(f32x4, lds128(oa + 16)) + __builtin_bit_cast(f32x4, lds128(oa + 64 * OROW + 16));
    const v4u sv = lds128(L_Y + ftok * 160 + (ftok >> 3) * 16 + fc8 * 16), xv = lds128(L_X + tid * 16);
    f32x2 o[4] = {{o0[0], o0[1]}, {o0[2], o0[3]}, {o1[0], o1[1]}, {o1[2], o1[3]}};
    unsigned xw[4] = {xv.x, xv.y, xv.z, xv.w}, sw[4] = {sv.x, sv.y, sv.z, sv.w}, rw[4];
#pragma unroll
    for (int j = 0; j < 4; ++j) {
      const f32x2 gb = silu_z(o[j] * -LOG2E);
      rw[j] = pk_bf(bf2(xw[j]) + bf2(sw[j]) * gb);
    }
    *(GAS v4u*)((unsigned char*)p.out + (size_t)(64 * tt64 + ftok) * 1280 + c80 * 160 + fc8 * 16) = v4u{rw[0], rw[1], rw[2], rw[3]};
  }
}

extern "C" void kernel_launch(void* const* d_in, const int* in_sizes, int n_in, void* d_out, int out_size, void* d_ws, size_t ws_size,
                              hipStream_t stream) {
  Params p;
  p.x = (const u16*)d_in[0]; p.k = (const u16*)d_in[1]; p.v = (const u16*)d_in[2];
  p.shk = (const u16*)d_in[3]; p.sk = (const u16*)d_in[4]; p.sD = (const u16*)d_in[5];
  p.w1 = (const u16*)d_in[7]; p.w2 = (const u16*)d_in[8];
  p.out = (u16*)d_out; p.ws = (unsigned char*)d_ws;
  fused_kernel<<<NWG, NTHR, 0, stream>>>(p);
}
```

```cpp
#define PF_CODE 35000
#define PF_TLB 1
#include <hip/hip_runtime.h>
#include <stdint.h>

typedef unsigned short u16;
typedef unsigned v4u __attribute__((ext_vector_type(4)));
typedef unsigned v2u __attribute__((ext_vector_type(2)));
typedef float f32x4 __attribute__((ext_vector_type(4)));
typedef float f32x2 __attribute__((ext_vector_type(2)));
typedef short bf16x8 __attribute__((ext_vector_type(8)));
typedef short v4s __attribute__((ext_vector_type(4)));
typedef __bf16 v2bf __attribute__((ext_vector_type(2)));
#define LAS __attribute__((address_space(3)))
#define GAS __attribute__((address_space(1)))
#define DEV __device__ __forceinline__
typedef const unsigned char* cptr;
typedef const __attribute__((address_space(4))) unsigned* kptr;

constexpr int S = 2048, D = 640, F = 1728;
constexpr int NWG = 256, NTHR = 768;
constexpr unsigned WS_EPOCH = 0;
constexpr unsigned WS_FP0 = 1024;
constexpr unsigned WS_FH = 4096;
constexpr unsigned WS_FY = 16384;
constexpr unsigned WS_STATS = 32768;
constexpr unsigned KVR_ROW = 4224;
constexpr unsigned WS_KVR = 65536;
constexpr unsigned WS_W2T = WS_KVR + 640 * KVR_ROW;
constexpr unsigned WS_HF = WS_W2T + 8 * 54 * 5 * 1024;
constexpr unsigned WS_YT = WS_HF + 128 * 54 * 1024;
constexpr unsigned WS_CSP = WS_YT + 640 * 4096;
constexpr unsigned WS_END = WS_CSP + 10 * 1792 * 8;
constexpr unsigned WS_DBG = WS_YT + 640 * 4096 + 262144;
constexpr unsigned L_E = 131072;
constexpr unsigned L_STATS = 65536 + 30720;
constexpr unsigned L_CS = 30720;
constexpr unsigned CH_R1 = 0, CH_Z1 = 4096, CH_R0 = 4128, CH_Z0 = 8224, CH_H0 = 8240, CH_PF = 12416, CH_H1 = 12432, CH_BYTES = 16528;
constexpr unsigned L_SYNC = 163840 - 128;
constexpr unsigned W_FULL = L_SYNC, W_FREE = L_SYNC + 16, W_AP = L_SYNC + 32, W_CV = L_SYNC + 48, W_PL = L_SYNC + 80;
constexpr unsigned L_AREA0 = L_SYNC - 4 * CH_BYTES;
constexpr unsigned L_P0 = L_E;
constexpr unsigned L_Y = L_E;
constexpr unsigned L_X = L_E + 12288;
constexpr unsigned L_O = 0;
constexpr unsigned OROW = 336;
constexpr unsigned SPIN_CAP = 1u << 15;
constexpr int AUX_SC1 = 16;
constexpr unsigned wslot(int g) { return (g % 5) * 32768u; }

DEV float bf_lo(unsigned u) { return __builtin_bit_cast(float, u << 16); }
DEV float bf_hi(unsigned u) { return __builtin_bit_cast(float, u & 0xffff0000u); }
DEV f32x2 bf2(unsigned u) { return f32x2{bf_lo(u), bf_hi(u)}; }
DEV unsigned pk_bf(float a, float b) { v2bf r = {(__bf16)a, (__bf16)b}; return __builtin_bit_cast(unsigned, r); }
DEV unsigned pk_bf(f32x2 a) { return pk_bf(a[0], a[1]); }
DEV float rbf(float a) { return bf_lo(pk_bf(a, a) & 0xffffu); }
DEV f32x4 mfma(v4u a, v4u b, f32x4 c) {
  return __builtin_amdgcn_mfma_f32_16x16x32_bf16(__builtin_bit_cast(bf16x8, a), __builtin_bit_cast(bf16x8, b), c, 0, 0, 0);
}
constexpr float LOG2E = 1.44269504088896f;
DEV f32x2 silu_z(f32x2 z) {
  f32x2 e = {__builtin_amdgcn_exp2f(z[0]), __builtin_amdgcn_exp2f(z[1])};
  f32x2 d = e * -LOG2E - LOG2E;
  f32x2 r = {__builtin_amdgcn_rcpf(d[0]), __builtin_amdgcn_rcpf(d[1])};
  return z * r;
}
DEV float dot2ones(unsigned u, float acc) {
  return __builtin_amdgcn_fdot2_f32_bf16(__builtin_bit_cast(v2bf, u), __builtin_bit_cast(v2bf, 0x3f803f80u), acc, false);
}

DEV void dma16(const void* sbase, unsigned voff, unsigned ldsoff) {
  asm volatile("s_mov_b32 m0, %2\n\ts_nop 0\n\tglobal_load_lds_dwordx4 %0, %1" ::"v"(voff), "s"(sbase), "s"(ldsoff) : "memory");
}
DEV void dma16s(const void* sbase, unsigned voff, unsigned ldsoff) {
  asm volatile("s_mov_b32 m0, %2\n\ts_nop 0\n\tglobal_load_lds_dwordx4 %0, %1 sc1" ::"v"(voff), "s"(sbase), "s"(ldsoff) : "memory");
}
DEV void dma4k(const void* sbase, unsigned voff, unsigned ldsoff) {
  asm volatile("s_mov_b32 m0, %2\n\ts_nop 0\n\tglobal_load_lds_dwordx4 %0, %1\n\tglobal_load_lds_dwordx4 %0, %1 offset:1024\n\t"
               "global_load_lds_dwordx4 %0, %1 offset:2048\n\tglobal_load_lds_dwordx4 %0, %1 offset:3072" ::"v"(voff), "s"(sbase), "s"(ldsoff) : "memory");
}
DEV void dma4ks(const void* sbase, unsigned voff, unsigned ldsoff) {
  asm volatile("s_mov_b32 m0, %2\n\ts_nop 0\n\tglobal_load_lds_dwordx4 %0, %1 sc1\n\tglobal_load_lds_dwordx4 %0, %1 offset:1024 sc1\n\t"
               "global_load_lds_dwordx4 %0, %1 offset:2048 sc1\n\tglobal_load_lds_dwordx4 %0, %1 offset:3072 sc1" ::"v"(voff), "s"(sbase), "s"(ldsoff) : "memory");
}
DEV void dma4v(const void* sbase, const unsigned (&vo)[4], unsigned ldsoff) {
  asm volatile("s_mov_b32 m0, %5\n\ts_nop 0\n\tglobal_load_lds_dwordx4 %0, %4\n\tglobal_load_lds_dwordx4 %1, %4 offset:1024\n\t"
               "global_load_lds_dwordx4 %2, %4 offset:2048\n\tglobal_load_lds_dwordx4 %3, %4 offset:3072"
               ::"v"(vo[0]), "v"(vo[1]), "v"(vo[2]), "v"(vo[3]), "s"(sbase), "s"(ldsoff) : "memory");
}
DEV void dma4vm(const void* sbase, const unsigned (&vo)[4], unsigned ldsoff, unsigned long long lastmask) {
  asm volatile("s_mov_b32 m0, %5\n\ts_nop 0\n\tglobal_load_lds_dwordx4 %0, %4\n\tglobal_load_lds_dwordx4 %1, %4 offset:1024\n\t"
               "global_load_lds_dwordx4 %2, %4 offset:2048\n\ts_mov_b64 exec, %6\n\tglobal_load_lds_dwordx4 %3, %4 offset:3072\n\ts_mov_b64 exec, -1"
               ::"v"(vo[0]), "v"(vo[1]), "v"(vo[2]), "v"(vo[3]), "s"(sbase), "s"(ldsoff), "s"(lastmask) : "memory");
}
DEV void touch(const void* sbase, unsigned voff) {
  asm volatile("s_mov_b32 m0, 0x28000\n\ts_nop 0\n\tglobal_load_lds_dword %0, %1" ::"v"(voff), "s"(sbase) : "memory");
}
DEV v4u ld128a(const void* sbase, unsigned voff) { v4u r; asm volatile("global_load_dwordx4 %0, %1, %2 sc1" : "=v"(r) : "v"(voff), "s"(sbase) : "memory"); return r; }
DEV v2u ld64a(const void* sbase, unsigned voff) { v2u r; asm volatile("global_load_dwordx2 %0, %1, %2 sc1" : "=v"(r) : "v"(voff), "s"(sbase) : "memory"); return r; }
DEV unsigned atom_rd(const void* sbase, unsigned voff, unsigned long long mask, unsigned dflt) {
  unsigned r; unsigned long long sv;
  asm volatile("v_mov_b32 %0, %6\n\ts_mov_b64 %1, exec\n\ts_mov_b64 exec, %5\n\tglobal_atomic_or %0, %2, %3, %4 sc0 sc1\n\ts_mov_b64 exec, %1"
               : "=&v"(r), "=&s"(sv) : "v"(voff), "v"(0u), "s"(sbase), "s"(mask), "s"(dflt) : "memory");
  return r;
}
DEV unsigned ld32a(const void* sbase, unsigned voff) { unsigned r; asm volatile("global_load_dword %0, %1, %2 sc1" : "=v"(r) : "v"(voff), "s"(sbase) : "memory"); return r; }
#define WAITV(n) asm volatile("s_waitcnt vmcnt(" #n ")" ::: "memory")
#define USE(x) asm volatile("" : "+v"(x))
#define BAR0() do { asm volatile("s_waitcnt lgkmcnt(0)" ::: "memory"); __builtin_amdgcn_s_barrier(); asm volatile("" ::: "memory"); } while (0)
#define SGB(m, n) __builtin_amdgcn_sched_group_barrier(m, n, 0)
#define SB0() __builtin_amdgcn_sched_barrier(0)

DEV v4u lds128(unsigned a) { return *(const LAS v4u*)(size_t)a; }
DEV v2u lds64(unsigned a) { return *(const LAS v2u*)(size_t)a; }
DEV v2u lds64v(unsigned a) { return *(const volatile LAS v2u*)(size_t)a; }
DEV unsigned lds32(unsigned a) { return *(const LAS unsigned*)(size_t)a; }
DEV void sts128(unsigned a, v4u v) { *(LAS v4u*)(size_t)a = v; }
DEV void sts64(unsigned a, v2u v) { *(LAS v2u*)(size_t)a = v; }
DEV void sts32(unsigned a, unsigned v) { *(LAS unsigned*)(size_t)a = v; }
DEV void sts16(unsigned a, unsigned v) { *(LAS u16*)(size_t)a = (u16)v; }
DEV v2u ldstr(unsigned a) { return __builtin_bit_cast(v2u, __builtin_amdgcn_ds_read_tr16_b64_v4i16((LAS v4s*)(size_t)a)); }

DEV void pub(unsigned a, unsigned v, int lane) { if (lane == 0) *(volatile LAS unsigned*)(size_t)a = v; }
DEV v4u peek4(unsigned a) { return *(const volatile LAS v4u*)(size_t)a; }
DEV bool ge4(v4u v, unsigned t) { return (unsigned)__builtin_amdgcn_readfirstlane(min(min(v.x, v.y), min(v.z, v.w))) >= t; }
DEV void wait4(unsigned a, unsigned t) {
#pragma unroll 1
  for (unsigned i = 0; i < (1u << 22); ++i) { if (ge4(peek4(a), t)) break; __builtin_amdgcn_s_sleep(1); }
  asm volatile("" ::: "memory");
}
DEV void wait2(unsigned a, unsigned t) {
#pragma unroll 1
  for (unsigned i = 0; i < (1u << 22); ++i) {
    v2u v = *(const volatile LAS v2u*)(size_t)a;
    if ((unsigned)__builtin_amdgcn_readfirstlane(min(v.x, v.y)) >= t) break;
    __builtin_amdgcn_s_sleep(1);
  }
  asm volatile("" ::: "memory");
}
#define WAITL() asm volatile("s_waitcnt lgkmcnt(0)" ::: "memory")
template <int CTRL, int RM> DEV float dpp_add(float v) {
  return v + __builtin_bit_cast(float, __builtin_amdgcn_update_dpp(0, __builtin_bit_cast(int, v), CTRL, RM, 0xf, false));
}
DEV float wave_sum(float v) {
  v = dpp_add<0x111, 0xf>(v); v = dpp_add<0x112, 0xf>(v); v = dpp_add<0x114, 0xf>(v); v = dpp_add<0x118, 0xf>(v);
  v = dpp_add<0x142, 0xa>(v); v = dpp_add<0x143, 0xc>(v);
  return __builtin_bit_cast(float, __builtin_amdgcn_readlane(__builtin_bit_cast(int, v), 63));
}

#define BAR() BAR0()
#define STAMP(i)

struct Params {
  const u16 *x, *k, *v, *shk, *sk, *sD, *w1, *w2;
  u16* out;
  unsigned char* ws;
};

template <int N> DEV bool wait_eq1(unsigned v, unsigned epoch) {
  unsigned d;
  asm volatile("s_waitcnt vmcnt(%3)\n\tv_xor_b32 %0, %2, %1" : "=v"(d) : "v"(v), "s"(epoch), "i"(N) : "memory");
  return __all(d == 0u);
}
template <int N> DEV bool wait_eq4(v4u v, unsigned epoch) {
  unsigned d, e;
  asm volatile("s_waitcnt vmcnt(%7)\n\tv_xor_b32 %0, %6, %2\n\tv_xor_b32 %1, %6, %3\n\tv_or_b32 %0, %0, %1\n\tv_xor_b32 %1, %6, %4\n\t"
               "v_or_b32 %0, %0, %1\n\tv_xor_b32 %1, %6, %5\n\tv_or_b32 %0, %0, %1"
               : "=&v"(d), "=&v"(e) : "v"(v.x), "v"(v.y), "v"(v.z), "v"(v.w), "s"(epoch), "i"(N) : "memory");
  return __all(d == 0u);
}
DEV void poll1(cptr base, unsigned off, unsigned epoch) {
#pragma unroll 1
  for (unsigned spins = 0; spins < SPIN_CAP; ++spins) {
    unsigned v = ld32a(base, off);
    if (wait_eq1<0>(v, epoch)) break;
    __builtin_amdgcn_s_sleep(6);
  }
}
DEV void poll4(cptr base, unsigned off, unsigned epoch) {
#pragma unroll 1
  for (unsigned spins = 0; spins < SPIN_CAP; ++spins) {
    v4u v = ld128a(base, off);
    if (wait_eq4<0>(v, epoch)) break;
    __builtin_amdgcn_s_sleep(6);
  }
}
DEV void flag_set(const void* sbase, unsigned voff, unsigned epoch) {
  asm volatile("global_atomic_swap %0, %1, %2 sc1" ::"v"(voff), "v"(epoch), "s"(sbase) : "memory");
}
DEV void poll_atomic(cptr base, unsigned off, unsigned long long mask, unsigned epoch) {
#pragma unroll 1
  for (unsigned spins = 0; spins < SPIN_CAP; ++spins) {
    const unsigned v = atom_rd(base, off, mask, epoch);
    if (wait_eq1<0>(v, epoch)) break;
    __builtin_amdgcn_s_sleep(2);
  }
}
template <int N> DEV unsigned epoch_fin(unsigned ev) {
  unsigned e;
  asm volatile("s_waitcnt vmcnt(%2)\n\tv_readfirstlane_b32 %0, %1\n\ts_nop 4" : "=s"(e) : "v"(ev), "i"(N) : "memory");
  return e * 2654435761u + 0x9E3779B9u;
}

DEV void conv_final(__amdgpu_buffer_rsrc_t rs, unsigned db, int ch, int chunk, v4u kvr, float dD) {
  const unsigned a = db + chunk * 32;
  f32x4 p0 = __builtin_bit_cast(f32x4, lds128(a)), p1 = __builtin_bit_cast(f32x4, lds128(a + 16));
  f32x4 q0 = __builtin_bit_cast(f32x4, lds128(a + 8192)), q1 = __builtin_bit_cast(f32x4, lds128(a + 8192 + 16));
  p0 += q0; p1 += q1;
  unsigned y01 = pk_bf(p0[0], p0[1]), y23 = pk_bf(p0[2], p0[3]), y45 = pk_bf(p1[0], p1[1]), y67 = pk_bf(p1[2], p1[3]);
  unsigned d01 = pk_bf(bf_hi(kvr.w) * dD, bf_lo(kvr.w) * dD), d23 = pk_bf(bf_hi(kvr.z) * dD, bf_lo(kvr.z) * dD);
  unsigned d45 = pk_bf(bf_hi(kvr.y) * dD, bf_lo(kvr.y) * dD), d67 = pk_bf(bf_hi(kvr.x) * dD, bf_lo(kvr.x) * dD);
  v4u o = {pk_bf(bf2(y01) + bf2(d01)), pk_bf(bf2(y23) + bf2(d23)), pk_bf(bf2(y45) + bf2(d45)), pk_bf(bf2(y67) + bf2(d67))};
  __builtin_amdgcn_raw_buffer_store_b128(o, rs, WS_YT + ch * 4096 + chunk * 16, 0, AUX_SC1);
}

__global__ void __launch_bounds__(NTHR, 3) fused_kernel(Params p) {
  __shared__ __attribute__((aligned(16))) unsigned char smem[163840];
  const int tid = threadIdx.x, lane = tid & 63;
  const int w = __builtin_amdgcn_readfirstlane(tid >> 6);
  const int b = blockIdx.x, xcd = b & 7, idx = b >> 3;
  const int half = xcd & 1, cti = idx & 7;
  const int rt = 4 * (xcd >> 1) + (idx >> 3);
  const int nfull = 6 - half;
  const bool isfull = cti < nfull;
  const int u0 = 14 * half + (isfull ? 2 * cti : nfull + cti);
  const unsigned l16 = lane * 16;
  { unsigned lb = (unsigned)(size_t)(LAS unsigned char*)smem; asm volatile("" ::"s"(lb)); }
  unsigned stv = 0; int stn = 32; (void)stv; (void)stn;
  cptr kpc; asm volatile("s_getpc_b64 %0" : "=s"(kpc));
  STAMP(0);
  unsigned epoch, epraw;
  asm volatile("s_load_dword %0, %1, 0x0" : "=s"(epraw) : "s"(p.ws + WS_EPOCH) : "memory");
#define EPOCH_FIN() asm volatile("s_waitcnt lgkmcnt(0)\n\ts_mul_i32 %0, %1, 0x9E3779B1\n\ts_add_u32 %0, %0, 0x9E3779B9" : "=s"(epoch) : "s"(epraw) : "memory", "scc")
  const bool kvrole = idx < 20;
  const int kvn = xcd * 20 + idx, kt = kvn / 10, kc = kvn % 10;
  const int wn = xcd * 12 + idx - 20;
#define LATE_CONSTS() \
  int rt_ = rt; asm volatile("" : "+s"(rt_)); \
  const __amdgpu_buffer_rsrc_t rs = __builtin_amdgcn_make_buffer_rsrc((void*)p.ws, 0, (int)WS_END, 0x00020000); \
  const int tt64 = 2 * rt_ + (cti >> 2), c80 = 4 * half + (cti & 3); \
  const int hn = rt_ * 5 + half * 2 + cti - nfull; \
  const int area = w >= 8 ? 2 + ((w >> 1) & 1) : (w >> 1) & 1; \
  const bool convw = isfull ? w >= 8 : (hn < 72 && (w < 4 || w >= 8)); \
  const int chb = isfull ? 2 * (rt_ * 11 + half * 6 + cti) - 2 : (hn < 72 ? 352 + 4 * hn : 0); \
  const int cch = (isfull && area < 2) ? chb + 2 : chb + area; \
  const unsigned cb = L_AREA0 + area * CH_BYTES; \
  cptr w2s = p.ws + WS_W2T + c80 * (54 * 5 * 1024) + ((w >> 1) & 1) * (27 * 5 * 1024) + (w & 1) * 7168; \
  const unsigned w2d = ((w >> 1) & 1) * 15360 + (w & 1) * 7168; \
  (void)rs; (void)tt64; (void)convw; (void)cch; (void)cb; (void)w2s; (void)w2d; (void)chb; (void)hn;
#define H_DMA() dma4k((cptr)p.sk + (size_t)cch * 4096 + (w & 1) * 4, l16, cb + ((w & 1) ? CH_H1 : CH_H0))
#define R_DMA() dma4ks(p.ws + WS_KVR + (size_t)cch * KVR_ROW + (w & 1) * 2, l16, cb + ((w & 1) ? CH_R0 : CH_R1))
#define CONV_PADS() do { const unsigned h01 = *(kptr)((cptr)p.sk + (size_t)cch * 4096); \
    if (!(w & 1) && lane < 3) sts128(cb + (lane == 0 ? CH_Z1 : lane == 1 ? CH_Z0 : CH_PF), v4u{0u, 0u, 0u, lane == 2 ? h01 : 0u}); } while (0)
#define W2GROUP(g) do { dma4ks(w2s + (g) * 15360, l16, w2d + wslot(g)); dma4ks(w2s + (g) * 15360 + 4096, l16, w2d + wslot(g) + 4096); } while (0)

  if (w < 4) {
    const int L = w;
    const unsigned bx = (lane >> 3) * 1280, cx = lane & 7;
    const unsigned cwz = (lane & 15) ^ ((lane >> 4) << 2);
    const int cmask = isfull ? 15 : 7;
    unsigned vx[4], vw[4];
#pragma unroll
    for (int j = 0; j < 4; ++j) {
      vx[j] = bx + ((cx ^ (lane >> 3)) << 4) + j * (8 * 1280 - 1024);
      vw[j] = (lane >> 4) * 3456 + (((cwz ^ j) & cmask) << 4) + j * (4 * 3456 - 1024);
    }
    int chunk = 3 * (xcd >> 1);
    cptr xb0 = (cptr)p.x + (size_t)(128 * rt + 32 * L) * 1280;
    cptr wb0 = (cptr)p.w1 + (size_t)(16 * L) * 3456 + 128 * u0;
    unsigned reg = L * 4096, rx = 32768;
#define STAGE() do { dma4v(xb0 + 128 * chunk, vx, reg); dma4v(wb0 + (size_t)chunk * (64 * 3456), vw, reg + 16384); chunk = chunk == 9 ? 0 : chunk + 1; reg ^= rx; rx ^= 98304; } while (0)
    STAGE();
    BAR0();
    STAGE();
    if (kvrole) {
      unsigned vo[4];
#pragma unroll
      for (int j = 0; j < 4; ++j) vo[j] = bx + ((cx ^ ((4 * L + j) & 7)) << 4) + j * (8 * 1280 - 1024);
      const size_t go = (size_t)(128 * kt + 32 * L) * 1280 + 128 * kc;
      dma4v((cptr)p.k + go, vo, L_P0 + L * 4096);
      dma4vm((cptr)p.v + go, vo, L_P0 + 16384 + L * 4096, L == 3 ? 0x00ffffffffffffffull : ~0ull);
    } else {
      unsigned vo[4];
#pragma unroll
      for (int j = 0; j < 4; ++j) vo[j] = (lane >> 4) * 1280 + ((cwz ^ j) << 4) + j * (4 * 1280 - 1024);
      const int wnc = wn < 90 ? wn : 0;
      cptr src = (cptr)p.w2 + (size_t)(96 * (wnc / 5)) * 1280 + 256 * (wnc % 5);
      const int g0 = L < 3 ? 2 * L : 4;
      dma4v(src + (size_t)g0 * (16 * 1280), vo, L_P0 + g0 * 4096);
      dma4v(src + (size_t)(g0 + 1) * (16 * 1280), vo, L_P0 + g0 * 4096 + 4096);
    }
    const unsigned myfull = W_FULL + 4 * L, mypl = W_PL + 4 * L;
    WAITV(16); pub(myfull, 1, lane);
    STAMP(1);
    STAGE();
    EPOCH_FIN();
    WAITV(16); pub(myfull, 2, lane);
    STAGE();
    const int srow = 128 * rt + 8 * (half * 8 + cti) + 2 * L;
    v4u xa, xc; unsigned xb, xd;
    asm volatile("global_load_dwordx4 %0, %4, %6\n\tglobal_load_dword %1, %5, %6 offset:1024\n\tglobal_load_dwordx4 %2, %4, %6 offset:1280\n\t"
                 "global_load_dword %3, %5, %6 offset:2304"
                 : "=&v"(xa), "=&v"(xb), "=&v"(xc), "=&v"(xd) : "v"(l16), "v"(lane * 4), "s"((cptr)p.x + (size_t)srow * 1280) : "memory");
    const int csj = (idx >> 3), csc = 3 * (xcd >> 1) + csj;
    const bool docs = csj < 3 && csc < 10;
#define RLOAD(R) do { cptr xs_ = xb0 + 128 * chunk; cptr ws_ = wb0 + (size_t)chunk * (64 * 3456); \
      _Pragma("unroll") for (int j = 0; j < 4; ++j) R[j] = *(const GAS v4u*)(xs_ + vx[j] + j * 1024); \
      _Pragma("unroll") for (int j = 0; j < 4; ++j) R[4 + j] = *(const GAS v4u*)(ws_ + vw[j] + j * 1024); \
      chunk = chunk == 9 ? 0 : chunk + 1; SB0(); } while (0)
#define RWRITE(R) do { const unsigned a_ = reg + l16; \
      _Pragma("unroll") for (int j = 0; j < 4; ++j) { sts128(a_ + j * 1024, R[j]); sts128(a_ + 16384 + j * 1024, R[4 + j]); } \
      reg ^= rx; rx ^= 98304; asm volatile("" ::: "memory"); } while (0)
    v4u RA[8], RB[8];
    WAITV(20); pub(mypl, docs ? 1 : 2, lane);
    RLOAD(RA);
    WAITV(20); pub(myfull, 3, lane);
    RLOAD(RB);
    WAITV(20); pub(myfull, 4, lane);
    STAMP(2);
    LATE_CONSTS();
    if (docs) {
      wait4(W_FULL, csj + 1);
      const int g = lane >> 4, q = (lane & 15) >> 2, pp = lane & 3;
      float cs0 = 0.f, cs1 = 0.f;
#pragma unroll
      for (int t = 0; t < 2; ++t) {
        const unsigned rowb = (csj == 2 ? 98304 : csj * 32768) + 16384 + (8 * g + 4 * t + q) * 256 + 8 * (pp & 1), swz = (q << 2) | ((2 * g + t) & 3);
        const unsigned a0 = rowb + (((4 * L + (pp >> 1)) ^ swz) << 4), a1 = rowb + (((4 * L + 2 + (pp >> 1)) ^ swz) << 4);
#pragma unroll
        for (int u = 0; u < 2; ++u) {
          v2u r0 = ldstr(a0 + 8192 * u), r1 = ldstr(a1 + 8192 * u);
          cs0 = dot2ones(r0.x, cs0); cs1 = dot2ones(r1.x, cs1); cs0 = dot2ones(r0.y, cs0); cs1 = dot2ones(r1.y, cs1);
        }
      }
      asm volatile("" ::: "memory");
      pub(mypl, 2, lane);
      cs0 += __shfl_xor(cs0, 16); cs1 += __shfl_xor(cs1, 16);
      cs0 += __shfl_xor(cs0, 32); cs1 += __shfl_xor(cs1, 32);
      if (lane < 32 && (isfull || L < 2))
        __builtin_amdgcn_raw_buffer_store_b64(v2u{__builtin_bit_cast(unsigned, lane < 16 ? cs0 : cs1), epoch}, rs, WS_CSP + (csc * 1792 + 64 * u0 + 32 * L + lane) * 8, 0, AUX_SC1);
      wait4(W_PL, 2);
    }
    STAMP(20); wait4(W_FREE, 1); STAMP(21); RWRITE(RA); pub(myfull, 5, lane); STAMP(22); RLOAD(RA); STAMP(23);
    wait4(W_FREE, 2); STAMP(24); RWRITE(RB); pub(myfull, 6, lane); STAMP(25); RLOAD(RB); STAMP(26);
    {
      asm volatile("" : "+v"(xa), "+v"(xb), "+v"(xc), "+v"(xd)::"memory");
      f32x2 a[5] = {bf2(xa.x), bf2(xa.y), bf2(xa.z), bf2(xa.w), bf2(xb)}, c[5] = {bf2(xc.x), bf2(xc.y), bf2(xc.z), bf2(xc.w), bf2(xd)};
      f32x2 sa = (a[0] + a[1]) + (a[2] + a[3]) + a[4], sc = (c[0] + c[1]) + (c[2] + c[3]) + c[4];
      const float ma = wave_sum(sa[0] + sa[1]) * (1.0f / D), mc = wave_sum(sc[0] + sc[1]) * (1.0f / D);
      f32x2 qa = {0.f, 0.f}, qc = {0.f, 0.f};
#pragma unroll
      for (int i = 0; i < 5; ++i) { f32x2 e = a[i] - ma; qa += e * e; f32x2 f = c[i] - mc; qc += f * f; }
      const float va = wave_sum(qa[0] + qa[1]) * (1.0f / D), vc = wave_sum(qc[0] + qc[1]) * (1.0f / D);
      const unsigned vb = pk_bf(bf2(pk_bf(va, vc)) + 1e-5f);
      const v4u o = {pk_bf(ma, __builtin_amdgcn_sqrtf(bf_lo(vb))), epoch, pk_bf(mc, __builtin_amdgcn_sqrtf(bf_hi(vb))), epoch};
      if (lane < 2) __builtin_amdgcn_raw_buffer_store_b64(lane ? v2u{o.z, o.w} : v2u{o.x, o.y}, rs, WS_STATS + (srow + lane) * 8, 0, AUX_SC1);
    }
    STAMP(27); wait4(W_FREE, 3); STAMP(28); RWRITE(RA); pub(myfull, 7, lane); STAMP(29); RLOAD(RA); STAMP(30);
    wait4(W_FREE, 4); STAMP(31); RWRITE(RB); pub(myfull, 8, lane); STAMP(32); RLOAD(RB); STAMP(33);
    wait4(W_FREE, 5); RWRITE(RA); pub(myfull, 9, lane);
    wait4(W_FREE, 6); RWRITE(RB); pub(myfull, 10, lane);
    STAMP(3);
    const int blk0 = (80 * c80) >> 7, blk1 = (80 * c80 + 79) >> 7;
    const unsigned fo = (L == 0 ? ((chb + 3) >> 6) * 16 + lane : 160 + blk0 * 32 + lane) * 4;
    const unsigned long long fm = L == 0 ? 0xffffull : (blk1 != blk0 ? 0x0003ffff0003ffffull : 0x3ffffull);
    if (L < 2) poll_atomic(p.ws + WS_FP0, fo, fm, epoch);
    pub(myfull, 11, lane);
    wait4(W_FULL, 11);
    STAMP(4);
    wait4(W_FREE, 8);
    if (isfull) W2GROUP(3); else { H_DMA(); R_DMA(); CONV_PADS(); }
    W2GROUP(2);
    wait4(W_FREE, 9); W2GROUP(0);
    wait4(W_FREE, 10); W2GROUP(1);
    if (isfull) {
      WAITV(0); pub(myfull, 24, lane);
      if (b == 0 && w == 3) {
#pragma unroll 1
        for (unsigned spins = 0; spins < SPIN_CAP; ++spins) {
          bool ok = true;
#pragma unroll 1
          for (int i = 0; i < 4; ++i) {
            const int bb = lane + 64 * i, x2 = bb & 7, i2 = bb >> 3, kn = x2 * 20 + i2, w2n = x2 * 12 + i2 - 20;
            const unsigned v = ld32a(p.ws + WS_FP0, (i2 < 20 ? (kn % 10) * 16 + kn / 10 : (w2n < 90 ? 160 + (w2n % 5) * 32 + w2n / 5 : 88 + w2n)) * 4);
            ok = wait_eq1<0>(v, epoch) && ok;
          }
          if (ok) break;
        }
        if (lane == 0) __builtin_amdgcn_raw_buffer_store_b32(epoch, rs, WS_EPOCH, 0, AUX_SC1);
      }
    } else { WAITV(0); pub(myfull, 23, lane); }
    STAMP(5);
  } else if (w < 8) {
    const int cw = w - 4;
    const int n = lane & 15, g = lane >> 4, q = (lane & 15) >> 2, pp = lane & 3;
    const int tok0 = isfull ? 64 * (cw & 1) : 32 * cw, fg = isfull ? cw >> 1 : 0;
    touch(kpc, min((unsigned)(cw * 64 + lane) * 128u, (unsigned)PF_CODE));
    touch(kpc, min((unsigned)(256 + cw * 64 + lane) * 128u, (unsigned)PF_CODE));
    if (cw == 0) touch(p.ws, min((unsigned)lane * 262144u, WS_END - 4));
    if (cw == 1) touch(p.out, min((unsigned)lane * 65536u, 2621440u - 4));
    if (cw == 2) touch(p.sk, min((unsigned)lane * 65536u, 2621440u - 4));
    BAR0();
    unsigned ax[2], aw[4][2];
#pragma unroll
    for (int u = 0; u < 2; ++u) ax[u] = (tok0 + n) * 128 + ((((4 * u + g) ^ n) & 7) << 4);
#pragma unroll
    for (int mt = 0; mt < 4; ++mt)
#pragma unroll
      for (int t = 0; t < 2; ++t)
        aw[mt][t] = 16384 + (8 * g + 4 * t + q) * 256 + (((2 * (4 * fg + mt) + (pp >> 1)) ^ ((q << 2) | ((2 * g + t) & 3))) << 4) + 8 * (pp & 1);
    f32x4 acc[4][4];
#pragma unroll
    for (int mt = 0; mt < 4; ++mt)
#pragma unroll
      for (int T = 0; T < 4; ++T) acc[mt][T] = f32x4{0.f, 0.f, 0.f, 0.f};
    v4u xf0[4], wf0[4], xf1[4], wf1[4];
#pragma unroll
    for (int i = 0; i < 4; ++i) { xf1[i] = v4u{0u, 0u, 0u, 0u}; wf1[i] = v4u{0u, 0u, 0u, 0u}; }
    EPOCH_FIN();
    LATE_CONSTS();
    const unsigned myfree = W_FREE + 4 * cw;
#define P1_READ(NT, XF, WF, U) do { \
      { v2u lo = ldstr(aw[0][0] + 8192 * U), hi = ldstr(aw[0][1] + 8192 * U); WF[0] = v4u{lo.x, lo.y, hi.x, hi.y}; } \
      _Pragma("unroll") for (int T = 0; T < NT; ++T) XF[T] = lds128(ax[U] + 2048 * T); \
      _Pragma("unroll") for (int mt = 1; mt < 4; ++mt) { v2u lo = ldstr(aw[mt][0] + 8192 * U), hi = ldstr(aw[mt][1] + 8192 * U); WF[mt] = v4u{lo.x, lo.y, hi.x, hi.y}; } \
    } while (0)
#define P1_MMA(NT, XF, WF) do { \
      _Pragma("unroll") for (int mt = 0; mt < 4; ++mt) _Pragma("unroll") for (int T = 0; T < NT; ++T) acc[mt][T] = mfma(WF[mt], XF[T], acc[mt][T]); \
    } while (0)
#define P1_ILV(NT) do { _Pragma("unroll") for (int i = 0; i < 4 * NT; ++i) { SGB(0x8, 1); SGB(0x100, NT == 4 ? 1 : 2); } SB0(); } while (0)
#define P1_LOOP(NT) do { \
      v4u fl = {0u, 0u, 0u, 0u}; \
      _Pragma("unroll 1") for (int s = 0; s < 10; ++s) { \
        if (!ge4(fl, s + 1)) wait4(W_FULL, s + 1); \
        asm volatile("" ::: "memory"); SB0(); \
        P1_READ(NT, xf0, wf0, 0); P1_MMA(NT, xf1, wf1); P1_ILV(NT); \
        fl = peek4(W_FULL); \
        P1_READ(NT, xf1, wf1, 1); P1_MMA(NT, xf0, wf0); P1_ILV(NT); \
        asm volatile("" ::: "memory"); \
        pub(myfree, s + 1, lane); \
        const unsigned dx = (s & 1) ? 65536u : 32768u; \
        ax[0] ^= dx; ax[1] ^= dx; \
        _Pragma("unroll") for (int mt = 0; mt < 4; ++mt) { aw[mt][0] ^= dx; aw[mt][1] ^= dx; } \
        STAMP(20 + s); \
      } \
      P1_MMA(NT, xf1, wf1); \
    } while (0)
    STAMP(1);
    __builtin_amdgcn_s_setprio(2);
    if (isfull) P1_LOOP(4); else P1_LOOP(2);
    STAMP(4);
    wait4(W_AP, 4);
    STAMP(5);
    f32x4 cs[4];
#pragma unroll
    for (int mt = 0; mt < 4; ++mt) cs[mt] = __builtin_bit_cast(f32x4, lds128(L_CS + (fg * 64 + mt * 16 + g * 4) * 4));
    const unsigned ho = WS_HF + ((8 * rt + (tok0 >> 4)) * 54 + 2 * (u0 + fg)) * 1024 + l16;
#pragma unroll
    for (int T = 0; T < 4; ++T) {
      if (T < 2 || isfull) {
        const f32x2 st = __builtin_bit_cast(f32x2, lds64(L_STATS + (tok0 + 16 * T + n) * 8));
#pragma unroll
        for (int pr = 0; pr < 2; ++pr) {
          unsigned o[4];
#pragma unroll
          for (int j = 0; j < 4; ++j) {
            const int mt = 2 * pr + (j >> 1), e = 2 * (j & 1);
            const f32x2 av = {acc[mt][T][e], acc[mt][T][e + 1]}, cv = {cs[mt][e], cs[mt][e + 1]};
            o[j] = pk_bf(silu_z(av * st[0] + cv * st[1]));
          }
          __builtin_amdgcn_raw_buffer_store_b128(v4u{o[0], o[1], o[2], o[3]}, rs, ho + T * (54 * 1024) + pr * 1024, 0, AUX_SC1);
          SB0();
        }
      }
    }
    STAMP(6);
    WAITV(0);
    if (lane < (isfull ? 2 : 1)) flag_set(p.ws + WS_FH, ((4 * rt + (tok0 >> 5) + lane) * 32 + u0 + fg) * 4, epoch);
    STAMP(7);
  } else {
    const int aw = w - 8;
    LATE_CONSTS();
    if (aw == 0 && lane < 24) {
      const bool cvon = lane < 12 || lane >= 20 || (isfull ? lane >= 16 : hn < 72);
      sts32(L_SYNC + lane * 4, cvon ? 0u : 99u);
    }
    const int tg8 = (lane & 7) + 8 * (aw & 1), cpl = (lane >> 3) + 8 * (aw >> 1);
    unsigned halo[2][3] = {{0u, 0u, 0u}, {0u, 0u, 0u}};
    v4u shk4[2] = {{0u, 0u, 0u, 0u}, {0u, 0u, 0u, 0u}};
    unsigned vlast[2] = {0u, 0u};
    if (kvrole) {
      if (tg8 == 15) {
#pragma unroll
        for (int it = 0; it < 2; ++it) vlast[it] = *(const GAS unsigned*)((cptr)p.v + (size_t)(128 * kt + 127) * 1280 + 128 * kc + 4 * cpl + 64 * it);
      }
#pragma unroll
      for (int it = 0; it < 2; ++it) shk4[it] = *(const GAS v4u*)((cptr)p.shk + (size_t)(64 * kc + 2 * cpl + 32 * it) * 8);
      if (tg8 == 0 && kt > 0) {
#pragma unroll
        for (int it = 0; it < 2; ++it)
#pragma unroll
          for (int i = 0; i < 3; ++i) halo[it][i] = *(const GAS unsigned*)((cptr)p.k + (size_t)(128 * kt - 3 + i) * 1280 + 128 * kc + 4 * cpl + 64 * it);
      }
    }
    BAR0();
    EPOCH_FIN();
    const unsigned myap = W_AP + 4 * aw;
    const unsigned ka = L_P0 + tg8 * 1024 + ((((cpl >> 2) ^ tg8) & 7) << 4) + (cpl & 3) * 4;
    const unsigned kp = tg8 ? L_P0 + (tg8 - 1) * 1024 + ((((cpl >> 2) ^ (tg8 - 1)) & 7) << 4) + (cpl & 3) * 4 : ka;
    __builtin_amdgcn_s_setprio(3);
    wait4(W_PL, 1);
    STAMP(1);
    if (kvrole) {
      unsigned kk[2][11], vv[2][8];
#pragma unroll
      for (int it = 0; it < 2; ++it) {
#pragma unroll
        for (int i = 0; i < 3; ++i) kk[it][i] = lds32((kp ^ (64 * it)) + (5 + i) * 128);
#pragma unroll
        for (int i = 0; i < 8; ++i) { kk[it][3 + i] = lds32((ka ^ (64 * it)) + i * 128); vv[it][i] = lds32((ka ^ (64 * it)) + 16384 + i * 128); }
      }
      asm volatile("" ::: "memory");
      pub(myap, 1, lane);
      const unsigned kvo = WS_KVR + (64 * kc + 2 * cpl) * KVR_ROW + (2040 - 128 * kt - 8 * tg8) * 2;
#pragma unroll
      for (int it = 0; it < 2; ++it) {
        const v4u sh = shk4[it];
        const f32x2 s0 = {bf_lo(sh.x), bf_lo(sh.z)}, s1 = {bf_hi(sh.x), bf_hi(sh.z)}, s2 = {bf_lo(sh.y), bf_lo(sh.w)}, s3 = {bf_hi(sh.y), bf_hi(sh.w)};
        f32x2 kf[11], o[8];
#pragma unroll
        for (int i = 0; i < 11; ++i) kf[i] = bf2(i < 3 ? (tg8 ? kk[it][i] : halo[it][i]) : kk[it][i]);
#pragma unroll
        for (int i = 0; i < 8; ++i) {
          f32x2 s = kf[i] * s3 + kf[i + 1] * s2 + kf[i + 2] * s1 + kf[i + 3] * s0;
          o[i] = bf2(pk_bf(s)) * bf2(i == 7 ? (tg8 == 15 ? vlast[it] : vv[it][i]) : vv[it][i]);
        }
        v4u ra = {pk_bf(o[7][0], o[6][0]), pk_bf(o[5][0], o[4][0]), pk_bf(o[3][0], o[2][0]), pk_bf(o[1][0], o[0][0])};
        v4u rb = {pk_bf(o[7][1], o[6][1]), pk_bf(o[5][1], o[4][1]), pk_bf(o[3][1], o[2][1]), pk_bf(o[1][1], o[0][1])};
        const unsigned o2 = kvo + it * (32 * KVR_ROW);
        __builtin_amdgcn_raw_buffer_store_b128(ra, rs, o2, 0, AUX_SC1);
        __builtin_amdgcn_raw_buffer_store_b128(rb, rs, o2 + KVR_ROW, 0, AUX_SC1);
        if (kt == 0 && tg8 == 0) {
          __builtin_amdgcn_raw_buffer_store_b128(v4u{0u, 0u, 0u, 0u}, rs, o2 + 16, 0, AUX_SC1);
          __builtin_amdgcn_raw_buffer_store_b128(v4u{0u, 0u, 0u, 0u}, rs, o2 + KVR_ROW + 16, 0, AUX_SC1);
        }
        SB0();
      }
    } else {
      const int g = lane >> 4, q = (lane & 15) >> 2, pp = lane & 3;
      v2u h2[2][3][2];
#pragma unroll
      for (int it = 0; it < 2; ++it)
#pragma unroll
        for (int u = 0; u < 3; ++u)
#pragma unroll
          for (int t = 0; t < 2; ++t)
            h2[it][u][t] = ldstr(L_P0 + (32 * u + 16 * t + 4 * g + q) * 256 + (((2 * (aw + 4 * it) + (pp >> 1)) ^ ((q << 2) | (g & 3))) << 4) + 8 * (pp & 1));
      asm volatile("" ::: "memory");
      pub(myap, 1, lane);
      if (wn < 90) {
#pragma unroll
        for (int it = 0; it < 2; ++it) {
          const int dt = 8 * (wn % 5) + aw + 4 * it;
#pragma unroll
          for (int u = 0; u < 3; ++u)
            __builtin_amdgcn_raw_buffer_store_b128(v4u{h2[it][u][0].x, h2[it][u][0].y, h2[it][u][1].x, h2[it][u][1].y}, rs,
                                                   WS_W2T + (((dt / 5) * 54 + 3 * (wn / 5) + u) * 5 + dt % 5) * 1024 + l16, 0, AUX_SC1);
        }
      }
    }
    __builtin_amdgcn_s_setprio(0);
    STAMP(2);
    WAITV(0); pub(myap, 2, lane);
    if (aw == 0) {
      wait4(W_AP, 2); wait4(W_FULL, 2); wait4(W_FREE, 1);
      if (lane == 0) flag_set(p.ws + WS_FP0, (kvrole ? kc * 16 + kt : (wn < 90 ? 160 + (wn % 5) * 32 + wn / 5 : 88 + wn)) * 4, epoch);
    }
    STAMP(3);
    wait4(W_AP, 1);
    H_DMA(); CONV_PADS();
    wait4(W_FREE, 7);
    v2u gr[10];
    const unsigned gb = aw < 2 ? WS_STATS + (128 * rt + 64 * aw) * 8 : WS_CSP + (64 * u0 + (isfull ? 64 * (aw - 2) : 0)) * 8;
#pragma unroll 1
    for (unsigned spins = 0; spins < SPIN_CAP; ++spins) {
      gr[0] = __builtin_amdgcn_raw_buffer_load_b64(rs, gb + lane * 8, 0, AUX_SC1);
      unsigned bad = 0;
      if (aw >= 2) {
#pragma unroll
        for (int i = 1; i < 10; ++i) gr[i] = __builtin_amdgcn_raw_buffer_load_b64(rs, gb + i * (1792 * 8) + lane * 8, 0, AUX_SC1);
        float c = __builtin_bit_cast(float, gr[0].x);
#pragma unroll
        for (int i = 1; i < 10; ++i) { bad |= gr[i].y ^ epoch; c += __builtin_bit_cast(float, gr[i].x); }
        gr[0].x = __builtin_bit_cast(unsigned, c);
      }
      bad |= gr[0].y ^ epoch;
      if (__all(bad == 0u)) break;
      __builtin_amdgcn_s_sleep(4);
    }
    wait4(W_FREE, 9);
    if (aw < 2) {
      const float ri = __builtin_amdgcn_rcpf(bf_hi(gr[0].x)) * LOG2E;
      sts64(L_STATS + (64 * aw + lane) * 8, __builtin_bit_cast(v2u, f32x2{-ri, bf_lo(gr[0].x) * ri}));
    } else sts32(L_CS + (64 * (aw - 2) + lane) * 4, gr[0].x);
    asm volatile("" ::: "memory");
    pub(myap, 4, lane);
    STAMP(4);
    wait4(W_FULL, 11);
    R_DMA();
    WAITV(0);
    STAMP(5);
  }
  LATE_CONSTS();
  STAMP(8);
  if (convw) {
    const int rh = w & 1;
    const unsigned cvp = W_CV + area * 8, mycv = cvp + rh * 4;
    pub(mycv, 1, lane);
    const int i16 = lane & 15, kg = lane >> 4;
    const unsigned rbase = cb + ((i16 & 1) ? CH_R1 : CH_R0);
    unsigned va = rbase + 4032 - 3584 - 64 * (i16 >> 1) + 16 * kg + 256 * rh;
    const unsigned vamax = rbase + 4096;
    const unsigned hbase = cb + ((i16 & 1) ? CH_H0 : CH_H1);
    unsigned vb = hbase + 4 * i16 - ((i16 & 1) ? 60 : 64) + 16 * kg + 256 * rh;
    const unsigned vbmin = hbase - 16;
    USE(va); USE(vb);
    f32x4 acc[8];
#pragma unroll
    for (int i = 0; i < 8; ++i) acc[i] = f32x4{0.f, 0.f, 0.f, 0.f};
    wait2(cvp, 1);
#define LDA_(i, o) lds128(va + 3584 - 512 * (i) + (o))
#define LDB_(dst, i, o) do { v2u lo = lds64v(vb + 512 * (i) + (o)), hi = lds64v(vb + 512 * (i) + (o) + 8); dst = v4u{lo.x, lo.y, hi.x, hi.y}; } while (0)
#define LDP(i, o) do { A[i] = LDA_(i, o); LDB_(B[i], i, o); } while (0)
#define STEP(m) do { _Pragma("unroll") for (int o = 0; o < (m); ++o) if ((m) + o <= 7) acc[(m) + o] = mfma(A[m], B[o], acc[(m) + o]); \
      if (2 * (m) <= 7) acc[2 * (m)] = mfma(A[m], B[m], acc[2 * (m)]); \
      _Pragma("unroll") for (int o = 0; o < (m); ++o) if ((m) + o <= 7) acc[(m) + o] = mfma(A[o], B[m], acc[(m) + o]); } while (0)
#define ILV(n) do { _Pragma("unroll") for (int i_ = 0; i_ < (n); ++i_) { SGB(0x8, 1); SGB(0x100, 1); } SGB(0x8, 16); SB0(); } while (0)
    v4u A[8], B[8];
    {
      A[0] = lds128(min(va + 3584, vamax));
      v2u lo = lds64v(max(vb, vbmin)), hi = lds64v(max(vb + 8, vbmin));
      B[0] = v4u{lo.x, lo.y, hi.x, hi.y};
      LDP(1, 0); LDP(2, 0); LDP(3, 0);
    }
    SB0();
#pragma unroll 1
    for (int r = 0; r < 4; ++r) {
      v4u nA0, nB0, nA1, nB1;
      LDP(4, 0); STEP(0); STEP(1); ILV(3);
      LDP(5, 0); STEP(2); ILV(3);
      LDP(6, 0); LDP(7, 0); STEP(3); ILV(6);
      nA0 = lds128(min(va + 3584 + 64, vamax)); LDB_(nB0, 0, 64); nA1 = LDA_(1, 64); LDB_(nB1, 1, 64); STEP(4); ILV(6);
      LDP(3, 64); STEP(5); ILV(3);
      LDP(2, 64); STEP(6); STEP(7); ILV(3);
      A[0] = nA0; B[0] = nB0; A[1] = nA1; B[1] = nB1;
      va += 64; vb += 64; USE(va); USE(vb);
    }
    const int chunk = 128 * rh + lane;
    const v4u kva = lds128(cb + CH_R1 + (255 - chunk) * 16), kvb = lds128(cb + CH_R1 + (191 - chunk) * 16);
    const float dD = bf_lo(*(kptr)((cptr)p.sD + (cch >> 1) * 4) >> ((cch & 1) * 16));
    STAMP(9);
    __builtin_amdgcn_s_setprio(3);
    asm volatile("" ::: "memory");
    pub(mycv, 2, lane); wait2(cvp, 2);
    {
      const unsigned a = cb + rh * 8192 + (64 * (lane >> 4) + 2 * (lane & 15)) * 4;
#pragma unroll
      for (int i = 0; i < 8; ++i) {
        sts64(a + 1024 * i, __builtin_bit_cast(v2u, f32x2{acc[i][0], acc[i][1]}));
        sts64(a + 1024 * i + 128, __builtin_bit_cast(v2u, f32x2{acc[i][2], acc[i][3]}));
      }
    }
    asm volatile("" ::: "memory");
    pub(mycv, 3, lane); wait2(cvp, 3);
    conv_final(rs, cb, cch, chunk, kva, dD);
    conv_final(rs, cb, cch, chunk + 64, kvb, dD);
    asm volatile("" ::: "memory");
    pub(mycv, 4, lane);
    __builtin_amdgcn_s_setprio(0);
  }
  STAMP(10);
#define Y_FLAG() do { if (convw && lane == 0) flag_set(p.ws + WS_FY, ((w & 1) * 640 + cch) * 4, epoch); } while (0)
  if (w < 4) {
    const unsigned myfull = W_FULL + 4 * w;
    v4u RG[4][8];
#pragma unroll
    for (int g = 0; g < 4; ++g)
#pragma unroll
      for (int j = 0; j < 8; ++j) RG[g][j] = __builtin_amdgcn_raw_buffer_load_b128(rs, (unsigned)(w2s - p.ws) + l16, (5 + g) * 15360 + j * 1024, AUX_SC1);
    SB0();
    wait4(W_CV, 4); wait4(W_CV + 16, 4);
    if (!isfull) W2GROUP(3);
    W2GROUP(4);
    WAITV(0); pub(myfull, 25, lane); Y_FLAG();
#pragma unroll
    for (int g = 0; g < 4; ++g) {
      wait4(W_FREE, 21 + g);
      unsigned a_ = w2d + wslot(5 + g) + l16; USE(a_);
#pragma unroll
      for (int j = 0; j < 8; ++j) sts128(a_ + j * 1024, RG[g][j]);
      asm volatile("" ::: "memory");
      pub(myfull, 26 + g, lane);
    }
  } else if (w < 8) {
    const int cw = w - 4, tp = cw & 1, kh = cw >> 1;
    const unsigned myfree = W_FREE + 4 * cw;
    const unsigned hoff = WS_HF + ((4 * tt64 + 2 * tp) * 54 + 27 * kh) * 1024 + l16;
    constexpr int HW = 6;
    v4u hf[27][2];
    f32x4 acc[2][5];
#pragma unroll
    for (int i = 0; i < 5; ++i) acc[0][i] = acc[1][i] = f32x4{0.f, 0.f, 0.f, 0.f};
    unsigned wa[3] = {kh * 15360 + l16, kh * 15360 + l16 + 65536, kh * 15360 + l16 + 131072};
    USE(wa[0]); USE(wa[1]); USE(wa[2]);
    poll_atomic(p.ws + WS_FH, ((2 * tt64 + tp) * 32 + 13 * kh + lane) * 4, 0x3fffull, epoch);
#define H_LD(i) do { hf[i][0] = __builtin_amdgcn_raw_buffer_load_b128(rs, hoff, (i) * 1024, AUX_SC1); \
      hf[i][1] = __builtin_amdgcn_raw_buffer_load_b128(rs, hoff, (i) * 1024 + 54 * 1024, AUX_SC1); } while (0)
#pragma unroll
    for (int i = 0; i < HW; ++i) H_LD(i);
    STAMP(11);
    v4u a0[5], a1[5];
    v4u fl;
#define P3_RD(A, ks) do { _Pragma("unroll") for (int i = 0; i < 5; ++i) A[i] = lds128(wa[wslot((ks) / 3) >> 16] + (wslot((ks) / 3) & 65535) + (((ks) % 3) * 5 + i) * 1024); \
      if ((ks) % 3 == 1 && (ks) > 6 && (ks) < 24) fl = peek4(W_FULL); } while (0)
#define P3_MM(A, ks) do { \
      _Pragma("unroll") for (int i = 0; i < 5; ++i) { acc[0][i] = mfma(A[i], hf[ks][0], acc[0][i]); acc[1][i] = mfma(A[i], hf[ks][1], acc[1][i]); } \
      if ((ks) + HW < 27) H_LD((ks) + HW); } while (0)
#define P3_ILV() do { _Pragma("unroll") for (int i = 0; i < 6; ++i) { SGB(0x8, 1); SGB(0x100, 1); } SGB(0x8, 4); SB0(); } while (0)
#define P3_GRP(g) do { asm volatile("" ::: "memory"); pub(myfree, 20 + (g), lane); if ((g) >= 3) { if (!ge4(fl, 21 + (g))) wait4(W_FULL, 21 + (g)); } asm volatile("" ::: "memory"); SB0(); } while (0)
    wait4(W_FULL, 23);
    P3_RD(a0, 0);
    SB0();
#pragma unroll
    for (int ks = 0; ks < 26; ks += 2) {
      if ((ks + 1) % 3 == 0) P3_GRP((ks + 1) / 3);
      P3_RD(a1, ks + 1); P3_MM(a0, ks); P3_ILV();
      if ((ks + 2) % 3 == 0) P3_GRP((ks + 2) / 3);
      P3_RD(a0, ks + 2); P3_MM(a1, ks + 1); P3_ILV();
      if (ks == 2) STAMP(12);
      if (ks == 12) STAMP(13);
    }
    P3_MM(a0, 26);
    STAMP(14);
    wait4(W_FREE, 27);
    {
      const unsigned oa = L_O + kh * (64 * OROW) + (32 * tp + (lane & 15)) * OROW + (lane >> 4) * 16;
#pragma unroll
      for (int i = 0; i < 5; ++i) { sts128(oa + i * 64, __builtin_bit_cast(v4u, acc[0][i])); sts128(oa + 16 * OROW + i * 64, __builtin_bit_cast(v4u, acc[1][i])); }
    }
    __builtin_amdgcn_s_setprio(0);
  } else {
    const int aw = w - 8, at = tid - 512;
    WAITV(0); Y_FLAG();
    v4u fx[3]; v2u fs[3];
#pragma unroll
    for (int i = 0; i < 3; ++i) {
      int it = at + 256 * i; if (it >= 640) it = 639;
      const int tok = it / 10, c8 = it % 10;
      fx[i] = *(const GAS v4u*)((cptr)p.x + (size_t)(64 * tt64 + tok) * 1280 + c80 * 160 + c8 * 16);
      fs[i] = __builtin_amdgcn_raw_buffer_load_b64(rs, WS_STATS + (64 * tt64 + tok) * 8, 0, AUX_SC1);
    }
    wait4(W_FREE, 23);
    poll_atomic(p.ws + WS_FY, ((tt64 >> 4) * 640 + c80 * 80 + aw * 20 + lane) * 4, 0xfffffull, epoch);
    v4u yv[3];
#pragma unroll
    for (int i = 0; i < 3; ++i) {
      int it = i * 64 + lane; if (it >= 160) it = 159;
      yv[i] = __builtin_amdgcn_raw_buffer_load_b128(rs, WS_YT + (c80 * 80 + aw * 20 + (it >> 3)) * 4096 + tt64 * 128 + (it & 7) * 16, 0, AUX_SC1);
    }
    STAMP(11);
    wait4(W_FREE, 25);
#pragma unroll
    for (int i = 0; i < 3; ++i) {
      int it = i * 64 + lane; if (it >= 160) it = 159;
      const unsigned a = L_Y + (it & 7) * (8 * 160 + 16) + (aw * 20 + (it >> 3)) * 2;
      sts16(a, yv[i].x); sts16(a + 160, yv[i].x >> 16); sts16(a + 320, yv[i].y); sts16(a + 480, yv[i].y >> 16);
      sts16(a + 640, yv[i].z); sts16(a + 800, yv[i].z >> 16); sts16(a + 960, yv[i].w); sts16(a + 1120, yv[i].w >> 16);
    }
    asm volatile("" ::: "memory");
    pub(W_AP + 4 * aw, 5, lane); wait4(W_AP, 5);
    STAMP(12);
#pragma unroll
    for (int i = 0; i < 3; ++i) {
      const int it = at + 256 * i;
      if (it < 640) {
        const int tok = it / 10, c8 = it % 10;
        const unsigned a = L_Y + tok * 160 + (tok >> 3) * 16 + c8 * 16;
        const v4u yv4 = lds128(a);
        const float m = bf_lo(fs[i].x), ri = __builtin_amdgcn_rcpf(bf_hi(fs[i].x));
        const unsigned xw[4] = {fx[i].x, fx[i].y, fx[i].z, fx[i].w}, yw[4] = {yv4.x, yv4.y, yv4.z, yv4.w};
        unsigned sw[4];
#pragma unroll
        for (int j = 0; j < 4; ++j) sw[j] = pk_bf(bf2(pk_bf(bf2(pk_bf(bf2(xw[j]) - m)) * ri)) * bf2(yw[j]));
        sts128(a, v4u{sw[0], sw[1], sw[2], sw[3]});
        sts128(L_X + it * 16, fx[i]);
      }
    }
  }
  STAMP(15);
  BAR0();
  STAMP(16);
  if (tid < 640) {
    const int ftok = tid / 10, fc8 = tid % 10;
    const unsigned oa = L_O + ftok * OROW + fc8 * 32;
    f32x4 o0 = __builtin_bit_cast(f32x4, lds128(oa)) + __builtin_bit_cast(f32x4, lds128(oa + 64 * OROW));
    f32x4 o1 = __builtin_bit_cast(f32x4, lds128(oa + 16)) + __builtin_bit_cast(f32x4, lds128(oa + 64 * OROW + 16));
    const v4u sv = lds128(L_Y + ftok * 160 + (ftok >> 3) * 16 + fc8 * 16), xv = lds128(L_X + tid * 16);
    f32x2 o[4] = {{o0[0], o0[1]}, {o0[2], o0[3]}, {o1[0], o1[1]}, {o1[2], o1[3]}};
    unsigned xw[4] = {xv.x, xv.y, xv.z, xv.w}, sw[4] = {sv.x, sv.y, sv.z, sv.w}, rw[4];
#pragma unroll
    for (int j = 0; j < 4; ++j) {
      const f32x2 gb = silu_z(o[j] * -LOG2E);
      rw[j] = pk_bf(bf2(xw[j]) + bf2(sw[j]) * gb);
    }
    *(GAS v4u*)((unsigned char*)p.out + (size_t)(64 * tt64 + ftok) * 1280 + c80 * 160 + fc8 * 16) = v4u{rw[0], rw[1], rw[2], rw[3]};
  }
}

extern "C" void kernel_launch(void* const* d_in, const int* in_sizes, int n_in, void* d_out, int out_size, void* d_ws, size_t ws_size,
                              hipStream_t stream) {
  Params p;
  p.x = (const u16*)d_in[0]; p.k = (const u16*)d_in[1]; p.v = (const u16*)d_in[2];
  p.shk = (const u16*)d_in[3]; p.sk = (const u16*)d_in[4]; p.sD = (const u16*)d_in[5];
  p.w1 = (const u16*)d_in[7]; p.w2 = (const u16*)d_in[8];
  p.out = (u16*)d_out; p.ws = (unsigned char*)d_ws;
  fused_kernel<<<NWG, NTHR, 0, stream>>>(p);
}
```

```cpp
#include <hip/hip_runtime.h>
#include <stdint.h>

typedef unsigned short u16;
typedef unsigned v4u __attribute__((ext_vector_type(4)));
typedef unsigned v2u __attribute__((ext_vector_type(2)));
typedef float f32x4 __attribute__((ext_vector_type(4)));
typedef float f32x2 __attribute__((ext_vector_type(2)));
typedef short bf16x8 __attribute__((ext_vector_type(8)));
typedef short v4s __attribute__((ext_vector_type(4)));
typedef __bf16 v2bf __attribute__((ext_vector_type(2)));
#define LAS __attribute__((address_space(3)))
#define GAS __attribute__((address_space(1)))
#define DEV __device__ __forceinline__
typedef const unsigned char* cptr;
typedef const __attribute__((address_space(4))) unsigned* kptr;

constexpr int S = 2048, D = 640, F = 1728;
constexpr int NWG = 256, NTHR = 768;
constexpr unsigned WS_EPOCH = 0;
constexpr unsigned WS_FP0 = 1024;
constexpr unsigned WS_FW = 3072;
constexpr unsigned WS_FH = 4096;
constexpr unsigned WS_FY = 16384;
constexpr unsigned WS_STATS = 32768;
constexpr unsigned KVR_ROW = 4224;
constexpr unsigned WS_KVR = 65536;
constexpr unsigned WS_W2T = WS_KVR + 640 * KVR_ROW;
constexpr unsigned WS_HF = WS_W2T + 8 * 54 * 5 * 1024;
constexpr unsigned WS_YT = WS_HF + 128 * 54 * 1024;
constexpr unsigned WS_CSP = WS_YT + 640 * 4096;
constexpr unsigned WS_MBKV = WS_CSP + 147456;
constexpr unsigned WS_W2R = WS_MBKV + 40960;
constexpr unsigned WS_END = WS_W2R + 4096;
constexpr unsigned WS_DBG = WS_YT + 640 * 4096 + 262144;
constexpr unsigned L_E = 131072;
constexpr unsigned L_STATS = 65536 + 30720;
constexpr unsigned L_CS = 98304 + 30720;
constexpr unsigned CH_R1 = 0, CH_Z1 = 4096, CH_R0 = 4128, CH_Z0 = 8224, CH_H0 = 8240, CH_PF = 12416, CH_H1 = 12432, CH_BYTES = 16528;
constexpr unsigned L_SYNC = 163840 - 128;
constexpr unsigned W_FULL = L_SYNC, W_FREE = L_SYNC + 16, W_AP = L_SYNC + 32, W_CV = L_SYNC + 48, W_PL = L_SYNC + 80, W_MK = L_SYNC + 96;
constexpr unsigned L_AREA0 = L_SYNC - 4 * CH_BYTES;
constexpr unsigned L_P0 = L_E;
constexpr unsigned L_Y = L_E;
constexpr unsigned L_X = L_E + 10240;
constexpr unsigned L_XN = L_E + 20480;
constexpr unsigned L_O = 32768;
constexpr unsigned OROW = 336;
constexpr unsigned SPIN_CAP = 1u << 15;
constexpr int AUX_SC1 = 16;
constexpr unsigned wslot(int g) { return (g & 3) * 32768u; }

DEV float asf(unsigned u) { return __builtin_bit_cast(float, u); }
DEV unsigned asu(float f) { return __builtin_bit_cast(unsigned, f); }
DEV float bf_lo(unsigned u) { return __builtin_bit_cast(float, u << 16); }
DEV float bf_hi(unsigned u) { return __builtin_bit_cast(float, u & 0xffff0000u); }
DEV f32x2 bf2(unsigned u) { return f32x2{bf_lo(u), bf_hi(u)}; }
DEV unsigned pk_bf(float a, float b) { v2bf r = {(__bf16)a, (__bf16)b}; return __builtin_bit_cast(unsigned, r); }
DEV unsigned pk_bf(f32x2 a) { return pk_bf(a[0], a[1]); }
DEV float rbf(float a) { return bf_lo(pk_bf(a, a) & 0xffffu); }
DEV f32x4 mfma(v4u a, v4u b, f32x4 c) {
  return __builtin_amdgcn_mfma_f32_16x16x32_bf16(__builtin_bit_cast(bf16x8, a), __builtin_bit_cast(bf16x8, b), c, 0, 0, 0);
}
DEV void mfma_ip(f32x4& c, v4u a, v4u b) { asm volatile("v_mfma_f32_16x16x32_bf16 %0, %1, %2, %0" : "+v"(c) : "v"(a), "v"(b)); }
DEV v4u lds128v(unsigned a) { return *(const volatile LAS v4u*)(size_t)a; }
constexpr float LOG2E = 1.44269504088896f;
DEV f32x2 silu_z(f32x2 z) {
  f32x2 e = {__builtin_amdgcn_exp2f(z[0]), __builtin_amdgcn_exp2f(z[1])};
  f32x2 d = e * -LOG2E - LOG2E;
  f32x2 r = {__builtin_amdgcn_rcpf(d[0]), __builtin_amdgcn_rcpf(d[1])};
  return z * r;
}
DEV float dot2ones(unsigned u, float acc) {
  return __builtin_amdgcn_fdot2_f32_bf16(__builtin_bit_cast(v2bf, u), __builtin_bit_cast(v2bf, 0x3f803f80u), acc, false);
}

DEV void dma16(const void* sbase, unsigned voff, unsigned ldsoff) {
  asm volatile("s_mov_b32 m0, %2\n\ts_nop 0\n\tglobal_load_lds_dwordx4 %0, %1" ::"v"(voff), "s"(sbase), "s"(ldsoff) : "memory");
}
DEV void dma16s(const void* sbase, unsigned voff, unsigned ldsoff) {
  asm volatile("s_mov_b32 m0, %2\n\ts_nop 0\n\tglobal_load_lds_dwordx4 %0, %1 sc1" ::"v"(voff), "s"(sbase), "s"(ldsoff) : "memory");
}
DEV void dma4k(const void* sbase, unsigned voff, unsigned ldsoff) {
  asm volatile("s_mov_b32 m0, %2\n\ts_nop 0\n\tglobal_load_lds_dwordx4 %0, %1\n\tglobal_load_lds_dwordx4 %0, %1 offset:1024\n\t"
               "global_load_lds_dwordx4 %0, %1 offset:2048\n\tglobal_load_lds_dwordx4 %0, %1 offset:3072" ::"v"(voff), "s"(sbase), "s"(ldsoff) : "memory");
}
DEV void dma4ks(const void* sbase, unsigned voff, unsigned ldsoff) {
  asm volatile("s_mov_b32 m0, %2\n\ts_nop 0\n\tglobal_load_lds_dwordx4 %0, %1 sc1\n\tglobal_load_lds_dwordx4 %0, %1 offset:1024 sc1\n\t"
               "global_load_lds_dwordx4 %0, %1 offset:2048 sc1\n\tglobal_load_lds_dwordx4 %0, %1 offset:3072 sc1" ::"v"(voff), "s"(sbase), "s"(ldsoff) : "memory");
}
DEV void dma4v(const void* sbase, const unsigned (&vo)[4], unsigned ldsoff) {
  asm volatile("s_mov_b32 m0, %5\n\ts_nop 0\n\tglobal_load_lds_dwordx4 %0, %4\n\tglobal_load_lds_dwordx4 %1, %4 offset:1024\n\t"
               "global_load_lds_dwordx4 %2, %4 offset:2048\n\tglobal_load_lds_dwordx4 %3, %4 offset:3072"
               ::"v"(vo[0]), "v"(vo[1]), "v"(vo[2]), "v"(vo[3]), "s"(sbase), "s"(ldsoff) : "memory");
}
DEV void dma4vm(const void* sbase, const unsigned (&vo)[4], unsigned ldsoff, unsigned long long lastmask) {
  asm volatile("s_mov_b32 m0, %5\n\ts_nop 0\n\tglobal_load_lds_dwordx4 %0, %4\n\tglobal_load_lds_dwordx4 %1, %4 offset:1024\n\t"
               "global_load_lds_dwordx4 %2, %4 offset:2048\n\ts_mov_b64 exec, %6\n\tglobal_load_lds_dwordx4 %3, %4 offset:3072\n\ts_mov_b64 exec, -1"
               ::"v"(vo[0]), "v"(vo[1]), "v"(vo[2]), "v"(vo[3]), "s"(sbase), "s"(ldsoff), "s"(lastmask) : "memory");
}
DEV void touch(const void* sbase, unsigned voff) {
  asm volatile("s_mov_b32 m0, 0x28000\n\ts_nop 0\n\tglobal_load_lds_dword %0, %1" ::"v"(voff), "s"(sbase) : "memory");
}
DEV v4u ld128a(const void* sbase, unsigned voff) { v4u r; asm volatile("global_load_dwordx4 %0, %1, %2 sc1" : "=v"(r) : "v"(voff), "s"(sbase) : "memory"); return r; }
DEV v2u ld64a(const void* sbase, unsigned voff) { v2u r; asm volatile("global_load_dwordx2 %0, %1, %2 sc1" : "=v"(r) : "v"(voff), "s"(sbase) : "memory"); return r; }
DEV unsigned atom_rd(const void* sbase, unsigned voff, unsigned long long mask, unsigned dflt) {
  unsigned r; unsigned long long sv;
  asm volatile("v_mov_b32 %0, %6\n\ts_mov_b64 %1, exec\n\ts_mov_b64 exec, %5\n\tglobal_atomic_or %0, %2, %3, %4 sc0 sc1\n\ts_mov_b64 exec, %1"
               : "=&v"(r), "=&s"(sv) : "v"(voff), "v"(0u), "s"(sbase), "s"(mask), "s"(dflt) : "memory");
  return r;
}
DEV unsigned ld32a(const void* sbase, unsigned voff) { unsigned r; asm volatile("global_load_dword %0, %1, %2 sc1" : "=v"(r) : "v"(voff), "s"(sbase) : "memory"); return r; }
#define WAITV(n) asm volatile("s_waitcnt vmcnt(" #n ")" ::: "memory")
#define USE(x) asm volatile("" : "+v"(x))
#define BAR0() do { asm volatile("s_waitcnt lgkmcnt(0)" ::: "memory"); __builtin_amdgcn_s_barrier(); asm volatile("" ::: "memory"); } while (0)
#define SGB(m, n) __builtin_amdgcn_sched_group_barrier(m, n, 0)
#define SB0() __builtin_amdgcn_sched_barrier(0)

DEV v4u lds128(unsigned a) { return *(const LAS v4u*)(size_t)a; }
DEV v2u lds64(unsigned a) { return *(const LAS v2u*)(size_t)a; }
DEV v2u lds64v(unsigned a) { return *(const volatile LAS v2u*)(size_t)a; }
DEV unsigned lds32(unsigned a) { return *(const LAS unsigned*)(size_t)a; }
DEV void sts128(unsigned a, v4u v) { *(LAS v4u*)(size_t)a = v; }
DEV void sts64(unsigned a, v2u v) { *(LAS v2u*)(size_t)a = v; }
DEV void sts32(unsigned a, unsigned v) { *(LAS unsigned*)(size_t)a = v; }
DEV void sts16(unsigned a, unsigned v) { *(LAS u16*)(size_t)a = (u16)v; }
DEV v2u ldstr(unsigned a) { return __builtin_bit_cast(v2u, __builtin_amdgcn_ds_read_tr16_b64_v4i16((LAS v4s*)(size_t)a)); }

#define SLP 2
DEV void pub(unsigned a, unsigned v, int lane) { if (lane == 0) *(volatile LAS unsigned*)(size_t)a = v; }
DEV unsigned pub_addr(unsigned a, int lane) { unsigned r = lane == 0 ? a : 0x30000u + ((a + lane * 4) & 255); asm volatile("" : "+v"(r)); return r; }
DEV void pubv(unsigned va, unsigned v) { *(volatile LAS unsigned*)(size_t)va = v; }
DEV unsigned min4u(v4u v) { return (unsigned)__builtin_amdgcn_readfirstlane(min(min(v.x, v.y), min(v.z, v.w))); }
DEV v4u peek4(unsigned a) { return *(const volatile LAS v4u*)(size_t)a; }
DEV bool ge4(v4u v, unsigned t) { return (unsigned)__builtin_amdgcn_readfirstlane(min(min(v.x, v.y), min(v.z, v.w))) >= t; }
DEV void wait4(unsigned a, unsigned t) {
#pragma unroll 1
  for (unsigned i = 0; i < (1u << 22); ++i) { if (ge4(peek4(a), t)) break; __builtin_amdgcn_s_sleep(SLP); }
  asm volatile("" ::: "memory");
}
DEV void wait2(unsigned a, unsigned t) {
#pragma unroll 1
  for (unsigned i = 0; i < (1u << 22); ++i) {
    v2u v = *(const volatile LAS v2u*)(size_t)a;
    if ((unsigned)__builtin_amdgcn_readfirstlane(min(v.x, v.y)) >= t) break;
    __builtin_amdgcn_s_sleep(1);
  }
  asm volatile("" ::: "memory");
}
DEV void wait1(unsigned a, unsigned t) {
#pragma unroll 1
  for (unsigned i = 0; i < (1u << 22); ++i) {
    if ((unsigned)__builtin_amdgcn_readfirstlane(*(const volatile LAS unsigned*)(size_t)a) >= t) break;
    __builtin_amdgcn_s_sleep(1);
  }
  asm volatile("" ::: "memory");
}
DEV float xsum16_32(float v) {
  float a = v, c = v;
  asm volatile("s_nop 1\n\tv_permlane16_swap_b32 %0, %1\n\ts_nop 0" : "+v"(a), "+v"(c));
  a += c; c = a;
  asm volatile("s_nop 1\n\tv_permlane32_swap_b32 %0, %1\n\ts_nop 0" : "+v"(a), "+v"(c));
  return a + c;
}
#define WAITL() asm volatile("s_waitcnt lgkmcnt(0)" ::: "memory")
template <int CTRL, int RM> DEV float dpp_add(float v) {
  return v + __builtin_bit_cast(float, __builtin_amdgcn_update_dpp(0, __builtin_bit_cast(int, v), CTRL, RM, 0xf, false));
}
DEV float wave_sum(float v) {
  v = dpp_add<0x111, 0xf>(v); v = dpp_add<0x112, 0xf>(v); v = dpp_add<0x114, 0xf>(v); v = dpp_add<0x118, 0xf>(v);
  v = dpp_add<0x142, 0xa>(v); v = dpp_add<0x143, 0xc>(v);
  return __builtin_bit_cast(float, __builtin_amdgcn_readlane(__builtin_bit_cast(int, v), 63));
}

#define BAR() BAR0()
#define STAMP(i)

#define ROTC 0
#define LPRIO 1
#define HWIN 8
#define APRIO 3
struct Params {
  const u16 *x, *k, *v, *shk, *sk, *sD, *w1, *w2;
  u16* out;
  unsigned char* ws;
};

template <int N> DEV bool wait_eq1(unsigned v, unsigned epoch) {
  unsigned d;
  asm volatile("s_waitcnt vmcnt(%3)\n\tv_xor_b32 %0, %2, %1" : "=v"(d) : "v"(v), "s"(epoch), "i"(N) : "memory");
  return __all(d == 0u);
}
template <int N> DEV bool wait_eq4(v4u v, unsigned epoch) {
  unsigned d, e;
  asm volatile("s_waitcnt vmcnt(%7)\n\tv_xor_b32 %0, %6, %2\n\tv_xor_b32 %1, %6, %3\n\tv_or_b32 %0, %0, %1\n\tv_xor_b32 %1, %6, %4\n\t"
               "v_or_b32 %0, %0, %1\n\tv_xor_b32 %1, %6, %5\n\tv_or_b32 %0, %0, %1"
               : "=&v"(d), "=&v"(e) : "v"(v.x), "v"(v.y), "v"(v.z), "v"(v.w), "s"(epoch), "i"(N) : "memory");
  return __all(d == 0u);
}
DEV void poll1(cptr base, unsigned off, unsigned epoch) {
#pragma unroll 1
  for (unsigned spins = 0; spins < SPIN_CAP; ++spins) {
    unsigned v = ld32a(base, off);
    if (wait_eq1<0>(v, epoch)) break;
    __builtin_amdgcn_s_sleep(6);
  }
}
DEV void poll4(cptr base, unsigned off, unsigned epoch) {
#pragma unroll 1
  for (unsigned spins = 0; spins < SPIN_CAP; ++spins) {
    v4u v = ld128a(base, off);
    if (wait_eq4<0>(v, epoch)) break;
    __builtin_amdgcn_s_sleep(6);
  }
}
DEV void flag_set(const void* sbase, unsigned voff, unsigned epoch) {
  asm volatile("global_atomic_swap %0, %1, %2 sc1" ::"v"(voff), "v"(epoch), "s"(sbase) : "memory");
}
DEV void poll_atomic(cptr base, unsigned off, unsigned long long mask, unsigned epoch) {
#pragma unroll 1
  for (unsigned spins = 0; spins < SPIN_CAP; ++spins) {
    const unsigned v = atom_rd(base, off, mask, epoch);
    if (wait_eq1<0>(v, epoch)) break;
    __builtin_amdgcn_s_sleep(2);
  }
}
template <int N> DEV unsigned epoch_fin(unsigned ev) {
  unsigned e;
  asm volatile("s_waitcnt vmcnt(%2)\n\tv_readfirstlane_b32 %0, %1\n\ts_nop 4" : "=s"(e) : "v"(ev), "i"(N) : "memory");
  return e * 2654435761u + 0x9E3779B9u;
}

DEV void conv_final(__amdgpu_buffer_rsrc_t rs, unsigned db, int ch, int chunk, v4u kvr, float dD) {
  const unsigned a = db + chunk * 32;
  f32x4 p0 = __builtin_bit_cast(f32x4, lds128(a)), p1 = __builtin_bit_cast(f32x4, lds128(a + 16));
  f32x4 q0 = __builtin_bit_cast(f32x4, lds128(a + 8192)), q1 = __builtin_bit_cast(f32x4, lds128(a + 8192 + 16));
  p0 += q0; p1 += q1;
  p0 += f32x4{bf_hi(kvr.w), bf_lo(kvr.w), bf_hi(kvr.z), bf_lo(kvr.z)} * dD;
  p1 += f32x4{bf_hi(kvr.y), bf_lo(kvr.y), bf_hi(kvr.x), bf_lo(kvr.x)} * dD;
  const v4u o = {pk_bf(p0[0], p0[1]), pk_bf(p0[2], p0[3]), pk_bf(p1[0], p1[1]), pk_bf(p1[2], p1[3])};
  __builtin_amdgcn_raw_buffer_store_b128(o, rs, WS_YT + ch * 4096 + chunk * 16, 0, AUX_SC1);
}

__global__ void __launch_bounds__(NTHR, 3) fused_kernel(Params p) {
  __shared__ __attribute__((aligned(16))) unsigned char smem[163840];
  const int tid = threadIdx.x, lane = tid & 63;
  const int w = __builtin_amdgcn_readfirstlane(tid >> 6);
  const int b = blockIdx.x, xcd = b & 7, idx = b >> 3;
  const int half = xcd & 1, cti = idx & 7;
  const int rt = 4 * (xcd >> 1) + (idx >> 3);
  const int nfull = 6 - half;
  const bool isfull = cti < nfull;
  const int u0 = 14 * half + (isfull ? 2 * cti : nfull + cti);
  const unsigned l16 = lane * 16;
  { unsigned lb = (unsigned)(size_t)(LAS unsigned char*)smem; asm volatile("" ::"s"(lb)); }
  unsigned stv = 0; (void)stv;
  STAMP(0);
  unsigned epoch, epraw;
  asm volatile("s_load_dword %0, %1, 0x0" : "=s"(epraw) : "s"(p.ws + WS_EPOCH) : "memory");
#define EPOCH_FIN() asm volatile("s_waitcnt lgkmcnt(0)\n\ts_mul_i32 %0, %1, 0x9E3779B1\n\ts_add_u32 %0, %0, 0x9E3779B9" : "=s"(epoch) : "s"(epraw) : "memory", "scc")
  const bool kvrole = idx < 20;
  const int kvn = xcd * 20 + idx, kt = kvn / 10, kc = kvn % 10;
  const int wn = xcd * 12 + idx - 20;
  const __amdgpu_buffer_rsrc_t rs = __builtin_amdgcn_make_buffer_rsrc((void*)p.ws, 0, (int)WS_END, 0x00020000);
  const unsigned lcs = isfull ? L_CS : L_STATS + 1024;
#define LATE_CONSTS() \
  int rt_ = rt; asm volatile("" : "+s"(rt_)); \
  const int tt64 = 2 * rt_ + (cti >> 2), c80 = 4 * half + (cti & 3); \
  const int hn = rt_ * 5 + half * 2 + cti - nfull; \
  const int area = w >= 8 ? 2 + ((w >> 1) & 1) : (w >> 1) & 1; \
  const bool convw = isfull ? w >= 8 : (hn < 72 && w >= 4); \
  const int chb = isfull ? 2 * (rt_ * 11 + half * 6 + cti) - 2 : (hn < 72 ? 352 + 4 * hn : 0); \
  const int cch = (isfull && area < 2) ? chb + 2 : chb + area; \
  const unsigned cb = L_AREA0 + area * CH_BYTES; \
  cptr w2s = p.ws + WS_W2T + c80 * (54 * 5 * 1024) + ((w >> 1) & 1) * (27 * 5 * 1024) + (w & 1) * 7168; \
  const unsigned w2d = ((w >> 1) & 1) * 15360 + (w & 1) * 7168; \
  (void)tt64; (void)convw; (void)cch; (void)cb; (void)w2s; (void)w2d; (void)chb; (void)hn;
#define H_DMA() dma4k((cptr)p.sk + (size_t)cch * 4096 + (w & 1) * 4, l16, cb + ((w & 1) ? CH_H1 : CH_H0))
#define R_DMA() dma4ks(p.ws + WS_KVR + (size_t)cch * KVR_ROW + (w & 1) * 2, l16, cb + ((w & 1) ? CH_R0 : CH_R1))
#define CONV_PADS() do { const unsigned h01 = *(kptr)((cptr)p.sk + (size_t)cch * 4096); \
    if (!(w & 1) && lane < 3) sts128(cb + (lane == 0 ? CH_Z1 : lane == 1 ? CH_Z0 : CH_PF), v4u{0u, 0u, 0u, lane == 2 ? h01 : 0u}); } while (0)
#define W2GROUP(g) do { WAITV(8); dma4ks(w2s + (g) * 15360, l16, w2d + wslot(g)); WAITV(8); dma4ks(w2s + (g) * 15360 + 4096, l16, w2d + wslot(g) + 4096); } while (0)
#define FENCE() asm volatile("" ::: "memory")
#define PRIO(x) __builtin_amdgcn_s_setprio(x)

  v2u stq = {0u, 0u};
  if (w < 4) {
    const int L = w;
    PRIO(LPRIO);
    const unsigned bx = (lane >> 3) * 1280, cx = lane & 7;
    const unsigned cwz = (lane & 15) ^ ((lane >> 4) << 2);
    const int cmask = isfull ? 15 : 7;
    unsigned vx[4], vw[4];
#pragma unroll
    for (int j = 0; j < 4; ++j) {
      vx[j] = bx + ((cx ^ (lane >> 3)) << 4) + j * (8 * 1280 - 1024);
      vw[j] = (lane >> 4) * 3456 + (((cwz ^ j) & cmask) << 4) + j * (4 * 3456 - 1024);
    }
    int chunk = (rt + ROTC * cti) % 10;
    cptr xb0 = (cptr)p.x + (size_t)(128 * rt + 32 * L) * 1280;
    cptr wb0 = (cptr)p.w1 + (size_t)(16 * L) * 3456 + 128 * u0;
    unsigned reg = L * 4096, rx = 32768;
#define STAGE_A() dma4v(xb0 + 128 * chunk, vx, reg)
#define STAGE_B() do { dma4v(wb0 + (size_t)chunk * (64 * 3456), vw, reg + 16384); chunk = chunk == 9 ? 0 : chunk + 1; reg ^= rx; rx ^= 98304; } while (0)
    unsigned vo[4];
    const unsigned myfull = W_FULL + 4 * L, vfull = pub_addr(myfull, lane);
    if (kvrole) {
      STAGE_A(); STAGE_B();
      BAR0();
#pragma unroll
      for (int j = 0; j < 4; ++j) vo[j] = bx + ((cx ^ ((4 * L + j) & 7)) << 4) + j * (8 * 1280 - 1024);
      const size_t go = (size_t)(128 * kt + 32 * L) * 1280 + 128 * kc;
      STAGE_A();
      WAITV(4); pubv(vfull, 1);
      STAGE_B();
      EPOCH_FIN();
      WAITV(4); dma4v((cptr)p.k + go, vo, L_P0 + L * 4096);
      WAITV(4); pubv(vfull, 2);
      dma4vm((cptr)p.v + go, vo, L_P0 + 16384 + L * 4096, L == 3 ? 0x00ffffffffffffffull : ~0ull);
      WAITV(4); STAGE_A();
      WAITV(4); pub(W_PL + 4 * L, 1, lane);
    } else {
#pragma unroll
      for (int j = 0; j < 4; ++j) vo[j] = (lane >> 4) * 1280 + ((cwz ^ j) << 4) + j * (4 * 1280 - 1024);
      const int wnc = wn < 90 ? wn : 0;
      const int g0 = L < 3 ? 2 * L : 4;
      cptr p0a = (cptr)p.w2 + (size_t)(96 * (wnc / 5) + 16 * g0) * 1280 + 256 * (wnc % 5);
      dma4v(p0a, vo, L_P0 + g0 * 4096); dma4v(p0a + 16 * 1280, vo, L_P0 + g0 * 4096 + 4096);
      BAR0();
      STAGE_A();
      EPOCH_FIN();
      WAITV(4); pub(W_PL + 4 * L, 1, lane);
      STAGE_B();
      WAITV(4); STAGE_A();
      WAITV(4); pubv(vfull, 1);
      STAGE_B();
      WAITV(4); STAGE_A();
      WAITV(4); pubv(vfull, 2);
    }
    chunk = __builtin_amdgcn_readfirstlane(chunk); reg = __builtin_amdgcn_readfirstlane(reg); rx = __builtin_amdgcn_readfirstlane(rx);
    STAMP(1);
    STAGE_B();
    WAITV(4); STAGE_A();
    WAITV(4); pubv(vfull, 3);
    STAGE_B();
    STAMP(2);
    if (rt < 10) wait4(W_AP, 1);
    {
      unsigned frm = min4u(peek4(W_FREE));
#pragma unroll 1
      for (int s = 1; s <= 6; ++s) {
        if (frm < (unsigned)s) { WAITV(0); pubv(vfull, s + 3); wait4(W_FREE, s); }
        WAITV(4); STAGE_A();
        WAITV(4); pubv(vfull, s + 3);
        STAGE_B();
        frm = min4u(peek4(W_FREE));
      }
    }
    WAITV(0); pubv(vfull, 10);
    STAMP(3);
    STAMP(5);
  } else if (w < 8) {
    const int cw = w - 4;
    const int n = lane & 15, g = lane >> 4, q = (lane & 15) >> 2, pp = lane & 3;
    const int tok0 = isfull ? 64 * (cw & 1) : 32 * cw, fg = isfull ? cw >> 1 : 0;
    BAR0();
    unsigned ax[2], aw[4][2];
#pragma unroll
    for (int u = 0; u < 2; ++u) ax[u] = (tok0 + n) * 128 + ((((4 * u + g) ^ n) & 7) << 4);
#pragma unroll
    for (int mt = 0; mt < 4; ++mt)
#pragma unroll
      for (int t = 0; t < 2; ++t)
        aw[mt][t] = 16384 + (8 * g + 4 * t + q) * 256 + (((2 * (4 * fg + mt) + (pp >> 1)) ^ ((q << 2) | ((2 * g + t) & 3))) << 4) + 8 * (pp & 1);
    f32x4 acc[4][4];
#pragma unroll
    for (int mt = 0; mt < 4; ++mt)
#pragma unroll
      for (int T = 0; T < 4; ++T) acc[mt][T] = f32x4{0.f, 0.f, 0.f, 0.f};
    v4u xf0[4], wf0[4], xf1[4], wf1[4];
    EPOCH_FIN();
    const unsigned vfree = pub_addr(W_FREE + 4 * cw, lane);
#define P1_READ(NT, XF, WF, U) do { \
      { v2u lo = ldstr(aw[0][0] + 8192 * U), hi = ldstr(aw[0][1] + 8192 * U); WF[0] = v4u{lo.x, lo.y, hi.x, hi.y}; } \
      _Pragma("unroll") for (int T = 0; T < NT; ++T) XF[T] = lds128(ax[U] + 2048 * T); \
      _Pragma("unroll") for (int mt = 1; mt < 4; ++mt) { v2u lo = ldstr(aw[mt][0] + 8192 * U), hi = ldstr(aw[mt][1] + 8192 * U); WF[mt] = v4u{lo.x, lo.y, hi.x, hi.y}; } \
    } while (0)
#define P1_MMA(NT, XF, WF) do { \
      _Pragma("unroll") for (int mt = 0; mt < 4; ++mt) _Pragma("unroll") for (int T = 0; T < NT; ++T) acc[mt][T] = mfma(WF[mt], XF[T], acc[mt][T]); \
    } while (0)
#define P1_ILV(NT) do { _Pragma("unroll") for (int i = 0; i < 4 * NT; ++i) { SGB(0x8, 1); SGB(0x100, NT == 4 ? 1 : 2); } SB0(); } while (0)
#define P1_ILV2(NT) do { _Pragma("unroll") for (int i = 0; i < 4 * NT; ++i) { SGB(0x8, 1); SGB(0x180, NT == 4 ? 1 : 2); SGB(0x2, NT == 4 ? 1 : 2); } SB0(); } while (0)
#define P1_LOOP(NT) do { \
      wait4(W_FULL, 1); \
      P1_READ(NT, xf0, wf0, 0); SB0(); \
      _Pragma("unroll 1") for (int s = 0; s < 9; ++s) { \
        const v4u fl = peek4(W_FULL); \
        P1_READ(NT, xf1, wf1, 1); \
        FENCE(); \
        pubv(vfree, s + 1); \
        const unsigned dx = (s & 1) ? 65536u : 32768u; \
        ax[0] ^= dx; ax[1] ^= dx; \
        _Pragma("unroll") for (int mt = 0; mt < 4; ++mt) { aw[mt][0] ^= dx; aw[mt][1] ^= dx; } \
        const unsigned flm = min4u(fl); \
        P1_MMA(NT, xf0, wf0); P1_ILV2(NT); \
        if (s == 0 || s == 2 || s == 5) STAMP(20 + s); \
        if (__builtin_expect(flm < (unsigned)s + 2, 0)) wait4(W_FULL, s + 2); \
        FENCE(); SB0(); \
        P1_READ(NT, xf0, wf0, 0); P1_MMA(NT, xf1, wf1); P1_ILV(NT); \
      } \
      P1_READ(NT, xf1, wf1, 1); \
      FENCE(); \
      pubv(vfree, 10); \
      P1_MMA(NT, xf0, wf0); P1_ILV2(NT); \
      STAMP(29); \
    } while (0)
    PRIO(2);
    if (isfull) { P1_LOOP(4); P1_MMA(4, xf1, wf1); } else { P1_LOOP(2); P1_MMA(2, xf1, wf1); }
    STAMP(4);
    wait2(W_AP + 8, 5);
    f32x4 cs[4];
#pragma unroll
    for (int mt = 0; mt < 4; ++mt) cs[mt] = __builtin_bit_cast(f32x4, lds128(lcs + (fg * 64 + mt * 16 + g * 4) * 4));
    const unsigned ho = WS_HF + ((8 * rt + (tok0 >> 4)) * 54 + 2 * (u0 + fg)) * 1024 + l16;
#pragma unroll
    for (int T = 0; T < 4; ++T) {
      if (T < 2 || isfull) {
        const f32x2 st = __builtin_bit_cast(f32x2, lds64(L_STATS + (tok0 + 16 * T + n) * 8));
#pragma unroll
        for (int pr = 0; pr < 2; ++pr) {
          unsigned o[4];
#pragma unroll
          for (int j = 0; j < 4; ++j) {
            const int mt = 2 * pr + (j >> 1), e = 2 * (j & 1);
            const f32x2 av = {acc[mt][T][e], acc[mt][T][e + 1]}, cv = {cs[mt][e], cs[mt][e + 1]};
            o[j] = pk_bf(silu_z(av * st[0] + cv * st[1]));
          }
          __builtin_amdgcn_raw_buffer_store_b128(v4u{o[0], o[1], o[2], o[3]}, rs, ho, T * (54 * 1024) + pr * 1024, AUX_SC1);
          SB0();
        }
      }
    }
    pubv(vfree, 11);
    STAMP(6);
    WAITV(0);
    if (lane < (isfull ? 2 : 1)) flag_set(p.ws + WS_FH, ((4 * rt + (tok0 >> 5) + lane) * 32 + u0 + fg) * 4, epoch);
    STAMP(7);
  } else {
    const int aw = w - 8;
    PRIO(3);
    if (aw == 0 && lane < 28) {
      const int hn = rt * 5 + half * 2 + cti - nfull;
      const bool cvon = lane < 12 || lane >= 20 || (isfull ? lane >= 16 : hn < 72);
      sts32(L_SYNC + lane * 4, cvon ? 0u : 99u);
    }
    const int srow = 128 * rt + 8 * (half * 8 + cti) + 2 * aw;
    cptr xr = (cptr)p.x + (size_t)srow * 1280;
    const v4u xa = *(const GAS v4u*)(xr + l16), xc = *(const GAS v4u*)(xr + 1280 + l16);
    const unsigned xb = *(const GAS unsigned*)(xr + 1024 + lane * 4), xd = *(const GAS unsigned*)(xr + 2304 + lane * 4);
    const int tg8 = (lane & 7) + 8 * (aw & 1), cpl = (lane >> 3) + 8 * (aw >> 1);
    unsigned halo[2][3] = {{0u, 0u, 0u}, {0u, 0u, 0u}};
    v4u shk4[2] = {{0u, 0u, 0u, 0u}, {0u, 0u, 0u, 0u}};
    unsigned vlast[2] = {0u, 0u};
    if (kvrole) {
      if (tg8 == 15) {
#pragma unroll
        for (int it = 0; it < 2; ++it) vlast[it] = *(const GAS unsigned*)((cptr)p.v + (size_t)(128 * kt + 127) * 1280 + 128 * kc + 4 * cpl + 64 * it);
      }
#pragma unroll
      for (int it = 0; it < 2; ++it) shk4[it] = *(const GAS v4u*)((cptr)p.shk + (size_t)(64 * kc + 2 * cpl + 32 * it) * 8);
      if (tg8 == 0 && kt > 0) {
#pragma unroll
        for (int it = 0; it < 2; ++it)
#pragma unroll
          for (int i = 0; i < 3; ++i) halo[it][i] = *(const GAS unsigned*)((cptr)p.k + (size_t)(128 * kt - 3 + i) * 1280 + 128 * kc + 4 * cpl + 64 * it);
      }
    }
    SB0();
    BAR0();
    PRIO(APRIO);
    EPOCH_FIN();
    const unsigned myap = W_AP + 4 * aw;
    const unsigned ka = L_P0 + tg8 * 1024 + ((((cpl >> 2) ^ tg8) & 7) << 4) + (cpl & 3) * 4;
    const unsigned kp = tg8 ? L_P0 + (tg8 - 1) * 1024 + ((((cpl >> 2) ^ (tg8 - 1)) & 7) << 4) + (cpl & 3) * 4 : ka;
    if (!kvrole) {
      wait4(W_PL, 1);
      if (wn < 90) {
      const int g = lane >> 4, q = (lane & 15) >> 2, pp = lane & 3;
#pragma unroll
      for (int it = 0; it < 2; ++it) {
        const int dt = 8 * (wn % 5) + aw + 4 * it;
#pragma unroll
        for (int u = 0; u < 3; ++u) {
          v2u h2[2];
#pragma unroll
          for (int t = 0; t < 2; ++t)
            h2[t] = ldstr(L_P0 + (32 * u + 16 * t + 4 * g + q) * 256 + (((2 * (aw + 4 * it) + (pp >> 1)) ^ ((q << 2) | (g & 3))) << 4) + 8 * (pp & 1));
          __builtin_amdgcn_raw_buffer_store_b128(v4u{h2[0].x, h2[0].y, h2[1].x, h2[1].y}, rs, WS_W2T + (((dt / 5) * 54 + 3 * (wn / 5) + u) * 5 + dt % 5) * 1024 + l16, 0, AUX_SC1);
        }
      }
    }
      SB0();
    }
    {
      f32x2 a[5] = {bf2(xa.x), bf2(xa.y), bf2(xa.z), bf2(xa.w), bf2(xb)}, c[5] = {bf2(xc.x), bf2(xc.y), bf2(xc.z), bf2(xc.w), bf2(xd)};
      f32x2 sa = (a[0] + a[1]) + (a[2] + a[3]) + a[4], sc = (c[0] + c[1]) + (c[2] + c[3]) + c[4];
      const float ma = wave_sum(sa[0] + sa[1]) * (1.0f / D), mc = wave_sum(sc[0] + sc[1]) * (1.0f / D);
      f32x2 qa = {0.f, 0.f}, qc = {0.f, 0.f};
#pragma unroll
      for (int i = 0; i < 5; ++i) { f32x2 e = a[i] - ma; qa += e * e; f32x2 f = c[i] - mc; qc += f * f; }
      const float va = wave_sum(qa[0] + qa[1]) * (1.0f / D), vc = wave_sum(qc[0] + qc[1]) * (1.0f / D);
      const unsigned vb = pk_bf(bf2(pk_bf(va, vc)) + 1e-5f);
      const v4u o = {pk_bf(ma, __builtin_amdgcn_sqrtf(bf_lo(vb))), epoch, pk_bf(mc, __builtin_amdgcn_sqrtf(bf_hi(vb))), epoch};
      if (lane < 2) __builtin_amdgcn_raw_buffer_store_b64(lane ? v2u{o.z, o.w} : v2u{o.x, o.y}, rs, WS_STATS + (srow + lane) * 8, 0, AUX_SC1);
    }
    if (!kvrole) {
      WAITV(1);
      pub(W_MK + 4 * aw, 1, lane);
      if (aw == 0) { wait4(W_MK, 1); if (lane == 0) flag_set(p.ws + WS_FW, wn * 4, epoch); }
    }
    if (rt < 10) {
      wait4(W_FULL, 1);
      const int g = lane >> 4, q = (lane & 15) >> 2, pp = lane & 3;
      float c0 = 0.f, c1 = 0.f, c2 = 0.f, c3 = 0.f;
#pragma unroll
      for (int t = 0; t < 2; ++t) {
        const unsigned rowb = 16384 + (8 * g + 4 * t + q) * 256 + 8 * (pp & 1), swz = (q << 2) | ((2 * g + t) & 3);
        const unsigned a0 = rowb + (((4 * aw + (pp >> 1)) ^ swz) << 4), a1 = rowb + (((4 * aw + 2 + (pp >> 1)) ^ swz) << 4);
#pragma unroll
        for (int u = 0; u < 2; ++u) {
          v2u r0 = ldstr(a0 + 8192 * u), r1 = ldstr(a1 + 8192 * u);
          c0 = dot2ones(r0.x, c0); c1 = dot2ones(r1.x, c1); c2 = dot2ones(r0.y, c2); c3 = dot2ones(r1.y, c3);
        }
      }
      FENCE();
      pub(myap, 1, lane);
      c0 = xsum16_32(c0 + c2); c1 = xsum16_32(c1 + c3);
      if (lane < 32)
        __builtin_amdgcn_raw_buffer_store_b64(v2u{asu(lane < 16 ? c0 : c1), epoch}, rs, WS_CSP + (rt * 1792 + 64 * u0 + 32 * (isfull ? aw : aw & 1) + lane) * 8, 0, AUX_SC1);
      SB0();
    }
    STAMP(6);
    wait4(W_PL, 1);
    STAMP(1);
    if (kvrole) {
      unsigned kk[2][11], vv[2][8];
#pragma unroll
      for (int it = 0; it < 2; ++it) {
#pragma unroll
        for (int i = 0; i < 3; ++i) kk[it][i] = lds32((kp ^ (64 * it)) + (5 + i) * 128);
#pragma unroll
        for (int i = 0; i < 8; ++i) { kk[it][3 + i] = lds32((ka ^ (64 * it)) + i * 128); vv[it][i] = lds32((ka ^ (64 * it)) + 16384 + i * 128); }
      }
      const unsigned kvo = WS_KVR + (64 * kc + 2 * cpl) * KVR_ROW + (2040 - 128 * kt - 8 * tg8) * 2;
      v4u ra[2], rb[2];
#pragma unroll
      for (int it = 0; it < 2; ++it) {
        const v4u sh = shk4[it];
        const f32x2 s0 = {bf_lo(sh.x), bf_lo(sh.z)}, s1 = {bf_hi(sh.x), bf_hi(sh.z)}, s2 = {bf_lo(sh.y), bf_lo(sh.w)}, s3 = {bf_hi(sh.y), bf_hi(sh.w)};
        f32x2 kf[11], o[8];
#pragma unroll
        for (int i = 0; i < 11; ++i) kf[i] = bf2(i < 3 ? (tg8 ? kk[it][i] : halo[it][i]) : kk[it][i]);
#pragma unroll
        for (int i = 0; i < 8; ++i) {
          f32x2 s = kf[i] * s3 + kf[i + 1] * s2 + kf[i + 2] * s1 + kf[i + 3] * s0;
          o[i] = bf2(pk_bf(s)) * bf2(i == 7 ? (tg8 == 15 ? vlast[it] : vv[it][i]) : vv[it][i]);
        }
        ra[it] = v4u{pk_bf(o[7][0], o[6][0]), pk_bf(o[5][0], o[4][0]), pk_bf(o[3][0], o[2][0]), pk_bf(o[1][0], o[0][0])};
        rb[it] = v4u{pk_bf(o[7][1], o[6][1]), pk_bf(o[5][1], o[4][1]), pk_bf(o[3][1], o[2][1]), pk_bf(o[1][1], o[0][1])};
        USE(ra[it]); USE(rb[it]);
      }
      STAMP(7);
#pragma unroll
      for (int it = 0; it < 2; ++it) {
        const unsigned o2 = kvo + it * (32 * KVR_ROW);
        __builtin_amdgcn_raw_buffer_store_b128(ra[it], rs, o2, 0, AUX_SC1);
        __builtin_amdgcn_raw_buffer_store_b128(rb[it], rs, o2 + KVR_ROW, 0, AUX_SC1);
        if (kt == 0 && tg8 == 0) {
          __builtin_amdgcn_raw_buffer_store_b128(v4u{0u, 0u, 0u, 0u}, rs, o2 + 16, 0, AUX_SC1);
          __builtin_amdgcn_raw_buffer_store_b128(v4u{0u, 0u, 0u, 0u}, rs, o2 + KVR_ROW + 16, 0, AUX_SC1);
        }
      }
    }
    SB0();
    STAMP(2);
    WAITV(0);
    pub(myap, 3, lane);
    wait4(W_AP, 3);
    if (aw == 0) {
      wait4(W_FREE, 1);
      if (lane == 32 || (kvrole && lane < 32))
        flag_set(p.ws, lane == 32 ? WS_FP0 + (kvrole ? kvn : 160 + wn) * 4 : WS_MBKV + (kc * 32 + lane) * 128 + kt * 4, epoch);
    }
    PRIO(1);
    STAMP(3);
    LATE_CONSTS();
    H_DMA(); CONV_PADS();
    if (aw == 1) {
      if (idx == 31) {
#pragma unroll 1
        for (unsigned spins = 0; spins < SPIN_CAP; ++spins) {
          __builtin_amdgcn_s_sleep(8);
          const v2u f = __builtin_amdgcn_raw_buffer_load_b64(rs, WS_FW + (lane < 48 ? lane : 0) * 8, 0, AUX_SC1);
          if (__all(f.x == epoch && f.y == epoch)) break;
        }
        if (lane < 4) flag_set(p.ws + WS_W2R, (xcd * 4 + lane) * 128, epoch);
      } else {
        wait4(W_FREE, 5);
        poll1(p.ws, WS_W2R + (xcd * 4 + (idx & 3)) * 128, epoch);
      }
      pub(myap, 4, lane);
    }
    STAMP(19);
    if (aw < 2) {
      if (aw == 0) {
        wait4(W_FREE, 6);
        poll1(p.ws, WS_MBKV + ((chb + 3) >> 1) * 128 + (lane & 15) * 4, epoch);
        pub(myap, 4, lane);
      }
      wait4(W_FREE, 7);
    } else {
      wait4(W_FREE, 6);
      v4u gr[10];
      const unsigned gb = aw == 2 ? WS_STATS + 128 * rt * 8 + l16 : WS_CSP + 64 * u0 * 8 + (isfull ? l16 : (l16 & 511));
#pragma unroll 1
      for (unsigned spins = 0; spins < SPIN_CAP; ++spins) {
        gr[0] = __builtin_amdgcn_raw_buffer_load_b128(rs, gb, 0, AUX_SC1);
        unsigned bad = 0;
        if (aw == 3) {
#pragma unroll
          for (int i = 1; i < 10; ++i) gr[i] = __builtin_amdgcn_raw_buffer_load_b128(rs, gb, i * (1792 * 8), AUX_SC1);
          float c0 = asf(gr[0].x), c1 = asf(gr[0].z);
#pragma unroll
          for (int i = 1; i < 10; ++i) { bad |= (gr[i].y ^ epoch) | (gr[i].w ^ epoch); c0 += asf(gr[i].x); c1 += asf(gr[i].z); }
          gr[0].x = asu(c0); gr[0].z = asu(c1);
        }
        bad |= (gr[0].y ^ epoch) | (gr[0].w ^ epoch);
        if (__all(bad == 0u)) break;
        __builtin_amdgcn_s_sleep(4);
      }
      stq = v2u{gr[0].x, gr[0].z};
      wait4(W_FREE, 8);
      if (aw == 2) {
        const float r0 = __builtin_amdgcn_rcpf(bf_hi(gr[0].x)) * LOG2E, r1 = __builtin_amdgcn_rcpf(bf_hi(gr[0].z)) * LOG2E;
        sts128(L_STATS + l16, __builtin_bit_cast(v4u, f32x4{-r0, bf_lo(gr[0].x) * r0, -r1, bf_lo(gr[0].z) * r1}));
      } else if (isfull || lane < 32) sts64(lcs + lane * 8, v2u{gr[0].x, gr[0].z});
      FENCE();
      pub(myap, 5, lane);
    }
    STAMP(4);
    wait1(W_AP, 4);
    R_DMA();
    WAITV(0);
    STAMP(5);
  }
  LATE_CONSTS();
  STAMP(8);
  if (convw) {
    const int rh = w & 1;
    const unsigned cvp = W_CV + area * 8, mycv = cvp + rh * 4;
    pub(mycv, 1, lane);
    const int i16 = lane & 15, kg = lane >> 4;
    const unsigned rbase = cb + ((i16 & 1) ? CH_R1 : CH_R0);
    unsigned va = rbase + 4032 - 3584 - 64 * (i16 >> 1) + 16 * kg + 256 * rh;
    const unsigned vamax = rbase + 4096;
    const unsigned hbase = cb + ((i16 & 1) ? CH_H0 : CH_H1);
    unsigned vb = hbase + 4 * i16 - ((i16 & 1) ? 60 : 64) + 16 * kg + 256 * rh;
    const unsigned vbmin = hbase - 16;
    USE(va); USE(vb);
    f32x4 acc[8];
#pragma unroll
    for (int i = 0; i < 8; ++i) acc[i] = f32x4{0.f, 0.f, 0.f, 0.f};
    const float dD = bf_lo(*(kptr)((cptr)p.sD + (cch >> 1) * 4) >> ((cch & 1) * 16));
    if (w >= 8) { wait2(cvp, 1); wait4(W_FREE, 11); } else wait4(W_FULL, 12);
    PRIO(3);
#define LDA_(i, o) lds128(va + 3584 - 512 * (i) + (o))
#define LDB_(dst, i, o) do { v2u lo = lds64v(vb + 512 * (i) + (o)), hi = lds64v(vb + 512 * (i) + (o) + 8); dst = v4u{lo.x, lo.y, hi.x, hi.y}; } while (0)
#define LDP(i, o) do { A[i] = LDA_(i, o); LDB_(B[i], i, o); } while (0)
#define STEP(m) do { _Pragma("unroll") for (int o = 0; o < (m); ++o) if ((m) + o <= 7) acc[(m) + o] = mfma(A[m], B[o], acc[(m) + o]); \
      if (2 * (m) <= 7) acc[2 * (m)] = mfma(A[m], B[m], acc[2 * (m)]); \
      _Pragma("unroll") for (int o = 0; o < (m); ++o) if ((m) + o <= 7) acc[(m) + o] = mfma(A[o], B[m], acc[(m) + o]); } while (0)
#define ILV(n) do { _Pragma("unroll") for (int i_ = 0; i_ < (n); ++i_) { SGB(0x8, 1); SGB(0x100, 1); } SGB(0x8, 16); SB0(); } while (0)
    v4u A[8], B[8];
    {
      A[0] = lds128(min(va + 3584, vamax));
      v2u lo = lds64v(max(vb, vbmin)), hi = lds64v(max(vb + 8, vbmin));
      B[0] = v4u{lo.x, lo.y, hi.x, hi.y};
      LDP(1, 0); LDP(2, 0); LDP(3, 0);
    }
    SB0();
#pragma unroll 1
    for (int r = 0; r < 4; ++r) {
      v4u nA0, nB0, nA1, nB1;
      LDP(4, 0); STEP(0); STEP(1); ILV(3);
      LDP(5, 0); STEP(2); ILV(3);
      LDP(6, 0); LDP(7, 0); STEP(3); ILV(6);
      nA0 = lds128(min(va + 3584 + 64, vamax)); LDB_(nB0, 0, 64); nA1 = LDA_(1, 64); LDB_(nB1, 1, 64); STEP(4); ILV(6);
      LDP(3, 64); STEP(5); ILV(3);
      LDP(2, 64); STEP(6); STEP(7); ILV(3);
      A[0] = nA0; B[0] = nB0; A[1] = nA1; B[1] = nB1;
      va += 64; vb += 64; USE(va); USE(vb);
    }
    const int chunk = 128 * rh + lane;
    const v4u kva = lds128(cb + CH_R1 + (255 - chunk) * 16), kvb = lds128(cb + CH_R1 + (191 - chunk) * 16);
    STAMP(9);
    PRIO(3);
    FENCE();
    pub(mycv, 2, lane); wait2(cvp, 2);
    {
      const unsigned a = cb + rh * 8192 + (64 * (lane >> 4) + 2 * (lane & 15)) * 4;
#pragma unroll
      for (int i = 0; i < 8; ++i) {
        sts64(a + 1024 * i, __builtin_bit_cast(v2u, f32x2{acc[i][0], acc[i][1]}));
        sts64(a + 1024 * i + 128, __builtin_bit_cast(v2u, f32x2{acc[i][2], acc[i][3]}));
      }
    }
    FENCE();
    pub(mycv, 3, lane); wait2(cvp, 3);
    conv_final(rs, cb, cch, chunk, kva, dD);
    conv_final(rs, cb, cch, chunk + 64, kvb, dD);
    FENCE();
    pub(mycv, 4, lane);
    if (w >= 8) { WAITV(0); if (lane == 0) flag_set(p.ws + WS_FY, (rh * 640 + cch) * 4, epoch); }
  }
  STAMP(10);
  if (w < 4) {
    const unsigned vfull = pub_addr(W_FULL + 4 * w, lane);
    if (!isfull) {
      wait4(W_FREE, 8);
      H_DMA(); CONV_PADS();
    }
    wait1(W_AP + 4, 4);
    v4u RG[4][8];
#define RG_LD(i, g) do { _Pragma("unroll") for (int j = 0; j < 8; ++j) { if ((j & 3) == 0) WAITV(8); \
      RG[i][j] = __builtin_amdgcn_raw_buffer_load_b128(rs, (unsigned)(w2s - p.ws) + l16, (g) * 15360 + j * 1024, AUX_SC1); } SB0(); } while (0)
#define RG_ST(i, g) do { wait4(W_FREE, 21 + (g) - 4); unsigned a_ = w2d + wslot(g) + l16; USE(a_); \
      _Pragma("unroll") for (int j = 0; j < 8; ++j) sts128(a_ + j * 1024, RG[i][j]); FENCE(); pubv(vfull, 21 + (g)); } while (0)
    wait4(W_FREE, 8);
    W2GROUP(2);
    if (isfull) W2GROUP(3); else { wait1(W_AP, 4); R_DMA(); }
    wait4(W_FREE, 9); W2GROUP(0);
    wait4(W_FREE, 10); W2GROUP(1);
    pubv(vfull, 12);
    RG_LD(0, 4);
    WAITV(8); pubv(vfull, isfull ? 24 : 23);
    RG_LD(1, 5); RG_LD(2, 6); RG_LD(3, 7);
    STAMP(11);
    if (!isfull) { wait4(W_CV, 4); W2GROUP(3); WAITV(0); pubv(vfull, 24); }
    PRIO(3);
    RG_ST(0, 4);
    RG_LD(0, 8);
    RG_ST(1, 5); RG_ST(2, 6); RG_ST(3, 7); RG_ST(0, 8);
    if (b == 0 && w == 3) {
      poll4(p.ws + WS_FP0, l16, epoch);
      if (lane == 0) __builtin_amdgcn_raw_buffer_store_b32(epoch, rs, WS_EPOCH, 0, AUX_SC1);
    }
    PRIO(0);
  } else if (w < 8) {
    const int cw = w - 4, tp = cw & 1, kh = cw >> 1;
    const unsigned vfree = pub_addr(W_FREE + 4 * cw, lane);
    const unsigned hoff = WS_HF + ((4 * tt64 + 2 * tp) * 54 + 27 * kh) * 1024 + l16;
    constexpr int HW = HWIN;
    v4u hf[27][2];
    f32x4 acc[2][5];
#pragma unroll
    for (int i = 0; i < 5; ++i) acc[0][i] = acc[1][i] = f32x4{0.f, 0.f, 0.f, 0.f};
    unsigned wa[2] = {kh * 15360 + l16, kh * 15360 + l16 + 65536};
    USE(wa[0]); USE(wa[1]);
    PRIO(2);
    {
      const unsigned fho = ((2 * tt64 + tp) * 32 + 13 * kh + lane) * 4;
      const unsigned v0 = atom_rd(p.ws + WS_FH, fho, 0x3fffull, epoch);
      const bool ok0 = wait_eq1<0>(v0, epoch);
      if (convw && lane == 0) flag_set(p.ws + WS_FY, ((w & 1) * 640 + cch) * 4, epoch);
      if (!ok0) poll_atomic(p.ws + WS_FH, fho, 0x3fffull, epoch);
    }
#define H_LD(i) do { hf[i][0] = __builtin_amdgcn_raw_buffer_load_b128(rs, hoff, (i) * 1024, AUX_SC1); \
      hf[i][1] = __builtin_amdgcn_raw_buffer_load_b128(rs, hoff, (i) * 1024 + 54 * 1024, AUX_SC1); } while (0)
#pragma unroll
    for (int i = 0; i < HW; ++i) H_LD(i);
    STAMP(11);
    v4u a0[5], a1[5];
    v4u fl;
    unsigned flm = 0;
#define P3_AD(ks, i) (wa[wslot((ks) / 3) >> 16] + (wslot((ks) / 3) & 65535) + (((ks) % 3) * 5 + (i)) * 1024)
#define P3_STEP(A, N, ks) do { \
      if (((ks) + 1) % 3 == 0 && (ks) < 26) { if (__builtin_expect(flm < 21 + ((ks) + 1) / 3, 0)) wait4(W_FULL, 21 + ((ks) + 1) / 3); FENCE(); } \
      _Pragma("unroll") for (int i = 0; i < 5; ++i) { \
        mfma_ip(acc[0][i], A[i], hf[ks][0]); \
        if ((ks) < 26) N[i] = lds128v(P3_AD((ks) + 1, i)); \
        mfma_ip(acc[1][i], A[i], hf[ks][1]); \
        if (i == 1 && (ks) % 3 == 0 && (ks) < 24) fl = peek4(W_FULL); \
        if (i == 3 && (ks) % 3 == 1 && (ks) < 24) flm = min4u(fl); \
        if (i == 1 && (ks) == 25) fl = peek4(W_FREE); \
      } \
      if (((ks) + 1) % 3 == 2 && (ks) < 25) pubv(vfree, 21 + ((ks) + 1) / 3); \
      SB0(); \
      if ((ks) + HW < 27) H_LD((ks) + HW); \
      SB0(); \
    } while (0)
    wait4(W_FULL, 21);
#pragma unroll
    for (int i = 0; i < 5; ++i) a0[i] = lds128v(P3_AD(0, i));
    SB0();
#pragma unroll
    for (int ks = 0; ks < 26; ks += 2) {
      P3_STEP(a0, a1, ks);
      P3_STEP(a1, a0, ks + 1);
      if (ks == 2) STAMP(12);
      if (ks == 12) STAMP(13);
    }
    P3_STEP(a0, a1, 26);
    const v4u fr = fl;
    asm volatile("s_nop 7\n\ts_nop 7" ::: "memory");
    STAMP(14);
    if (min4u(fr) < 27) wait4(W_FREE, 27);
    FENCE();
    {
      const unsigned oa = L_O + kh * (64 * OROW) + (32 * tp + (lane & 15)) * OROW + (lane >> 4) * 16;
#pragma unroll
      for (int i = 0; i < 5; ++i) { sts128(oa + i * 64, __builtin_bit_cast(v4u, acc[0][i])); sts128(oa + 16 * OROW + i * 64, __builtin_bit_cast(v4u, acc[1][i])); }
    }
    PRIO(0);
  } else {
    const int aw = w - 8;
    PRIO(1);
    wait4(W_CV + 16, 4);
    if (!convw) { pub(W_AP + 4 * aw, 6, lane); wait4(W_AP, 6); }
    const int npc = aw < 3 ? 3 : 1;
#pragma unroll 1
    for (int i = 0; i < npc; ++i) {
      const int it = (3 * aw + i) * 64 + lane;
      dma16((cptr)p.x + (size_t)(64 * tt64) * 1280 + c80 * 160, (it / 10) * 1280 + (it % 10) * 16, L_X + (3 * aw + i) * 1024);
    }
    if (aw == 2) sts64(L_XN + lane * 8, stq);
    STAMP(11);
    wait4(W_FREE, 23);
    poll_atomic(p.ws + WS_FY, ((tt64 >> 4) * 640 + c80 * 80 + aw * 24 + lane) * 4, aw < 3 ? 0xffffffull : 0xffull, epoch);
    STAMP(12);
#pragma unroll 1
    for (int i = 0; i < npc; ++i) {
      const int pc = 3 * aw + i;
      dma16s(p.ws + WS_YT + (size_t)(c80 * 80 + 8 * pc) * 4096 + tt64 * 128, (lane >> 3) * 4096 + (((lane ^ pc) & 7) << 4), L_Y + pc * 1024);
    }
    WAITV(0);
    PRIO(0);
  }
  STAMP(15);
  BAR0();
  STAMP(16);
  if (tid < 640) {
    const int ftok = tid / 10, fc8 = tid % 10;
    const unsigned oa = L_O + ftok * OROW + fc8 * 32;
    f32x4 o0 = __builtin_bit_cast(f32x4, lds128(oa)) + __builtin_bit_cast(f32x4, lds128(oa + 64 * OROW));
    f32x4 o1 = __builtin_bit_cast(f32x4, lds128(oa + 16)) + __builtin_bit_cast(f32x4, lds128(oa + 64 * OROW + 16));
    const v4u xv = lds128(L_X + tid * 16);
    const unsigned fs = lds32(L_XN + (64 * (tt64 & 1) + ftok) * 4);
    const float fm = bf_lo(fs), fr = __builtin_amdgcn_rcpf(bf_hi(fs));
    const unsigned ya = L_Y + fc8 * 1024 + ((((ftok >> 3) ^ fc8) & 7) << 4) + (ftok & 7) * 2;
    f32x2 o[4] = {{o0[0], o0[1]}, {o0[2], o0[3]}, {o1[0], o1[1]}, {o1[2], o1[3]}};
    unsigned xw[4] = {xv.x, xv.y, xv.z, xv.w}, rw[4];
#pragma unroll
    for (int j = 0; j < 4; ++j) {
      const f32x2 y = {bf_lo(*(const LAS u16*)(size_t)(ya + 256 * j)), bf_lo(*(const LAS u16*)(size_t)(ya + 256 * j + 128))};
      const f32x2 gb = silu_z(o[j] * -LOG2E);
      const f32x2 xf = bf2(xw[j]);
      rw[j] = pk_bf(xf + bf2(pk_bf((xf - fm) * fr)) * y * gb);
    }
    *(GAS v4u*)((unsigned char*)p.out + (size_t)(64 * tt64 + ftok) * 1280 + c80 * 160 + fc8 * 16) = v4u{rw[0], rw[1], rw[2], rw[3]};
  }
}

extern "C" void kernel_launch(void* const* d_in, const int* in_sizes, int n_in, void* d_out, int out_size, void* d_ws, size_t ws_size,
                              hipStream_t stream) {
  Params p;
  p.x = (const u16*)d_in[0]; p.k = (const u16*)d_in[1]; p.v = (const u16*)d_in[2];
  p.shk = (const u16*)d_in[3]; p.sk = (const u16*)d_in[4]; p.sD = (const u16*)d_in[5];
  p.w1 = (const u16*)d_in[7]; p.w2 = (const u16*)d_in[8];
  p.out = (u16*)d_out; p.ws = (unsigned char*)d_ws;
  fused_kernel<<<NWG, NTHR, 0, stream>>>(p);
}
```

```cpp
#define DIAG 0
#define DIAGW 4
#include <hip/hip_runtime.h>
#include <stdint.h>

typedef unsigned short u16;
typedef unsigned v4u __attribute__((ext_vector_type(4)));
typedef unsigned v2u __attribute__((ext_vector_type(2)));
typedef float f32x4 __attribute__((ext_vector_type(4)));
typedef float f32x2 __attribute__((ext_vector_type(2)));
typedef short bf16x8 __attribute__((ext_vector_type(8)));
typedef short v4s __attribute__((ext_vector_type(4)));
typedef __bf16 v2bf __attribute__((ext_vector_type(2)));
#define LAS __attribute__((address_space(3)))
#define GAS __attribute__((address_space(1)))
#define DEV __device__ __forceinline__
typedef const unsigned char* cptr;
typedef const __attribute__((address_space(4))) unsigned* kptr;

constexpr int S = 2048, D = 640, F = 1728;
constexpr int NWG = 256, NTHR = 768;
constexpr unsigned WS_EPOCH = 0;
constexpr unsigned WS_FP0 = 1024;
constexpr unsigned WS_FW = 3072;
constexpr unsigned WS_FH = 4096;
constexpr unsigned WS_FY = 16384;
constexpr unsigned WS_STATS = 32768;
constexpr unsigned KVR_ROW = 4224;
constexpr unsigned WS_KVR = 65536;
constexpr unsigned WS_W2T = WS_KVR + 640 * KVR_ROW;
constexpr unsigned WS_HF = WS_W2T + 8 * 54 * 5 * 1024;
constexpr unsigned WS_YT = WS_HF + 128 * 54 * 1024;
constexpr unsigned WS_CSP = WS_YT + 640 * 4096;
constexpr unsigned WS_MBKV = WS_CSP + 147456;
constexpr unsigned WS_W2R = WS_MBKV + 40960;
constexpr unsigned WS_END = WS_W2R + 4096;
constexpr unsigned WS_DBG = WS_YT + 640 * 4096 + 262144;
constexpr unsigned L_E = 131072;
constexpr unsigned L_STATS = 65536 + 30720;
constexpr unsigned L_CS = 98304 + 30720;
constexpr unsigned CH_R1 = 0, CH_Z1 = 4096, CH_R0 = 4128, CH_Z0 = 8224, CH_H0 = 8240, CH_PF = 12416, CH_H1 = 12432, CH_BYTES = 16528;
constexpr unsigned L_SYNC = 163840 - 128;
constexpr unsigned W_FULL = L_SYNC, W_FREE = L_SYNC + 16, W_AP = L_SYNC + 32, W_CV = L_SYNC + 48, W_PL = L_SYNC + 80, W_MK = L_SYNC + 96;
constexpr unsigned L_AREA0 = L_SYNC - 4 * CH_BYTES;
constexpr unsigned L_P0 = L_E;
constexpr unsigned L_Y = L_E;
constexpr unsigned L_X = L_E + 10240;
constexpr unsigned L_XN = L_E + 20480;
constexpr unsigned L_O = 32768;
constexpr unsigned OROW = 336;
constexpr unsigned SPIN_CAP = 1u << 15;
constexpr int AUX_SC1 = 16;
constexpr unsigned wslot(int g) { return (g & 3) * 32768u; }

DEV float asf(unsigned u) { return __builtin_bit_cast(float, u); }
DEV unsigned asu(float f) { return __builtin_bit_cast(unsigned, f); }
DEV float bf_lo(unsigned u) { return __builtin_bit_cast(float, u << 16); }
DEV float bf_hi(unsigned u) { return __builtin_bit_cast(float, u & 0xffff0000u); }
DEV f32x2 bf2(unsigned u) { return f32x2{bf_lo(u), bf_hi(u)}; }
DEV unsigned pk_bf(float a, float b) { v2bf r = {(__bf16)a, (__bf16)b}; return __builtin_bit_cast(unsigned, r); }
DEV unsigned pk_bf(f32x2 a) { return pk_bf(a[0], a[1]); }
DEV float rbf(float a) { return bf_lo(pk_bf(a, a) & 0xffffu); }
DEV f32x4 mfma(v4u a, v4u b, f32x4 c) {
  return __builtin_amdgcn_mfma_f32_16x16x32_bf16(__builtin_bit_cast(bf16x8, a), __builtin_bit_cast(bf16x8, b), c, 0, 0, 0);
}
DEV void mfma_ip(f32x4& c, v4u a, v4u b) { asm volatile("v_mfma_f32_16x16x32_bf16 %0, %1, %2, %0" : "+v"(c) : "v"(a), "v"(b)); }
DEV v4u lds128v(unsigned a) { return *(const volatile LAS v4u*)(size_t)a; }
constexpr float LOG2E = 1.44269504088896f;
DEV f32x2 silu_z(f32x2 z) {
  f32x2 e = {__builtin_amdgcn_exp2f(z[0]), __builtin_amdgcn_exp2f(z[1])};
  f32x2 d = e * -LOG2E - LOG2E;
  f32x2 r = {__builtin_amdgcn_rcpf(d[0]), __builtin_amdgcn_rcpf(d[1])};
  return z * r;
}
DEV float dot2ones(unsigned u, float acc) {
  return __builtin_amdgcn_fdot2_f32_bf16(__builtin_bit_cast(v2bf, u), __builtin_bit_cast(v2bf, 0x3f803f80u), acc, false);
}

DEV void dma16(const void* sbase, unsigned voff, unsigned ldsoff) {
  asm volatile("s_mov_b32 m0, %2\n\ts_nop 0\n\tglobal_load_lds_dwordx4 %0, %1" ::"v"(voff), "s"(sbase), "s"(ldsoff) : "memory");
}
DEV void dma16s(const void* sbase, unsigned voff, unsigned ldsoff) {
  asm volatile("s_mov_b32 m0, %2\n\ts_nop 0\n\tglobal_load_lds_dwordx4 %0, %1 sc1" ::"v"(voff), "s"(sbase), "s"(ldsoff) : "memory");
}
DEV void dma4k(const void* sbase, unsigned voff, unsigned ldsoff) {
  asm volatile("s_mov_b32 m0, %2\n\ts_nop 0\n\tglobal_load_lds_dwordx4 %0, %1\n\tglobal_load_lds_dwordx4 %0, %1 offset:1024\n\t"
               "global_load_lds_dwordx4 %0, %1 offset:2048\n\tglobal_load_lds_dwordx4 %0, %1 offset:3072" ::"v"(voff), "s"(sbase), "s"(ldsoff) : "memory");
}
DEV void dma4ks(const void* sbase, unsigned voff, unsigned ldsoff) {
  asm volatile("s_mov_b32 m0, %2\n\ts_nop 0\n\tglobal_load_lds_dwordx4 %0, %1 sc1\n\tglobal_load_lds_dwordx4 %0, %1 offset:1024 sc1\n\t"
               "global_load_lds_dwordx4 %0, %1 offset:2048 sc1\n\tglobal_load_lds_dwordx4 %0, %1 offset:3072 sc1" ::"v"(voff), "s"(sbase), "s"(ldsoff) : "memory");
}
DEV void dma4v(const void* sbase, const unsigned (&vo)[4], unsigned ldsoff) {
  asm volatile("s_mov_b32 m0, %5\n\ts_nop 0\n\tglobal_load_lds_dwordx4 %0, %4\n\tglobal_load_lds_dwordx4 %1, %4 offset:1024\n\t"
               "global_load_lds_dwordx4 %2, %4 offset:2048\n\tglobal_load_lds_dwordx4 %3, %4 offset:3072"
               ::"v"(vo[0]), "v"(vo[1]), "v"(vo[2]), "v"(vo[3]), "s"(sbase), "s"(ldsoff) : "memory");
}
DEV void dma4vm(const void* sbase, const unsigned (&vo)[4], unsigned ldsoff, unsigned long long lastmask) {
  asm volatile("s_mov_b32 m0, %5\n\ts_nop 0\n\tglobal_load_lds_dwordx4 %0, %4\n\tglobal_load_lds_dwordx4 %1, %4 offset:1024\n\t"
               "global_load_lds_dwordx4 %2, %4 offset:2048\n\ts_mov_b64 exec, %6\n\tglobal_load_lds_dwordx4 %3, %4 offset:3072\n\ts_mov_b64 exec, -1"
               ::"v"(vo[0]), "v"(vo[1]), "v"(vo[2]), "v"(vo[3]), "s"(sbase), "s"(ldsoff), "s"(lastmask) : "memory");
}
DEV void touch(const void* sbase, unsigned voff) {
  asm volatile("s_mov_b32 m0, 0x28000\n\ts_nop 0\n\tglobal_load_lds_dword %0, %1" ::"v"(voff), "s"(sbase) : "memory");
}
DEV v4u ld128a(const void* sbase, unsigned voff) { v4u r; asm volatile("global_load_dwordx4 %0, %1, %2 sc1" : "=v"(r) : "v"(voff), "s"(sbase) : "memory"); return r; }
DEV v2u ld64a(const void* sbase, unsigned voff) { v2u r; asm volatile("global_load_dwordx2 %0, %1, %2 sc1" : "=v"(r) : "v"(voff), "s"(sbase) : "memory"); return r; }
DEV unsigned atom_rd(const void* sbase, unsigned voff, unsigned long long mask, unsigned dflt) {
  unsigned r; unsigned long long sv;
  asm volatile("v_mov_b32 %0, %6\n\ts_mov_b64 %1, exec\n\ts_mov_b64 exec, %5\n\tglobal_atomic_or %0, %2, %3, %4 sc0 sc1\n\ts_mov_b64 exec, %1"
               : "=&v"(r), "=&s"(sv) : "v"(voff), "v"(0u), "s"(sbase), "s"(mask), "s"(dflt) : "memory");
  return r;
}
DEV unsigned ld32a(const void* sbase, unsigned voff) { unsigned r; asm volatile("global_load_dword %0, %1, %2 sc1" : "=v"(r) : "v"(voff), "s"(sbase) : "memory"); return r; }
#define WAITV(n) asm volatile("s_waitcnt vmcnt(" #n ")" ::: "memory")
#define USE(x) asm volatile("" : "+v"(x))
#define BAR0() do { asm volatile("s_waitcnt lgkmcnt(0)" ::: "memory"); __builtin_amdgcn_s_barrier(); asm volatile("" ::: "memory"); } while (0)
#define SGB(m, n) __builtin_amdgcn_sched_group_barrier(m, n, 0)
#define SB0() __builtin_amdgcn_sched_barrier(0)

DEV v4u lds128(unsigned a) { return *(const LAS v4u*)(size_t)a; }
DEV v2u lds64(unsigned a) { return *(const LAS v2u*)(size_t)a; }
DEV v2u lds64v(unsigned a) { return *(const volatile LAS v2u*)(size_t)a; }
DEV unsigned lds32(unsigned a) { return *(const LAS unsigned*)(size_t)a; }
DEV void sts128(unsigned a, v4u v) { *(LAS v4u*)(size_t)a = v; }
DEV void sts64(unsigned a, v2u v) { *(LAS v2u*)(size_t)a = v; }
DEV void sts32(unsigned a, unsigned v) { *(LAS unsigned*)(size_t)a = v; }
DEV void sts16(unsigned a, unsigned v) { *(LAS u16*)(size_t)a = (u16)v; }
DEV v2u ldstr(unsigned a) { return __builtin_bit_cast(v2u, __builtin_amdgcn_ds_read_tr16_b64_v4i16((LAS v4s*)(size_t)a)); }

#define SLP 2
DEV void pub(unsigned a, unsigned v, int lane) { if (lane == 0) *(volatile LAS unsigned*)(size_t)a = v; }
DEV unsigned pub_addr(unsigned a, int lane) { unsigned r = lane == 0 ? a : 0x30000u + ((a + lane * 4) & 255); asm volatile("" : "+v"(r)); return r; }
DEV void pubv(unsigned va, unsigned v) { *(volatile LAS unsigned*)(size_t)va = v; }
DEV unsigned min4u(v4u v) { return (unsigned)__builtin_amdgcn_readfirstlane(min(min(v.x, v.y), min(v.z, v.w))); }
DEV v4u peek4(unsigned a) { return *(const volatile LAS v4u*)(size_t)a; }
DEV bool ge4(v4u v, unsigned t) { return (unsigned)__builtin_amdgcn_readfirstlane(min(min(v.x, v.y), min(v.z, v.w))) >= t; }
DEV void wait4(unsigned a, unsigned t) {
#pragma unroll 1
  for (unsigned i = 0; i < (1u << 22); ++i) { if (ge4(peek4(a), t)) break; __builtin_amdgcn_s_sleep(SLP); }
  asm volatile("" ::: "memory");
}
DEV void wait2(unsigned a, unsigned t) {
#pragma unroll 1
  for (unsigned i = 0; i < (1u << 22); ++i) {
    v2u v = *(const volatile LAS v2u*)(size_t)a;
    if ((unsigned)__builtin_amdgcn_readfirstlane(min(v.x, v.y)) >= t) break;
    __builtin_amdgcn_s_sleep(1);
  }
  asm volatile("" ::: "memory");
}
DEV void wait1(unsigned a, unsigned t) {
#pragma unroll 1
  for (unsigned i = 0; i < (1u << 22); ++i) {
    if ((unsigned)__builtin_amdgcn_readfirstlane(*(const volatile LAS unsigned*)(size_t)a) >= t) break;
    __builtin_amdgcn_s_sleep(1);
  }
  asm volatile("" ::: "memory");
}
DEV float xsum16_32(float v) {
  float a = v, c = v;
  asm volatile("s_nop 1\n\tv_permlane16_swap_b32 %0, %1\n\ts_nop 0" : "+v"(a), "+v"(c));
  a += c; c = a;
  asm volatile("s_nop 1\n\tv_permlane32_swap_b32 %0, %1\n\ts_nop 0" : "+v"(a), "+v"(c));
  return a + c;
}
#define WAITL() asm volatile("s_waitcnt lgkmcnt(0)" ::: "memory")
template <int CTRL, int RM> DEV float dpp_add(float v) {
  return v + __builtin_bit_cast(float, __builtin_amdgcn_update_dpp(0, __builtin_bit_cast(int, v), CTRL, RM, 0xf, false));
}
DEV float wave_sum(float v) {
  v = dpp_add<0x111, 0xf>(v); v = dpp_add<0x112, 0xf>(v); v = dpp_add<0x114, 0xf>(v); v = dpp_add<0x118, 0xf>(v);
  v = dpp_add<0x142, 0xa>(v); v = dpp_add<0x143, 0xc>(v);
  return __builtin_bit_cast(float, __builtin_amdgcn_readlane(__builtin_bit_cast(int, v), 63));
}

#define BAR() BAR0()
#define STAMP(i) do { if ((i) == 0) dg0 = (unsigned)__builtin_amdgcn_s_memrealtime(); if ((i) == DIAG) dg1 = (unsigned)__builtin_amdgcn_s_memrealtime(); } while (0)

#define ROTC 0
#define LPRIO 1
#define HWIN 8
#define APRIO 3
struct Params {
  const u16 *x, *k, *v, *shk, *sk, *sD, *w1, *w2;
  u16* out;
  unsigned char* ws;
};

template <int N> DEV bool wait_eq1(unsigned v, unsigned epoch) {
  unsigned d;
  asm volatile("s_waitcnt vmcnt(%3)\n\tv_xor_b32 %0, %2, %1" : "=v"(d) : "v"(v), "s"(epoch), "i"(N) : "memory");
  return __all(d == 0u);
}
template <int N> DEV bool wait_eq4(v4u v, unsigned epoch) {
  unsigned d, e;
  asm volatile("s_waitcnt vmcnt(%7)\n\tv_xor_b32 %0, %6, %2\n\tv_xor_b32 %1, %6, %3\n\tv_or_b32 %0, %0, %1\n\tv_xor_b32 %1, %6, %4\n\t"
               "v_or_b32 %0, %0, %1\n\tv_xor_b32 %1, %6, %5\n\tv_or_b32 %0, %0, %1"
               : "=&v"(d), "=&v"(e) : "v"(v.x), "v"(v.y), "v"(v.z), "v"(v.w), "s"(epoch), "i"(N) : "memory");
  return __all(d == 0u);
}
DEV void poll1(cptr base, unsigned off, unsigned epoch) {
#pragma unroll 1
  for (unsigned spins = 0; spins < SPIN_CAP; ++spins) {
    unsigned v = ld32a(base, off);
    if (wait_eq1<0>(v, epoch)) break;
    __builtin_amdgcn_s_sleep(6);
  }
}
DEV void poll4(cptr base, unsigned off, unsigned epoch) {
#pragma unroll 1
  for (unsigned spins = 0; spins < SPIN_CAP; ++spins) {
    v4u v = ld128a(base, off);
    if (wait_eq4<0>(v, epoch)) break;
    __builtin_amdgcn_s_sleep(6);
  }
}
DEV void flag_set(const void* sbase, unsigned voff, unsigned epoch) {
  asm volatile("global_atomic_swap %0, %1, %2 sc1" ::"v"(voff), "v"(epoch), "s"(sbase) : "memory");
}
DEV void poll_atomic(cptr base, unsigned off, unsigned long long mask, unsigned epoch) {
#pragma unroll 1
  for (unsigned spins = 0; spins < SPIN_CAP; ++spins) {
    const unsigned v = atom_rd(base, off, mask, epoch);
    if (wait_eq1<0>(v, epoch)) break;
    __builtin_amdgcn_s_sleep(2);
  }
}
template <int N> DEV unsigned epoch_fin(unsigned ev) {
  unsigned e;
  asm volatile("s_waitcnt vmcnt(%2)\n\tv_readfirstlane_b32 %0, %1\n\ts_nop 4" : "=s"(e) : "v"(ev), "i"(N) : "memory");
  return e * 2654435761u + 0x9E3779B9u;
}

DEV void conv_final(__amdgpu_buffer_rsrc_t rs, unsigned db, int ch, int chunk, v4u kvr, float dD) {
  const unsigned a = db + chunk * 32;
  f32x4 p0 = __builtin_bit_cast(f32x4, lds128(a)), p1 = __builtin_bit_cast(f32x4, lds128(a + 16));
  f32x4 q0 = __builtin_bit_cast(f32x4, lds128(a + 8192)), q1 = __builtin_bit_cast(f32x4, lds128(a + 8192 + 16));
  p0 += q0; p1 += q1;
  p0 += f32x4{bf_hi(kvr.w), bf_lo(kvr.w), bf_hi(kvr.z), bf_lo(kvr.z)} * dD;
  p1 += f32x4{bf_hi(kvr.y), bf_lo(kvr.y), bf_hi(kvr.x), bf_lo(kvr.x)} * dD;
  const v4u o = {pk_bf(p0[0], p0[1]), pk_bf(p0[2], p0[3]), pk_bf(p1[0], p1[1]), pk_bf(p1[2], p1[3])};
  __builtin_amdgcn_raw_buffer_store_b128(o, rs, WS_YT + ch * 4096 + chunk * 16, 0, AUX_SC1);
}

__global__ void __launch_bounds__(NTHR, 3) fused_kernel(Params p) {
  __shared__ __attribute__((aligned(16))) unsigned char smem[163840];
  const int tid = threadIdx.x, lane = tid & 63;
  const int w = __builtin_amdgcn_readfirstlane(tid >> 6);
  const int b = blockIdx.x, xcd = b & 7, idx = b >> 3;
  const int half = xcd & 1, cti = idx & 7;
  const int rt = 4 * (xcd >> 1) + (idx >> 3);
  const int nfull = 6 - half;
  const bool isfull = cti < nfull;
  const int u0 = 14 * half + (isfull ? 2 * cti : nfull + cti);
  const unsigned l16 = lane * 16;
  { unsigned lb = (unsigned)(size_t)(LAS unsigned char*)smem; asm volatile("" ::"s"(lb)); }
  unsigned stv = 0; (void)stv; unsigned dg0 = 0, dg1 = 0;
  STAMP(0);
  unsigned epoch, epraw;
  asm volatile("s_load_dword %0, %1, 0x0" : "=s"(epraw) : "s"(p.ws + WS_EPOCH) : "memory");
#define EPOCH_FIN() asm volatile("s_waitcnt lgkmcnt(0)\n\ts_mul_i32 %0, %1, 0x9E3779B1\n\ts_add_u32 %0, %0, 0x9E3779B9" : "=s"(epoch) : "s"(epraw) : "memory", "scc")
  const bool kvrole = idx < 20;
  const int kvn = xcd * 20 + idx, kt = kvn / 10, kc = kvn % 10;
  const int wn = xcd * 12 + idx - 20;
  const __amdgpu_buffer_rsrc_t rs = __builtin_amdgcn_make_buffer_rsrc((void*)p.ws, 0, (int)WS_END, 0x00020000);
  const unsigned lcs = isfull ? L_CS : L_STATS + 1024;
#define LATE_CONSTS() \
  int rt_ = rt; asm volatile("" : "+s"(rt_)); \
  const int tt64 = 2 * rt_ + (cti >> 2), c80 = 4 * half + (cti & 3); \
  const int hn = rt_ * 5 + half * 2 + cti - nfull; \
  const int area = w >= 8 ? 2 + ((w >> 1) & 1) : (w >> 1) & 1; \
  const bool convw = isfull ? w >= 8 : (hn < 72 && w >= 4); \
  const int chb = isfull ? 2 * (rt_ * 11 + half * 6 + cti) - 2 : (hn < 72 ? 352 + 4 * hn : 0); \
  const int cch = (isfull && area < 2) ? chb + 2 : chb + area; \
  const unsigned cb = L_AREA0 + area * CH_BYTES; \
  cptr w2s = p.ws + WS_W2T + c80 * (54 * 5 * 1024) + ((w >> 1) & 1) * (27 * 5 * 1024) + (w & 1) * 7168; \
  const unsigned w2d = ((w >> 1) & 1) * 15360 + (w & 1) * 7168; \
  (void)tt64; (void)convw; (void)cch; (void)cb; (void)w2s; (void)w2d; (void)chb; (void)hn;
#define H_DMA() dma4k((cptr)p.sk + (size_t)cch * 4096 + (w & 1) * 4, l16, cb + ((w & 1) ? CH_H1 : CH_H0))
#define R_DMA() dma4ks(p.ws + WS_KVR + (size_t)cch * KVR_ROW + (w & 1) * 2, l16, cb + ((w & 1) ? CH_R0 : CH_R1))
#define CONV_PADS() do { const unsigned h01 = *(kptr)((cptr)p.sk + (size_t)cch * 4096); \
    if (!(w & 1) && lane < 3) sts128(cb + (lane == 0 ? CH_Z1 : lane == 1 ? CH_Z0 : CH_PF), v4u{0u, 0u, 0u, lane == 2 ? h01 : 0u}); } while (0)
#define W2GROUP(g) do { WAITV(8); dma4ks(w2s + (g) * 15360, l16, w2d + wslot(g)); WAITV(8); dma4ks(w2s + (g) * 15360 + 4096, l16, w2d + wslot(g) + 4096); } while (0)
#define FENCE() asm volatile("" ::: "memory")
#define PRIO(x) __builtin_amdgcn_s_setprio(x)

  v2u stq = {0u, 0u};
  if (w < 4) {
    const int L = w;
    PRIO(LPRIO);
    const unsigned bx = (lane >> 3) * 1280, cx = lane & 7;
    const unsigned cwz = (lane & 15) ^ ((lane >> 4) << 2);
    const int cmask = isfull ? 15 : 7;
    unsigned vx[4], vw[4];
#pragma unroll
    for (int j = 0; j < 4; ++j) {
      vx[j] = bx + ((cx ^ (lane >> 3)) << 4) + j * (8 * 1280 - 1024);
      vw[j] = (lane >> 4) * 3456 + (((cwz ^ j) & cmask) << 4) + j * (4 * 3456 - 1024);
    }
    int chunk = (rt + ROTC * cti) % 10;
    cptr xb0 = (cptr)p.x + (size_t)(128 * rt + 32 * L) * 1280;
    cptr wb0 = (cptr)p.w1 + (size_t)(16 * L) * 3456 + 128 * u0;
    unsigned reg = L * 4096, rx = 32768;
#define STAGE_A() dma4v(xb0 + 128 * chunk, vx, reg)
#define STAGE_B() do { dma4v(wb0 + (size_t)chunk * (64 * 3456), vw, reg + 16384); chunk = chunk == 9 ? 0 : chunk + 1; reg ^= rx; rx ^= 98304; } while (0)
    unsigned vo[4];
    const unsigned myfull = W_FULL + 4 * L, vfull = pub_addr(myfull, lane);
    if (kvrole) {
      STAGE_A(); STAGE_B();
      BAR0();
#pragma unroll
      for (int j = 0; j < 4; ++j) vo[j] = bx + ((cx ^ ((4 * L + j) & 7)) << 4) + j * (8 * 1280 - 1024);
      const size_t go = (size_t)(128 * kt + 32 * L) * 1280 + 128 * kc;
      STAGE_A();
      WAITV(4); pubv(vfull, 1);
      STAGE_B();
      EPOCH_FIN();
      WAITV(4); dma4v((cptr)p.k + go, vo, L_P0 + L * 4096);
      WAITV(4); pubv(vfull, 2);
      dma4vm((cptr)p.v + go, vo, L_P0 + 16384 + L * 4096, L == 3 ? 0x00ffffffffffffffull : ~0ull);
      WAITV(4); STAGE_A();
      WAITV(4); pub(W_PL + 4 * L, 1, lane);
    } else {
#pragma unroll
      for (int j = 0; j < 4; ++j) vo[j] = (lane >> 4) * 1280 + ((cwz ^ j) << 4) + j * (4 * 1280 - 1024);
      const int wnc = wn < 90 ? wn : 0;
      const int g0 = L < 3 ? 2 * L : 4;
      cptr p0a = (cptr)p.w2 + (size_t)(96 * (wnc / 5) + 16 * g0) * 1280 + 256 * (wnc % 5);
      dma4v(p0a, vo, L_P0 + g0 * 4096); dma4v(p0a + 16 * 1280, vo, L_P0 + g0 * 4096 + 4096);
      BAR0();
      STAGE_A();
      EPOCH_FIN();
      WAITV(4); pub(W_PL + 4 * L, 1, lane);
      STAGE_B();
      WAITV(4); STAGE_A();
      WAITV(4); pubv(vfull, 1);
      STAGE_B();
      WAITV(4); STAGE_A();
      WAITV(4); pubv(vfull, 2);
    }
    chunk = __builtin_amdgcn_readfirstlane(chunk); reg = __builtin_amdgcn_readfirstlane(reg); rx = __builtin_amdgcn_readfirstlane(rx);
    STAMP(1);
    STAGE_B();
    WAITV(4); STAGE_A();
    WAITV(4); pubv(vfull, 3);
    STAGE_B();
    STAMP(2);
    if (rt < 10) wait4(W_AP, 1);
    {
      unsigned frm = min4u(peek4(W_FREE));
#pragma unroll 1
      for (int s = 1; s <= 6; ++s) {
        if (frm < (unsigned)s) { WAITV(0); pubv(vfull, s + 3); wait4(W_FREE, s); }
        WAITV(4); STAGE_A();
        WAITV(4); pubv(vfull, s + 3);
        STAGE_B();
        frm = min4u(peek4(W_FREE));
      }
    }
    WAITV(0); pubv(vfull, 10);
    STAMP(3);
    STAMP(5);
  } else if (w < 8) {
    const int cw = w - 4;
    const int n = lane & 15, g = lane >> 4, q = (lane & 15) >> 2, pp = lane & 3;
    const int tok0 = isfull ? 64 * (cw & 1) : 32 * cw, fg = isfull ? cw >> 1 : 0;
    BAR0();
    unsigned ax[2], aw[4][2];
#pragma unroll
    for (int u = 0; u < 2; ++u) ax[u] = (tok0 + n) * 128 + ((((4 * u + g) ^ n) & 7) << 4);
#pragma unroll
    for (int mt = 0; mt < 4; ++mt)
#pragma unroll
      for (int t = 0; t < 2; ++t)
        aw[mt][t] = 16384 + (8 * g + 4 * t + q) * 256 + (((2 * (4 * fg + mt) + (pp >> 1)) ^ ((q << 2) | ((2 * g + t) & 3))) << 4) + 8 * (pp & 1);
    f32x4 acc[4][4];
#pragma unroll
    for (int mt = 0; mt < 4; ++mt)
#pragma unroll
      for (int T = 0; T < 4; ++T) acc[mt][T] = f32x4{0.f, 0.f, 0.f, 0.f};
    v4u xf0[4], wf0[4], xf1[4], wf1[4];
    EPOCH_FIN();
    const unsigned vfree = pub_addr(W_FREE + 4 * cw, lane);
#define P1_READ(NT, XF, WF, U) do { \
      { v2u lo = ldstr(aw[0][0] + 8192 * U), hi = ldstr(aw[0][1] + 8192 * U); WF[0] = v4u{lo.x, lo.y, hi.x, hi.y}; } \
      _Pragma("unroll") for (int T = 0; T < NT; ++T) XF[T] = lds128(ax[U] + 2048 * T); \
      _Pragma("unroll") for (int mt = 1; mt < 4; ++mt) { v2u lo = ldstr(aw[mt][0] + 8192 * U), hi = ldstr(aw[mt][1] + 8192 * U); WF[mt] = v4u{lo.x, lo.y, hi.x, hi.y}; } \
    } while (0)
#define P1_MMA(NT, XF, WF) do { \
      _Pragma("unroll") for (int mt = 0; mt < 4; ++mt) _Pragma("unroll") for (int T = 0; T < NT; ++T) acc[mt][T] = mfma(WF[mt], XF[T], acc[mt][T]); \
    } while (0)
#define P1_ILV(NT) do { _Pragma("unroll") for (int i = 0; i < 4 * NT; ++i) { SGB(0x8, 1); SGB(0x100, NT == 4 ? 1 : 2); } SB0(); } while (0)
#define P1_ILV2(NT) do { _Pragma("unroll") for (int i = 0; i < 4 * NT; ++i) { SGB(0x8, 1); SGB(0x180, NT == 4 ? 1 : 2); SGB(0x2, NT == 4 ? 1 : 2); } SB0(); } while (0)
#define P1_LOOP(NT) do { \
      wait4(W_FULL, 1); \
      P1_READ(NT, xf0, wf0, 0); SB0(); \
      _Pragma("unroll 1") for (int s = 0; s < 9; ++s) { \
        const v4u fl = peek4(W_FULL); \
        P1_READ(NT, xf1, wf1, 1); \
        FENCE(); \
        pubv(vfree, s + 1); \
        const unsigned dx = (s & 1) ? 65536u : 32768u; \
        ax[0] ^= dx; ax[1] ^= dx; \
        _Pragma("unroll") for (int mt = 0; mt < 4; ++mt) { aw[mt][0] ^= dx; aw[mt][1] ^= dx; } \
        const unsigned flm = min4u(fl); \
        P1_MMA(NT, xf0, wf0); P1_ILV2(NT); \
        if (s == 0 || s == 2 || s == 5) STAMP(20 + s); \
        if (__builtin_expect(flm < (unsigned)s + 2, 0)) wait4(W_FULL, s + 2); \
        FENCE(); SB0(); \
        P1_READ(NT, xf0, wf0, 0); P1_MMA(NT, xf1, wf1); P1_ILV(NT); \
      } \
      P1_READ(NT, xf1, wf1, 1); \
      FENCE(); \
      pubv(vfree, 10); \
      P1_MMA(NT, xf0, wf0); P1_ILV2(NT); \
      STAMP(29); \
    } while (0)
    PRIO(2);
    if (isfull) { P1_LOOP(4); P1_MMA(4, xf1, wf1); } else { P1_LOOP(2); P1_MMA(2, xf1, wf1); }
    STAMP(4);
    wait2(W_AP + 8, 5);
    f32x4 cs[4];
#pragma unroll
    for (int mt = 0; mt < 4; ++mt) cs[mt] = __builtin_bit_cast(f32x4, lds128(lcs + (fg * 64 + mt * 16 + g * 4) * 4));
    const unsigned ho = WS_HF + ((8 * rt + (tok0 >> 4)) * 54 + 2 * (u0 + fg)) * 1024 + l16;
#pragma unroll
    for (int T = 0; T < 4; ++T) {
      if (T < 2 || isfull) {
        const f32x2 st = __builtin_bit_cast(f32x2, lds64(L_STATS + (tok0 + 16 * T + n) * 8));
#pragma unroll
        for (int pr = 0; pr < 2; ++pr) {
          unsigned o[4];
#pragma unroll
          for (int j = 0; j < 4; ++j) {
            const int mt = 2 * pr + (j >> 1), e = 2 * (j & 1);
            const f32x2 av = {acc[mt][T][e], acc[mt][T][e + 1]}, cv = {cs[mt][e], cs[mt][e + 1]};
            o[j] = pk_bf(silu_z(av * st[0] + cv * st[1]));
          }
          __builtin_amdgcn_raw_buffer_store_b128(v4u{o[0], o[1], o[2], o[3]}, rs, ho, T * (54 * 1024) + pr * 1024, AUX_SC1);
          SB0();
        }
      }
    }
    pubv(vfree, 11);
    STAMP(6);
    WAITV(0);
    if (lane < (isfull ? 2 : 1)) flag_set(p.ws + WS_FH, ((4 * rt + (tok0 >> 5) + lane) * 32 + u0 + fg) * 4, epoch);
    STAMP(7);
  } else {
    const int aw = w - 8;
    PRIO(3);
    if (aw == 0 && lane < 28) {
      const int hn = rt * 5 + half * 2 + cti - nfull;
      const bool cvon = lane < 12 || lane >= 20 || (isfull ? lane >= 16 : hn < 72);
      sts32(L_SYNC + lane * 4, cvon ? 0u : 99u);
    }
    const int srow = 128 * rt + 8 * (half * 8 + cti) + 2 * aw;
    cptr xr = (cptr)p.x + (size_t)srow * 1280;
    const v4u xa = *(const GAS v4u*)(xr + l16), xc = *(const GAS v4u*)(xr + 1280 + l16);
    const unsigned xb = *(const GAS unsigned*)(xr + 1024 + lane * 4), xd = *(const GAS unsigned*)(xr + 2304 + lane * 4);
    const int tg8 = (lane & 7) + 8 * (aw & 1), cpl = (lane >> 3) + 8 * (aw >> 1);
    unsigned halo[2][3] = {{0u, 0u, 0u}, {0u, 0u, 0u}};
    v4u shk4[2] = {{0u, 0u, 0u, 0u}, {0u, 0u, 0u, 0u}};
    unsigned vlast[2] = {0u, 0u};
    if (kvrole) {
      if (tg8 == 15) {
#pragma unroll
        for (int it = 0; it < 2; ++it) vlast[it] = *(const GAS unsigned*)((cptr)p.v + (size_t)(128 * kt + 127) * 1280 + 128 * kc + 4 * cpl + 64 * it);
      }
#pragma unroll
      for (int it = 0; it < 2; ++it) shk4[it] = *(const GAS v4u*)((cptr)p.shk + (size_t)(64 * kc + 2 * cpl + 32 * it) * 8);
      if (tg8 == 0 && kt > 0) {
#pragma unroll
        for (int it = 0; it < 2; ++it)
#pragma unroll
          for (int i = 0; i < 3; ++i) halo[it][i] = *(const GAS unsigned*)((cptr)p.k + (size_t)(128 * kt - 3 + i) * 1280 + 128 * kc + 4 * cpl + 64 * it);
      }
    }
    SB0();
    BAR0();
    PRIO(APRIO);
    EPOCH_FIN();
    const unsigned myap = W_AP + 4 * aw;
    const unsigned ka = L_P0 + tg8 * 1024 + ((((cpl >> 2) ^ tg8) & 7) << 4) + (cpl & 3) * 4;
    const unsigned kp = tg8 ? L_P0 + (tg8 - 1) * 1024 + ((((cpl >> 2) ^ (tg8 - 1)) & 7) << 4) + (cpl & 3) * 4 : ka;
    if (!kvrole) {
      wait4(W_PL, 1);
      if (wn < 90) {
      const int g = lane >> 4, q = (lane & 15) >> 2, pp = lane & 3;
#pragma unroll
      for (int it = 0; it < 2; ++it) {
        const int dt = 8 * (wn % 5) + aw + 4 * it;
#pragma unroll
        for (int u = 0; u < 3; ++u) {
          v2u h2[2];
#pragma unroll
          for (int t = 0; t < 2; ++t)
            h2[t] = ldstr(L_P0 + (32 * u + 16 * t + 4 * g + q) * 256 + (((2 * (aw + 4 * it) + (pp >> 1)) ^ ((q << 2) | (g & 3))) << 4) + 8 * (pp & 1));
          __builtin_amdgcn_raw_buffer_store_b128(v4u{h2[0].x, h2[0].y, h2[1].x, h2[1].y}, rs, WS_W2T + (((dt / 5) * 54 + 3 * (wn / 5) + u) * 5 + dt % 5) * 1024 + l16, 0, AUX_SC1);
        }
      }
    }
      SB0();
    }
    {
      f32x2 a[5] = {bf2(xa.x), bf2(xa.y), bf2(xa.z), bf2(xa.w), bf2(xb)}, c[5] = {bf2(xc.x), bf2(xc.y), bf2(xc.z), bf2(xc.w), bf2(xd)};
      f32x2 sa = (a[0] + a[1]) + (a[2] + a[3]) + a[4], sc = (c[0] + c[1]) + (c[2] + c[3]) + c[4];
      const float ma = wave_sum(sa[0] + sa[1]) * (1.0f / D), mc = wave_sum(sc[0] + sc[1]) * (1.0f / D);
      f32x2 qa = {0.f, 0.f}, qc = {0.f, 0.f};
#pragma unroll
      for (int i = 0; i < 5; ++i) { f32x2 e = a[i] - ma; qa += e * e; f32x2 f = c[i] - mc; qc += f * f; }
      const float va = wave_sum(qa[0] + qa[1]) * (1.0f / D), vc = wave_sum(qc[0] + qc[1]) * (1.0f / D);
      const unsigned vb = pk_bf(bf2(pk_bf(va, vc)) + 1e-5f);
      const v4u o = {pk_bf(ma, __builtin_amdgcn_sqrtf(bf_lo(vb))), epoch, pk_bf(mc, __builtin_amdgcn_sqrtf(bf_hi(vb))), epoch};
      if (lane < 2) __builtin_amdgcn_raw_buffer_store_b64(lane ? v2u{o.z, o.w} : v2u{o.x, o.y}, rs, WS_STATS + (srow + lane) * 8, 0, AUX_SC1);
    }
    if (!kvrole) {
      WAITV(1);
      pub(W_MK + 4 * aw, 1, lane);
      if (aw == 0) { wait4(W_MK, 1); if (lane == 0) flag_set(p.ws + WS_FW, wn * 4, epoch); }
    }
    if (rt < 10) {
      wait4(W_FULL, 1);
      const int g = lane >> 4, q = (lane & 15) >> 2, pp = lane & 3;
      float c0 = 0.f, c1 = 0.f, c2 = 0.f, c3 = 0.f;
#pragma unroll
      for (int t = 0; t < 2; ++t) {
        const unsigned rowb = 16384 + (8 * g + 4 * t + q) * 256 + 8 * (pp & 1), swz = (q << 2) | ((2 * g + t) & 3);
        const unsigned a0 = rowb + (((4 * aw + (pp >> 1)) ^ swz) << 4), a1 = rowb + (((4 * aw + 2 + (pp >> 1)) ^ swz) << 4);
#pragma unroll
        for (int u = 0; u < 2; ++u) {
          v2u r0 = ldstr(a0 + 8192 * u), r1 = ldstr(a1 + 8192 * u);
          c0 = dot2ones(r0.x, c0); c1 = dot2ones(r1.x, c1); c2 = dot2ones(r0.y, c2); c3 = dot2ones(r1.y, c3);
        }
      }
      FENCE();
      pub(myap, 1, lane);
      c0 = xsum16_32(c0 + c2); c1 = xsum16_32(c1 + c3);
      if (lane < 32)
        __builtin_amdgcn_raw_buffer_store_b64(v2u{asu(lane < 16 ? c0 : c1), epoch}, rs, WS_CSP + (rt * 1792 + 64 * u0 + 32 * (isfull ? aw : aw & 1) + lane) * 8, 0, AUX_SC1);
      SB0();
    }
    STAMP(6);
    wait4(W_PL, 1);
    STAMP(1);
    if (kvrole) {
      unsigned kk[2][11], vv[2][8];
#pragma unroll
      for (int it = 0; it < 2; ++it) {
#pragma unroll
        for (int i = 0; i < 3; ++i) kk[it][i] = lds32((kp ^ (64 * it)) + (5 + i) * 128);
#pragma unroll
        for (int i = 0; i < 8; ++i) { kk[it][3 + i] = lds32((ka ^ (64 * it)) + i * 128); vv[it][i] = lds32((ka ^ (64 * it)) + 16384 + i * 128); }
      }
      const unsigned kvo = WS_KVR + (64 * kc + 2 * cpl) * KVR_ROW + (2040 - 128 * kt - 8 * tg8) * 2;
      v4u ra[2], rb[2];
#pragma unroll
      for (int it = 0; it < 2; ++it) {
        const v4u sh = shk4[it];
        const f32x2 s0 = {bf_lo(sh.x), bf_lo(sh.z)}, s1 = {bf_hi(sh.x), bf_hi(sh.z)}, s2 = {bf_lo(sh.y), bf_lo(sh.w)}, s3 = {bf_hi(sh.y), bf_hi(sh.w)};
        f32x2 kf[11], o[8];
#pragma unroll
        for (int i = 0; i < 11; ++i) kf[i] = bf2(i < 3 ? (tg8 ? kk[it][i] : halo[it][i]) : kk[it][i]);
#pragma unroll
        for (int i = 0; i < 8; ++i) {
          f32x2 s = kf[i] * s3 + kf[i + 1] * s2 + kf[i + 2] * s1 + kf[i + 3] * s0;
          o[i] = bf2(pk_bf(s)) * bf2(i == 7 ? (tg8 == 15 ? vlast[it] : vv[it][i]) : vv[it][i]);
        }
        ra[it] = v4u{pk_bf(o[7][0], o[6][0]), pk_bf(o[5][0], o[4][0]), pk_bf(o[3][0], o[2][0]), pk_bf(o[1][0], o[0][0])};
        rb[it] = v4u{pk_bf(o[7][1], o[6][1]), pk_bf(o[5][1], o[4][1]), pk_bf(o[3][1], o[2][1]), pk_bf(o[1][1], o[0][1])};
        USE(ra[it]); USE(rb[it]);
      }
      STAMP(7);
#pragma unroll
      for (int it = 0; it < 2; ++it) {
        const unsigned o2 = kvo + it * (32 * KVR_ROW);
        __builtin_amdgcn_raw_buffer_store_b128(ra[it], rs, o2, 0, AUX_SC1);
        __builtin_amdgcn_raw_buffer_store_b128(rb[it], rs, o2 + KVR_ROW, 0, AUX_SC1);
        if (kt == 0 && tg8 == 0) {
          __builtin_amdgcn_raw_buffer_store_b128(v4u{0u, 0u, 0u, 0u}, rs, o2 + 16, 0, AUX_SC1);
          __builtin_amdgcn_raw_buffer_store_b128(v4u{0u, 0u, 0u, 0u}, rs, o2 + KVR_ROW + 16, 0, AUX_SC1);
        }
      }
    }
    SB0();
    STAMP(2);
    WAITV(0);
    pub(myap, 3, lane);
    wait4(W_AP, 3);
    if (aw == 0) {
      wait4(W_FREE, 1);
      if (lane == 32 || (kvrole && lane < 32))
        flag_set(p.ws, lane == 32 ? WS_FP0 + (kvrole ? kvn : 160 + wn) * 4 : WS_MBKV + (kc * 32 + lane) * 128 + kt * 4, epoch);
    }
    PRIO(1);
    STAMP(3);
    LATE_CONSTS();
    H_DMA(); CONV_PADS();
    if (aw == 1) {
      if (idx == 31) {
#pragma unroll 1
        for (unsigned spins = 0; spins < SPIN_CAP; ++spins) {
          __builtin_amdgcn_s_sleep(8);
          const v2u f = __builtin_amdgcn_raw_buffer_load_b64(rs, WS_FW + (lane < 48 ? lane : 0) * 8, 0, AUX_SC1);
          if (__all(f.x == epoch && f.y == epoch)) break;
        }
        if (lane < 4) flag_set(p.ws + WS_W2R, (xcd * 4 + lane) * 128, epoch);
      } else {
        wait4(W_FREE, 5);
        poll1(p.ws, WS_W2R + (xcd * 4 + (idx & 3)) * 128, epoch);
      }
      pub(myap, 4, lane);
    }
    STAMP(19);
    if (aw < 2) {
      if (aw == 0) {
        wait4(W_FREE, 6);
        poll1(p.ws, WS_MBKV + ((chb + 3) >> 1) * 128 + (lane & 15) * 4, epoch);
        pub(myap, 4, lane);
      }
      wait4(W_FREE, 7);
    } else {
      wait4(W_FREE, 6);
      v4u gr[10];
      const unsigned gb = aw == 2 ? WS_STATS + 128 * rt * 8 + l16 : WS_CSP + 64 * u0 * 8 + (isfull ? l16 : (l16 & 511));
#pragma unroll 1
      for (unsigned spins = 0; spins < SPIN_CAP; ++spins) {
        gr[0] = __builtin_amdgcn_raw_buffer_load_b128(rs, gb, 0, AUX_SC1);
        unsigned bad = 0;
        if (aw == 3) {
#pragma unroll
          for (int i = 1; i < 10; ++i) gr[i] = __builtin_amdgcn_raw_buffer_load_b128(rs, gb, i * (1792 * 8), AUX_SC1);
          float c0 = asf(gr[0].x), c1 = asf(gr[0].z);
#pragma unroll
          for (int i = 1; i < 10; ++i) { bad |= (gr[i].y ^ epoch) | (gr[i].w ^ epoch); c0 += asf(gr[i].x); c1 += asf(gr[i].z); }
          gr[0].x = asu(c0); gr[0].z = asu(c1);
        }
        bad |= (gr[0].y ^ epoch) | (gr[0].w ^ epoch);
        if (__all(bad == 0u)) break;
        __builtin_amdgcn_s_sleep(4);
      }
      stq = v2u{gr[0].x, gr[0].z};
      wait4(W_FREE, 8);
      if (aw == 2) {
        const float r0 = __builtin_amdgcn_rcpf(bf_hi(gr[0].x)) * LOG2E, r1 = __builtin_amdgcn_rcpf(bf_hi(gr[0].z)) * LOG2E;
        sts128(L_STATS + l16, __builtin_bit_cast(v4u, f32x4{-r0, bf_lo(gr[0].x) * r0, -r1, bf_lo(gr[0].z) * r1}));
      } else if (isfull || lane < 32) sts64(lcs + lane * 8, v2u{gr[0].x, gr[0].z});
      FENCE();
      pub(myap, 5, lane);
    }
    STAMP(4);
    wait1(W_AP, 4);
    R_DMA();
    WAITV(0);
    STAMP(5);
  }
  LATE_CONSTS();
  STAMP(8);
  if (convw) {
    const int rh = w & 1;
    const unsigned cvp = W_CV + area * 8, mycv = cvp + rh * 4;
    pub(mycv, 1, lane);
    const int i16 = lane & 15, kg = lane >> 4;
    const unsigned rbase = cb + ((i16 & 1) ? CH_R1 : CH_R0);
    unsigned va = rbase + 4032 - 3584 - 64 * (i16 >> 1) + 16 * kg + 256 * rh;
    const unsigned vamax = rbase + 4096;
    const unsigned hbase = cb + ((i16 & 1) ? CH_H0 : CH_H1);
    unsigned vb = hbase + 4 * i16 - ((i16 & 1) ? 60 : 64) + 16 * kg + 256 * rh;
    const unsigned vbmin = hbase - 16;
    USE(va); USE(vb);
    f32x4 acc[8];
#pragma unroll
    for (int i = 0; i < 8; ++i) acc[i] = f32x4{0.f, 0.f, 0.f, 0.f};
    const float dD = bf_lo(*(kptr)((cptr)p.sD + (cch >> 1) * 4) >> ((cch & 1) * 16));
    if (w >= 8) { wait2(cvp, 1); wait4(W_FREE, 11); } else wait4(W_FULL, 12);
    PRIO(3);
#define LDA_(i, o) lds128(va + 3584 - 512 * (i) + (o))
#define LDB_(dst, i, o) do { v2u lo = lds64v(vb + 512 * (i) + (o)), hi = lds64v(vb + 512 * (i) + (o) + 8); dst = v4u{lo.x, lo.y, hi.x, hi.y}; } while (0)
#define LDP(i, o) do { A[i] = LDA_(i, o); LDB_(B[i], i, o); } while (0)
#define STEP(m) do { _Pragma("unroll") for (int o = 0; o < (m); ++o) if ((m) + o <= 7) acc[(m) + o] = mfma(A[m], B[o], acc[(m) + o]); \
      if (2 * (m) <= 7) acc[2 * (m)] = mfma(A[m], B[m], acc[2 * (m)]); \
      _Pragma("unroll") for (int o = 0; o < (m); ++o) if ((m) + o <= 7) acc[(m) + o] = mfma(A[o], B[m], acc[(m) + o]); } while (0)
#define ILV(n) do { _Pragma("unroll") for (int i_ = 0; i_ < (n); ++i_) { SGB(0x8, 1); SGB(0x100, 1); } SGB(0x8, 16); SB0(); } while (0)
    v4u A[8], B[8];
    {
      A[0] = lds128(min(va + 3584, vamax));
      v2u lo = lds64v(max(vb, vbmin)), hi = lds64v(max(vb + 8, vbmin));
      B[0] = v4u{lo.x, lo.y, hi.x, hi.y};
      LDP(1, 0); LDP(2, 0); LDP(3, 0);
    }
    SB0();
#pragma unroll 1
    for (int r = 0; r < 4; ++r) {
      v4u nA0, nB0, nA1, nB1;
      LDP(4, 0); STEP(0); STEP(1); ILV(3);
      LDP(5, 0); STEP(2); ILV(3);
      LDP(6, 0); LDP(7, 0); STEP(3); ILV(6);
      nA0 = lds128(min(va + 3584 + 64, vamax)); LDB_(nB0, 0, 64); nA1 = LDA_(1, 64); LDB_(nB1, 1, 64); STEP(4); ILV(6);
      LDP(3, 64); STEP(5); ILV(3);
      LDP(2, 64); STEP(6); STEP(7); ILV(3);
      A[0] = nA0; B[0] = nB0; A[1] = nA1; B[1] = nB1;
      va += 64; vb += 64; USE(va); USE(vb);
    }
    const int chunk = 128 * rh + lane;
    const v4u kva = lds128(cb + CH_R1 + (255 - chunk) * 16), kvb = lds128(cb + CH_R1 + (191 - chunk) * 16);
    STAMP(9);
    PRIO(3);
    FENCE();
    pub(mycv, 2, lane); wait2(cvp, 2);
    {
      const unsigned a = cb + rh * 8192 + (64 * (lane >> 4) + 2 * (lane & 15)) * 4;
#pragma unroll
      for (int i = 0; i < 8; ++i) {
        sts64(a + 1024 * i, __builtin_bit_cast(v2u, f32x2{acc[i][0], acc[i][1]}));
        sts64(a + 1024 * i + 128, __builtin_bit_cast(v2u, f32x2{acc[i][2], acc[i][3]}));
      }
    }
    FENCE();
    pub(mycv, 3, lane); wait2(cvp, 3);
    conv_final(rs, cb, cch, chunk, kva, dD);
    conv_final(rs, cb, cch, chunk + 64, kvb, dD);
    FENCE();
    pub(mycv, 4, lane);
    if (w >= 8) { WAITV(0); if (lane == 0) flag_set(p.ws + WS_FY, (rh * 640 + cch) * 4, epoch); }
  }
  STAMP(10);
  if (w < 4) {
    const unsigned vfull = pub_addr(W_FULL + 4 * w, lane);
    if (!isfull) {
      wait4(W_FREE, 8);
      H_DMA(); CONV_PADS();
    }
    wait1(W_AP + 4, 4);
    v4u RG[4][8];
#define RG_LD(i, g) do { _Pragma("unroll") for (int j = 0; j < 8; ++j) { if ((j & 3) == 0) WAITV(8); \
      RG[i][j] = __builtin_amdgcn_raw_buffer_load_b128(rs, (unsigned)(w2s - p.ws) + l16, (g) * 15360 + j * 1024, AUX_SC1); } SB0(); } while (0)
#define RG_ST(i, g) do { wait4(W_FREE, 21 + (g) - 4); unsigned a_ = w2d + wslot(g) + l16; USE(a_); \
      _Pragma("unroll") for (int j = 0; j < 8; ++j) sts128(a_ + j * 1024, RG[i][j]); FENCE(); pubv(vfull, 21 + (g)); } while (0)
    wait4(W_FREE, 8);
    W2GROUP(2);
    if (isfull) W2GROUP(3); else { wait1(W_AP, 4); R_DMA(); }
    wait4(W_FREE, 9); W2GROUP(0);
    wait4(W_FREE, 10); W2GROUP(1);
    pubv(vfull, 12);
    RG_LD(0, 4);
    WAITV(8); pubv(vfull, isfull ? 24 : 23);
    RG_LD(1, 5); RG_LD(2, 6); RG_LD(3, 7);
    STAMP(11);
    if (!isfull) { wait4(W_CV, 4); W2GROUP(3); WAITV(0); pubv(vfull, 24); }
    PRIO(3);
    RG_ST(0, 4);
    RG_LD(0, 8);
    RG_ST(1, 5); RG_ST(2, 6); RG_ST(3, 7); RG_ST(0, 8);
    if (b == 0 && w == 3) {
      poll4(p.ws + WS_FP0, l16, epoch);
      if (lane == 0) __builtin_amdgcn_raw_buffer_store_b32(epoch, rs, WS_EPOCH, 0, AUX_SC1);
    }
    PRIO(0);
  } else if (w < 8) {
    const int cw = w - 4, tp = cw & 1, kh = cw >> 1;
    const unsigned vfree = pub_addr(W_FREE + 4 * cw, lane);
    const unsigned hoff = WS_HF + ((4 * tt64 + 2 * tp) * 54 + 27 * kh) * 1024 + l16;
    constexpr int HW = HWIN;
    v4u hf[27][2];
    f32x4 acc[2][5];
#pragma unroll
    for (int i = 0; i < 5; ++i) acc[0][i] = acc[1][i] = f32x4{0.f, 0.f, 0.f, 0.f};
    unsigned wa[2] = {kh * 15360 + l16, kh * 15360 + l16 + 65536};
    USE(wa[0]); USE(wa[1]);
    PRIO(2);
    {
      const unsigned fho = ((2 * tt64 + tp) * 32 + 13 * kh + lane) * 4;
      const unsigned v0 = atom_rd(p.ws + WS_FH, fho, 0x3fffull, epoch);
      const bool ok0 = wait_eq1<0>(v0, epoch);
      if (convw && lane == 0) flag_set(p.ws + WS_FY, ((w & 1) * 640 + cch) * 4, epoch);
      if (!ok0) poll_atomic(p.ws + WS_FH, fho, 0x3fffull, epoch);
    }
#define H_LD(i) do { hf[i][0] = __builtin_amdgcn_raw_buffer_load_b128(rs, hoff, (i) * 1024, AUX_SC1); \
      hf[i][1] = __builtin_amdgcn_raw_buffer_load_b128(rs, hoff, (i) * 1024 + 54 * 1024, AUX_SC1); } while (0)
#pragma unroll
    for (int i = 0; i < HW; ++i) H_LD(i);
    STAMP(11);
    v4u a0[5], a1[5];
    v4u fl;
    unsigned flm = 0;
#define P3_AD(ks, i) (wa[wslot((ks) / 3) >> 16] + (wslot((ks) / 3) & 65535) + (((ks) % 3) * 5 + (i)) * 1024)
#define P3_STEP(A, N, ks) do { \
      if (((ks) + 1) % 3 == 0 && (ks) < 26) { if (__builtin_expect(flm < 21 + ((ks) + 1) / 3, 0)) wait4(W_FULL, 21 + ((ks) + 1) / 3); FENCE(); } \
      _Pragma("unroll") for (int i = 0; i < 5; ++i) { \
        mfma_ip(acc[0][i], A[i], hf[ks][0]); \
        if ((ks) < 26) N[i] = lds128v(P3_AD((ks) + 1, i)); \
        mfma_ip(acc[1][i], A[i], hf[ks][1]); \
        if (i == 1 && (ks) % 3 == 0 && (ks) < 24) fl = peek4(W_FULL); \
        if (i == 3 && (ks) % 3 == 1 && (ks) < 24) flm = min4u(fl); \
        if (i == 1 && (ks) == 25) fl = peek4(W_FREE); \
      } \
      if (((ks) + 1) % 3 == 2 && (ks) < 25) pubv(vfree, 21 + ((ks) + 1) / 3); \
      SB0(); \
      if ((ks) + HW < 27) H_LD((ks) + HW); \
      SB0(); \
    } while (0)
    wait4(W_FULL, 21);
#pragma unroll
    for (int i = 0; i < 5; ++i) a0[i] = lds128v(P3_AD(0, i));
    SB0();
#pragma unroll
    for (int ks = 0; ks < 26; ks += 2) {
      P3_STEP(a0, a1, ks);
      P3_STEP(a1, a0, ks + 1);
      if (ks == 2) STAMP(12);
      if (ks == 12) STAMP(13);
    }
    P3_STEP(a0, a1, 26);
    const v4u fr = fl;
    asm volatile("s_nop 7\n\ts_nop 7" ::: "memory");
    STAMP(14);
    if (min4u(fr) < 27) wait4(W_FREE, 27);
    FENCE();
    {
      const unsigned oa = L_O + kh * (64 * OROW) + (32 * tp + (lane & 15)) * OROW + (lane >> 4) * 16;
#pragma unroll
      for (int i = 0; i < 5; ++i) { sts128(oa + i * 64, __builtin_bit_cast(v4u, acc[0][i])); sts128(oa + 16 * OROW + i * 64, __builtin_bit_cast(v4u, acc[1][i])); }
    }
    PRIO(0);
  } else {
    const int aw = w - 8;
    PRIO(1);
    wait4(W_CV + 16, 4);
    if (!convw) { pub(W_AP + 4 * aw, 6, lane); wait4(W_AP, 6); }
    const int npc = aw < 3 ? 3 : 1;
#pragma unroll 1
    for (int i = 0; i < npc; ++i) {
      const int it = (3 * aw + i) * 64 + lane;
      dma16((cptr)p.x + (size_t)(64 * tt64) * 1280 + c80 * 160, (it / 10) * 1280 + (it % 10) * 16, L_X + (3 * aw + i) * 1024);
    }
    if (aw == 2) sts64(L_XN + lane * 8, stq);
    STAMP(11);
    wait4(W_FREE, 23);
    poll_atomic(p.ws + WS_FY, ((tt64 >> 4) * 640 + c80 * 80 + aw * 24 + lane) * 4, aw < 3 ? 0xffffffull : 0xffull, epoch);
    STAMP(12);
#pragma unroll 1
    for (int i = 0; i < npc; ++i) {
      const int pc = 3 * aw + i;
      dma16s(p.ws + WS_YT + (size_t)(c80 * 80 + 8 * pc) * 4096 + tt64 * 128, (lane >> 3) * 4096 + (((lane ^ pc) & 7) << 4), L_Y + pc * 1024);
    }
    WAITV(0);
    PRIO(0);
  }
  STAMP(15);
  BAR0();
  STAMP(16);
  if (tid < 640) {
    const int ftok = tid / 10, fc8 = tid % 10;
    const unsigned oa = L_O + ftok * OROW + fc8 * 32;
    f32x4 o0 = __builtin_bit_cast(f32x4, lds128(oa)) + __builtin_bit_cast(f32x4, lds128(oa + 64 * OROW));
    f32x4 o1 = __builtin_bit_cast(f32x4, lds128(oa + 16)) + __builtin_bit_cast(f32x4, lds128(oa + 64 * OROW + 16));
    const v4u xv = lds128(L_X + tid * 16);
    const unsigned fs = lds32(L_XN + (64 * (tt64 & 1) + ftok) * 4);
    const float fm = bf_lo(fs), fr = __builtin_amdgcn_rcpf(bf_hi(fs));
    const unsigned ya = L_Y + fc8 * 1024 + ((((ftok >> 3) ^ fc8) & 7) << 4) + (ftok & 7) * 2;
    f32x2 o[4] = {{o0[0], o0[1]}, {o0[2], o0[3]}, {o1[0], o1[1]}, {o1[2], o1[3]}};
    unsigned xw[4] = {xv.x, xv.y, xv.z, xv.w}, rw[4];
#pragma unroll
    for (int j = 0; j < 4; ++j) {
      const f32x2 y = {bf_lo(*(const LAS u16*)(size_t)(ya + 256 * j)), bf_lo(*(const LAS u16*)(size_t)(ya + 256 * j + 128))};
      const f32x2 gb = silu_z(o[j] * -LOG2E);
      const f32x2 xf = bf2(xw[j]);
      rw[j] = pk_bf(xf + bf2(pk_bf((xf - fm) * fr)) * y * gb);
    }
    *(GAS v4u*)((unsigned char*)p.out + (size_t)(64 * tt64 + ftok) * 1280 + c80 * 160 + fc8 * 16) = v4u{rw[0], rw[1], rw[2], rw[3]};
  }
  STAMP(17);
  if (w == DIAGW) { const unsigned tend = 4000u + 10u * (dg1 - dg0); while ((unsigned)__builtin_amdgcn_s_memrealtime() - dg0 < tend) __builtin_amdgcn_s_sleep(4); }
}

extern "C" void kernel_launch(void* const* d_in, const int* in_sizes, int n_in, void* d_out, int out_size, void* d_ws, size_t ws_size,
                              hipStream_t stream) {
  Params p;
  p.x = (const u16*)d_in[0]; p.k = (const u16*)d_in[1]; p.v = (const u16*)d_in[2];
  p.shk = (const u16*)d_in[3]; p.sk = (const u16*)d_in[4]; p.sD = (const u16*)d_in[5];
  p.w1 = (const u16*)d_in[7]; p.w2 = (const u16*)d_in[8];
  p.out = (u16*)d_out; p.ws = (unsigned char*)d_ws;
  fused_kernel<<<NWG, NTHR, 0, stream>>>(p);
}
```

```cpp
#define OUTAUX 2
#include <hip/hip_runtime.h>
#include <stdint.h>

typedef unsigned short u16;
typedef unsigned v4u __attribute__((ext_vector_type(4)));
typedef unsigned v2u __attribute__((ext_vector_type(2)));
typedef float f32x4 __attribute__((ext_vector_type(4)));
typedef float f32x2 __attribute__((ext_vector_type(2)));
typedef short bf16x8 __attribute__((ext_vector_type(8)));
typedef short v4s __attribute__((ext_vector_type(4)));
typedef __bf16 v2bf __attribute__((ext_vector_type(2)));
#define LAS __attribute__((address_space(3)))
#define GAS __attribute__((address_space(1)))
#define DEV __device__ __forceinline__
typedef const unsigned char* cptr;
typedef const __attribute__((address_space(4))) unsigned* kptr;

constexpr int S = 2048, D = 640, F = 1728;
constexpr int NWG = 256, NTHR = 768;
constexpr unsigned WS_EPOCH = 0;
constexpr unsigned WS_FP0 = 1024;
constexpr unsigned WS_FW = 3072;
constexpr unsigned WS_FH = 4096;
constexpr unsigned WS_FY = 16384;
constexpr unsigned WS_STATS = 32768;
constexpr unsigned KVR_ROW = 4224;
constexpr unsigned WS_KVR = 65536;
constexpr unsigned WS_W2T = WS_KVR + 640 * KVR_ROW;
constexpr unsigned WS_HF = WS_W2T + 8 * 54 * 5 * 1024;
constexpr unsigned WS_YT = WS_HF + 128 * 54 * 1024;
constexpr unsigned WS_CSP = WS_YT + 640 * 4096;
constexpr unsigned WS_MBKV = WS_CSP + 147456;
constexpr unsigned WS_W2R = WS_MBKV + 40960;
constexpr unsigned WS_END = WS_W2R + 4096;
constexpr unsigned WS_DBG = WS_YT + 640 * 4096 + 262144;
constexpr unsigned L_E = 131072;
constexpr unsigned L_STATS = 65536 + 30720;
constexpr unsigned L_CS = 98304 + 30720;
constexpr unsigned CH_R1 = 0, CH_Z1 = 4096, CH_R0 = 4128, CH_Z0 = 8224, CH_H0 = 8240, CH_PF = 12416, CH_H1 = 12432, CH_BYTES = 16528;
constexpr unsigned L_SYNC = 163840 - 128;
constexpr unsigned W_FULL = L_SYNC, W_FREE = L_SYNC + 16, W_AP = L_SYNC + 32, W_CV = L_SYNC + 48, W_PL = L_SYNC + 80, W_MK = L_SYNC + 96;
constexpr unsigned L_AREA0 = L_SYNC - 4 * CH_BYTES;
constexpr unsigned L_P0 = L_E;
constexpr unsigned L_Y = L_E;
constexpr unsigned L_X = L_E + 10240;
constexpr unsigned L_XN = L_E + 20480;
constexpr unsigned L_O = 32768;
constexpr unsigned OROW = 336;
constexpr unsigned SPIN_CAP = 1u << 15;
constexpr int AUX_SC1 = 16;
constexpr unsigned wslot(int g) { return (g & 3) * 32768u; }

DEV float asf(unsigned u) { return __builtin_bit_cast(float, u); }
DEV unsigned asu(float f) { return __builtin_bit_cast(unsigned, f); }
DEV float bf_lo(unsigned u) { return __builtin_bit_cast(float, u << 16); }
DEV float bf_hi(unsigned u) { return __builtin_bit_cast(float, u & 0xffff0000u); }
DEV f32x2 bf2(unsigned u) { return f32x2{bf_lo(u), bf_hi(u)}; }
DEV unsigned pk_bf(float a, float b) { v2bf r = {(__bf16)a, (__bf16)b}; return __builtin_bit_cast(unsigned, r); }
DEV unsigned pk_bf(f32x2 a) { return pk_bf(a[0], a[1]); }
DEV float rbf(float a) { return bf_lo(pk_bf(a, a) & 0xffffu); }
DEV f32x4 mfma(v4u a, v4u b, f32x4 c) {
  return __builtin_amdgcn_mfma_f32_16x16x32_bf16(__builtin_bit_cast(bf16x8, a), __builtin_bit_cast(bf16x8, b), c, 0, 0, 0);
}
DEV void mfma_ip(f32x4& c, v4u a, v4u b) { asm volatile("v_mfma_f32_16x16x32_bf16 %0, %1, %2, %0" : "+v"(c) : "v"(a), "v"(b)); }
DEV v4u lds128v(unsigned a) { return *(const volatile LAS v4u*)(size_t)a; }
constexpr float LOG2E = 1.44269504088896f;
DEV f32x2 silu_z(f32x2 z) {
  f32x2 e = {__builtin_amdgcn_exp2f(z[0]), __builtin_amdgcn_exp2f(z[1])};
  f32x2 d = e * -LOG2E - LOG2E;
  f32x2 r = {__builtin_amdgcn_rcpf(d[0]), __builtin_amdgcn_rcpf(d[1])};
  return z * r;
}
DEV float dot2ones(unsigned u, float acc) {
  return __builtin_amdgcn_fdot2_f32_bf16(__builtin_bit_cast(v2bf, u), __builtin_bit_cast(v2bf, 0x3f803f80u), acc, false);
}

DEV void dma16(const void* sbase, unsigned voff, unsigned ldsoff) {
  asm volatile("s_mov_b32 m0, %2\n\ts_nop 0\n\tglobal_load_lds_dwordx4 %0, %1" ::"v"(voff), "s"(sbase), "s"(ldsoff) : "memory");
}
DEV void dma16s(const void* sbase, unsigned voff, unsigned ldsoff) {
  asm volatile("s_mov_b32 m0, %2\n\ts_nop 0\n\tglobal_load_lds_dwordx4 %0, %1 sc1" ::"v"(voff), "s"(sbase), "s"(ldsoff) : "memory");
}
DEV void dma4k(const void* sbase, unsigned voff, unsigned ldsoff) {
  asm volatile("s_mov_b32 m0, %2\n\ts_nop 0\n\tglobal_load_lds_dwordx4 %0, %1\n\tglobal_load_lds_dwordx4 %0, %1 offset:1024\n\t"
               "global_load_lds_dwordx4 %0, %1 offset:2048\n\tglobal_load_lds_dwordx4 %0, %1 offset:3072" ::"v"(voff), "s"(sbase), "s"(ldsoff) : "memory");
}
DEV void dma4ks(const void* sbase, unsigned voff, unsigned ldsoff) {
  asm volatile("s_mov_b32 m0, %2\n\ts_nop 0\n\tglobal_load_lds_dwordx4 %0, %1 sc1\n\tglobal_load_lds_dwordx4 %0, %1 offset:1024 sc1\n\t"
               "global_load_lds_dwordx4 %0, %1 offset:2048 sc1\n\tglobal_load_lds_dwordx4 %0, %1 offset:3072 sc1" ::"v"(voff), "s"(sbase), "s"(ldsoff) : "memory");
}
DEV void dma4v(const void* sbase, const unsigned (&vo)[4], unsigned ldsoff) {
  asm volatile("s_mov_b32 m0, %5\n\ts_nop 0\n\tglobal_load_lds_dwordx4 %0, %4\n\tglobal_load_lds_dwordx4 %1, %4 offset:1024\n\t"
               "global_load_lds_dwordx4 %2, %4 offset:2048\n\tglobal_load_lds_dwordx4 %3, %4 offset:3072"
               ::"v"(vo[0]), "v"(vo[1]), "v"(vo[2]), "v"(vo[3]), "s"(sbase), "s"(ldsoff) : "memory");
}
DEV void dma4vm(const void* sbase, const unsigned (&vo)[4], unsigned ldsoff, unsigned long long lastmask) {
  asm volatile("s_mov_b32 m0, %5\n\ts_nop 0\n\tglobal_load_lds_dwordx4 %0, %4\n\tglobal_load_lds_dwordx4 %1, %4 offset:1024\n\t"
               "global_load_lds_dwordx4 %2, %4 offset:2048\n\ts_mov_b64 exec, %6\n\tglobal_load_lds_dwordx4 %3, %4 offset:3072\n\ts_mov_b64 exec, -1"
               ::"v"(vo[0]), "v"(vo[1]), "v"(vo[2]), "v"(vo[3]), "s"(sbase), "s"(ldsoff), "s"(lastmask) : "memory");
}
DEV void touch(const void* sbase, unsigned voff) {
  asm volatile("s_mov_b32 m0, 0x28000\n\ts_nop 0\n\tglobal_load_lds_dword %0, %1" ::"v"(voff), "s"(sbase) : "memory");
}
DEV v4u ld128a(const void* sbase, unsigned voff) { v4u r; asm volatile("global_load_dwordx4 %0, %1, %2 sc1" : "=v"(r) : "v"(voff), "s"(sbase) : "memory"); return r; }
DEV v2u ld64a(const void* sbase, unsigned voff) { v2u r; asm volatile("global_load_dwordx2 %0, %1, %2 sc1" : "=v"(r) : "v"(voff), "s"(sbase) : "memory"); return r; }
DEV unsigned atom_rd(const void* sbase, unsigned voff, unsigned long long mask, unsigned dflt) {
  unsigned r; unsigned long long sv;
  asm volatile("v_mov_b32 %0, %6\n\ts_mov_b64 %1, exec\n\ts_mov_b64 exec, %5\n\tglobal_atomic_or %0, %2, %3, %4 sc0 sc1\n\ts_mov_b64 exec, %1"
               : "=&v"(r), "=&s"(sv) : "v"(voff), "v"(0u), "s"(sbase), "s"(mask), "s"(dflt) : "memory");
  return r;
}
DEV unsigned ld32a(const void* sbase, unsigned voff) { unsigned r; asm volatile("global_load_dword %0, %1, %2 sc1" : "=v"(r) : "v"(voff), "s"(sbase) : "memory"); return r; }
#define WAITV(n) asm volatile("s_waitcnt vmcnt(" #n ")" ::: "memory")
#define USE(x) asm volatile("" : "+v"(x))
#define BAR0() do { asm volatile("s_waitcnt lgkmcnt(0)" ::: "memory"); __builtin_amdgcn_s_barrier(); asm volatile("" ::: "memory"); } while (0)
#define SGB(m, n) __builtin_amdgcn_sched_group_barrier(m, n, 0)
#define SB0() __builtin_amdgcn_sched_barrier(0)

DEV v4u lds128(unsigned a) { return *(const LAS v4u*)(size_t)a; }
DEV v2u lds64(unsigned a) { return *(const LAS v2u*)(size_t)a; }
DEV v2u lds64v(unsigned a) { return *(const volatile LAS v2u*)(size_t)a; }
DEV unsigned lds32(unsigned a) { return *(const LAS unsigned*)(size_t)a; }
DEV void sts128(unsigned a, v4u v) { *(LAS v4u*)(size_t)a = v; }
DEV void sts64(unsigned a, v2u v) { *(LAS v2u*)(size_t)a = v; }
DEV void sts32(unsigned a, unsigned v) { *(LAS unsigned*)(size_t)a = v; }
DEV void sts16(unsigned a, unsigned v) { *(LAS u16*)(size_t)a = (u16)v; }
DEV v2u ldstr(unsigned a) { return __builtin_bit_cast(v2u, __builtin_amdgcn_ds_read_tr16_b64_v4i16((LAS v4s*)(size_t)a)); }

#define SLP 2
DEV void pub(unsigned a, unsigned v, int lane) { if (lane == 0) *(volatile LAS unsigned*)(size_t)a = v; }
DEV unsigned pub_addr(unsigned a, int lane) { unsigned r = lane == 0 ? a : 0x30000u + ((a + lane * 4) & 255); asm volatile("" : "+v"(r)); return r; }
DEV void pubv(unsigned va, unsigned v) { *(volatile LAS unsigned*)(size_t)va = v; }
DEV unsigned min4u(v4u v) { return (unsigned)__builtin_amdgcn_readfirstlane(min(min(v.x, v.y), min(v.z, v.w))); }
DEV v4u peek4(unsigned a) { return *(const volatile LAS v4u*)(size_t)a; }
DEV bool ge4(v4u v, unsigned t) { return (unsigned)__builtin_amdgcn_readfirstlane(min(min(v.x, v.y), min(v.z, v.w))) >= t; }
DEV void wait4(unsigned a, unsigned t) {
#pragma unroll 1
  for (unsigned i = 0; i < (1u << 22); ++i) { if (ge4(peek4(a), t)) break; __builtin_amdgcn_s_sleep(SLP); }
  asm volatile("" ::: "memory");
}
DEV void wait2(unsigned a, unsigned t) {
#pragma unroll 1
  for (unsigned i = 0; i < (1u << 22); ++i) {
    v2u v = *(const volatile LAS v2u*)(size_t)a;
    if ((unsigned)__builtin_amdgcn_readfirstlane(min(v.x, v.y)) >= t) break;
    __builtin_amdgcn_s_sleep(1);
  }
  asm volatile("" ::: "memory");
}
DEV void wait1(unsigned a, unsigned t) {
#pragma unroll 1
  for (unsigned i = 0; i < (1u << 22); ++i) {
    if ((unsigned)__builtin_amdgcn_readfirstlane(*(const volatile LAS unsigned*)(size_t)a) >= t) break;
    __builtin_amdgcn_s_sleep(1);
  }
  asm volatile("" ::: "memory");
}
DEV float xsum16_32(float v) {
  float a = v, c = v;
  asm volatile("s_nop 1\n\tv_permlane16_swap_b32 %0, %1\n\ts_nop 0" : "+v"(a), "+v"(c));
  a += c; c = a;
  asm volatile("s_nop 1\n\tv_permlane32_swap_b32 %0, %1\n\ts_nop 0" : "+v"(a), "+v"(c));
  return a + c;
}
#define WAITL() asm volatile("s_waitcnt lgkmcnt(0)" ::: "memory")
template <int CTRL, int RM> DEV float dpp_add(float v) {
  return v + __builtin_bit_cast(float, __builtin_amdgcn_update_dpp(0, __builtin_bit_cast(int, v), CTRL, RM, 0xf, false));
}
DEV float wave_sum(float v) {
  v = dpp_add<0x111, 0xf>(v); v = dpp_add<0x112, 0xf>(v); v = dpp_add<0x114, 0xf>(v); v = dpp_add<0x118, 0xf>(v);
  v = dpp_add<0x142, 0xa>(v); v = dpp_add<0x143, 0xc>(v);
  return __builtin_bit_cast(float, __builtin_amdgcn_readlane(__builtin_bit_cast(int, v), 63));
}

#define BAR() BAR0()
#define STAMP(i)

#define ROTC 0
#define LPRIO 1
#define HWIN 8
#define APRIO 3
struct Params {
  const u16 *x, *k, *v, *shk, *sk, *sD, *w1, *w2;
  u16* out;
  unsigned char* ws;
};

template <int N> DEV bool wait_eq1(unsigned v, unsigned epoch) {
  unsigned d;
  asm volatile("s_waitcnt vmcnt(%3)\n\tv_xor_b32 %0, %2, %1" : "=v"(d) : "v"(v), "s"(epoch), "i"(N) : "memory");
  return __all(d == 0u);
}
template <int N> DEV bool wait_eq4(v4u v, unsigned epoch) {
  unsigned d, e;
  asm volatile("s_waitcnt vmcnt(%7)\n\tv_xor_b32 %0, %6, %2\n\tv_xor_b32 %1, %6, %3\n\tv_or_b32 %0, %0, %1\n\tv_xor_b32 %1, %6, %4\n\t"
               "v_or_b32 %0, %0, %1\n\tv_xor_b32 %1, %6, %5\n\tv_or_b32 %0, %0, %1"
               : "=&v"(d), "=&v"(e) : "v"(v.x), "v"(v.y), "v"(v.z), "v"(v.w), "s"(epoch), "i"(N) : "memory");
  return __all(d == 0u);
}
DEV void poll1(cptr base, unsigned off, unsigned epoch) {
#pragma unroll 1
  for (unsigned spins = 0; spins < SPIN_CAP; ++spins) {
    unsigned v = ld32a(base, off);
    if (wait_eq1<0>(v, epoch)) break;
    __builtin_amdgcn_s_sleep(6);
  }
}
DEV void poll4(cptr base, unsigned off, unsigned epoch) {
#pragma unroll 1
  for (unsigned spins = 0; spins < SPIN_CAP; ++spins) {
    v4u v = ld128a(base, off);
    if (wait_eq4<0>(v, epoch)) break;
    __builtin_amdgcn_s_sleep(6);
  }
}
DEV void flag_set(const void* sbase, unsigned voff, unsigned epoch) {
  asm volatile("global_atomic_swap %0, %1, %2 sc1" ::"v"(voff), "v"(epoch), "s"(sbase) : "memory");
}
DEV void poll_atomic(cptr base, unsigned off, unsigned long long mask, unsigned epoch) {
#pragma unroll 1
  for (unsigned spins = 0; spins < SPIN_CAP; ++spins) {
    const unsigned v = atom_rd(base, off, mask, epoch);
    if (wait_eq1<0>(v, epoch)) break;
    __builtin_amdgcn_s_sleep(2);
  }
}
template <int N> DEV unsigned epoch_fin(unsigned ev) {
  unsigned e;
  asm volatile("s_waitcnt vmcnt(%2)\n\tv_readfirstlane_b32 %0, %1\n\ts_nop 4" : "=s"(e) : "v"(ev), "i"(N) : "memory");
  return e * 2654435761u + 0x9E3779B9u;
}

DEV void conv_final(__amdgpu_buffer_rsrc_t rs, unsigned db, int ch, int chunk, v4u kvr, float dD) {
  const unsigned a = db + chunk * 32;
  f32x4 p0 = __builtin_bit_cast(f32x4, lds128(a)), p1 = __builtin_bit_cast(f32x4, lds128(a + 16));
  f32x4 q0 = __builtin_bit_cast(f32x4, lds128(a + 8192)), q1 = __builtin_bit_cast(f32x4, lds128(a + 8192 + 16));
  p0 += q0; p1 += q1;
  p0 += f32x4{bf_hi(kvr.w), bf_lo(kvr.w), bf_hi(kvr.z), bf_lo(kvr.z)} * dD;
  p1 += f32x4{bf_hi(kvr.y), bf_lo(kvr.y), bf_hi(kvr.x), bf_lo(kvr.x)} * dD;
  const v4u o = {pk_bf(p0[0], p0[1]), pk_bf(p0[2], p0[3]), pk_bf(p1[0], p1[1]), pk_bf(p1[2], p1[3])};
  __builtin_amdgcn_raw_buffer_store_b128(o, rs, WS_YT + ch * 4096 + chunk * 16, 0, AUX_SC1);
}

__global__ void __launch_bounds__(NTHR, 3) fused_kernel(Params p) {
  __shared__ __attribute__((aligned(16))) unsigned char smem[163840];
  const int tid = threadIdx.x, lane = tid & 63;
  const int w = __builtin_amdgcn_readfirstlane(tid >> 6);
  const int b = blockIdx.x, xcd = b & 7, idx = b >> 3;
  const int half = xcd & 1, cti = idx & 7;
  const int rt = 4 * (xcd >> 1) + (idx >> 3);
  const int nfull = 6 - half;
  const bool isfull = cti < nfull;
  const int u0 = 14 * half + (isfull ? 2 * cti : nfull + cti);
  const unsigned l16 = lane * 16;
  { unsigned lb = (unsigned)(size_t)(LAS unsigned char*)smem; asm volatile("" ::"s"(lb)); }
  unsigned stv = 0; (void)stv;
  STAMP(0);
  unsigned epoch, epraw;
  asm volatile("s_load_dword %0, %1, 0x0" : "=s"(epraw) : "s"(p.ws + WS_EPOCH) : "memory");
#define EPOCH_FIN() asm volatile("s_waitcnt lgkmcnt(0)\n\ts_mul_i32 %0, %1, 0x9E3779B1\n\ts_add_u32 %0, %0, 0x9E3779B9" : "=s"(epoch) : "s"(epraw) : "memory", "scc")
  const bool kvrole = idx < 20;
  const int kvn = xcd * 20 + idx, kt = kvn / 10, kc = kvn % 10;
  const int wn = xcd * 12 + idx - 20;
  const __amdgpu_buffer_rsrc_t rs = __builtin_amdgcn_make_buffer_rsrc((void*)p.ws, 0, (int)WS_END, 0x00020000);
  const unsigned lcs = isfull ? L_CS : L_STATS + 1024;
#define LATE_CONSTS() \
  int rt_ = rt; asm volatile("" : "+s"(rt_)); \
  const int tt64 = 2 * rt_ + (cti >> 2), c80 = 4 * half + (cti & 3); \
  const int hn = rt_ * 5 + half * 2 + cti - nfull; \
  const int area = w >= 8 ? 2 + ((w >> 1) & 1) : (w >> 1) & 1; \
  const bool convw = isfull ? w >= 8 : (hn < 72 && w >= 4); \
  const int chb = isfull ? 2 * (rt_ * 11 + half * 6 + cti) - 2 : (hn < 72 ? 352 + 4 * hn : 0); \
  const int cch = (isfull && area < 2) ? chb + 2 : chb + area; \
  const unsigned cb = L_AREA0 + area * CH_BYTES; \
  cptr w2s = p.ws + WS_W2T + c80 * (54 * 5 * 1024) + ((w >> 1) & 1) * (27 * 5 * 1024) + (w & 1) * 7168; \
  const unsigned w2d = ((w >> 1) & 1) * 15360 + (w & 1) * 7168; \
  (void)tt64; (void)convw; (void)cch; (void)cb; (void)w2s; (void)w2d; (void)chb; (void)hn;
#define H_DMA() dma4k((cptr)p.sk + (size_t)cch * 4096 + (w & 1) * 4, l16, cb + ((w & 1) ? CH_H1 : CH_H0))
#define R_DMA() dma4ks(p.ws + WS_KVR + (size_t)cch * KVR_ROW + (w & 1) * 2, l16, cb + ((w & 1) ? CH_R0 : CH_R1))
#define CONV_PADS() do { const unsigned h01 = *(kptr)((cptr)p.sk + (size_t)cch * 4096); \
    if (!(w & 1) && lane < 3) sts128(cb + (lane == 0 ? CH_Z1 : lane == 1 ? CH_Z0 : CH_PF), v4u{0u, 0u, 0u, lane == 2 ? h01 : 0u}); } while (0)
#define W2GROUP(g) do { WAITV(8); dma4ks(w2s + (g) * 15360, l16, w2d + wslot(g)); WAITV(8); dma4ks(w2s + (g) * 15360 + 4096, l16, w2d + wslot(g) + 4096); } while (0)
#define FENCE() asm volatile("" ::: "memory")
#define PRIO(x) __builtin_amdgcn_s_setprio(x)

  v2u stq = {0u, 0u};
  if (w < 4) {
    const int L = w;
    PRIO(LPRIO);
    const unsigned bx = (lane >> 3) * 1280, cx = lane & 7;
    const unsigned cwz = (lane & 15) ^ ((lane >> 4) << 2);
    const int cmask = isfull ? 15 : 7;
    unsigned vx[4], vw[4];
#pragma unroll
    for (int j = 0; j < 4; ++j) {
      vx[j] = bx + ((cx ^ (lane >> 3)) << 4) + j * (8 * 1280 - 1024);
      vw[j] = (lane >> 4) * 3456 + (((cwz ^ j) & cmask) << 4) + j * (4 * 3456 - 1024);
    }
    int chunk = (rt + ROTC * cti) % 10;
    cptr xb0 = (cptr)p.x + (size_t)(128 * rt + 32 * L) * 1280;
    cptr wb0 = (cptr)p.w1 + (size_t)(16 * L) * 3456 + 128 * u0;
    unsigned reg = L * 4096, rx = 32768;
#define STAGE_A() dma4v(xb0 + 128 * chunk, vx, reg)
#define STAGE_B() do { dma4v(wb0 + (size_t)chunk * (64 * 3456), vw, reg + 16384); chunk = chunk == 9 ? 0 : chunk + 1; reg ^= rx; rx ^= 98304; } while (0)
    unsigned vo[4];
    const unsigned myfull = W_FULL + 4 * L, vfull = pub_addr(myfull, lane);
    if (kvrole) {
      STAGE_A(); STAGE_B();
      BAR0();
#pragma unroll
      for (int j = 0; j < 4; ++j) vo[j] = bx + ((cx ^ ((4 * L + j) & 7)) << 4) + j * (8 * 1280 - 1024);
      const size_t go = (size_t)(128 * kt + 32 * L) * 1280 + 128 * kc;
      STAGE_A();
      WAITV(4); pubv(vfull, 1);
      STAGE_B();
      EPOCH_FIN();
      WAITV(4); dma4v((cptr)p.k + go, vo, L_P0 + L * 4096);
      WAITV(4); pubv(vfull, 2);
      dma4vm((cptr)p.v + go, vo, L_P0 + 16384 + L * 4096, L == 3 ? 0x00ffffffffffffffull : ~0ull);
      WAITV(4); STAGE_A();
      WAITV(4); pub(W_PL + 4 * L, 1, lane);
    } else {
#pragma unroll
      for (int j = 0; j < 4; ++j) vo[j] = (lane >> 4) * 1280 + ((cwz ^ j) << 4) + j * (4 * 1280 - 1024);
      const int wnc = wn < 90 ? wn : 0;
      const int g0 = L < 3 ? 2 * L : 4;
      cptr p0a = (cptr)p.w2 + (size_t)(96 * (wnc / 5) + 16 * g0) * 1280 + 256 * (wnc % 5);
      dma4v(p0a, vo, L_P0 + g0 * 4096); dma4v(p0a + 16 * 1280, vo, L_P0 + g0 * 4096 + 4096);
      BAR0();
      STAGE_A();
      EPOCH_FIN();
      WAITV(4); pub(W_PL + 4 * L, 1, lane);
      STAGE_B();
      WAITV(4); STAGE_A();
      WAITV(4); pubv(vfull, 1);
      STAGE_B();
      WAITV(4); STAGE_A();
      WAITV(4); pubv(vfull, 2);
    }
    chunk = __builtin_amdgcn_readfirstlane(chunk); reg = __builtin_amdgcn_readfirstlane(reg); rx = __builtin_amdgcn_readfirstlane(rx);
    STAMP(1);
    STAGE_B();
    WAITV(4); STAGE_A();
    WAITV(4); pubv(vfull, 3);
    STAGE_B();
    STAMP(2);
    if (rt < 10) wait4(W_AP, 1);
    {
      unsigned frm = min4u(peek4(W_FREE));
#pragma unroll 1
      for (int s = 1; s <= 6; ++s) {
        if (frm < (unsigned)s) { WAITV(0); pubv(vfull, s + 3); wait4(W_FREE, s); }
        WAITV(4); STAGE_A();
        WAITV(4); pubv(vfull, s + 3);
        STAGE_B();
        frm = min4u(peek4(W_FREE));
      }
    }
    WAITV(0); pubv(vfull, 10);
    STAMP(3);
    STAMP(5);
  } else if (w < 8) {
    const int cw = w - 4;
    const int n = lane & 15, g = lane >> 4, q = (lane & 15) >> 2, pp = lane & 3;
    const int tok0 = isfull ? 64 * (cw & 1) : 32 * cw, fg = isfull ? cw >> 1 : 0;
    BAR0();
    unsigned ax[2], aw[4][2];
#pragma unroll
    for (int u = 0; u < 2; ++u) ax[u] = (tok0 + n) * 128 + ((((4 * u + g) ^ n) & 7) << 4);
#pragma unroll
    for (int mt = 0; mt < 4; ++mt)
#pragma unroll
      for (int t = 0; t < 2; ++t)
        aw[mt][t] = 16384 + (8 * g + 4 * t + q) * 256 + (((2 * (4 * fg + mt) + (pp >> 1)) ^ ((q << 2) | ((2 * g + t) & 3))) << 4) + 8 * (pp & 1);
    f32x4 acc[4][4];
#pragma unroll
    for (int mt = 0; mt < 4; ++mt)
#pragma unroll
      for (int T = 0; T < 4; ++T) acc[mt][T] = f32x4{0.f, 0.f, 0.f, 0.f};
    v4u xf0[4], wf0[4], xf1[4], wf1[4];
    EPOCH_FIN();
    const unsigned vfree = pub_addr(W_FREE + 4 * cw, lane);
#define P1_READ(NT, XF, WF, U) do { \
      { v2u lo = ldstr(aw[0][0] + 8192 * U), hi = ldstr(aw[0][1] + 8192 * U); WF[0] = v4u{lo.x, lo.y, hi.x, hi.y}; } \
      _Pragma("unroll") for (int T = 0; T < NT; ++T) XF[T] = lds128(ax[U] + 2048 * T); \
      _Pragma("unroll") for (int mt = 1; mt < 4; ++mt) { v2u lo = ldstr(aw[mt][0] + 8192 * U), hi = ldstr(aw[mt][1] + 8192 * U); WF[mt] = v4u{lo.x, lo.y, hi.x, hi.y}; } \
    } while (0)
#define P1_MMA(NT, XF, WF) do { \
      _Pragma("unroll") for (int mt = 0; mt < 4; ++mt) _Pragma("unroll") for (int T = 0; T < NT; ++T) acc[mt][T] = mfma(WF[mt], XF[T], acc[mt][T]); \
    } while (0)
#define P1_ILV(NT) do { _Pragma("unroll") for (int i = 0; i < 4 * NT; ++i) { SGB(0x8, 1); SGB(0x100, NT == 4 ? 1 : 2); } SB0(); } while (0)
#define P1_ILV2(NT) do { _Pragma("unroll") for (int i = 0; i < 4 * NT; ++i) { SGB(0x8, 1); SGB(0x180, NT == 4 ? 1 : 2); SGB(0x2, NT == 4 ? 1 : 2); } SB0(); } while (0)
#define P1_LOOP(NT) do { \
      wait4(W_FULL, 1); \
      P1_READ(NT, xf0, wf0, 0); SB0(); \
      _Pragma("unroll 1") for (int s = 0; s < 9; ++s) { \
        const v4u fl = peek4(W_FULL); \
        P1_READ(NT, xf1, wf1, 1); \
        FENCE(); \
        pubv(vfree, s + 1); \
        const unsigned dx = (s & 1) ? 65536u : 32768u; \
        ax[0] ^= dx; ax[1] ^= dx; \
        _Pragma("unroll") for (int mt = 0; mt < 4; ++mt) { aw[mt][0] ^= dx; aw[mt][1] ^= dx; } \
        const unsigned flm = min4u(fl); \
        P1_MMA(NT, xf0, wf0); P1_ILV2(NT); \
        if (s == 0 || s == 2 || s == 5) STAMP(20 + s); \
        if (__builtin_expect(flm < (unsigned)s + 2, 0)) wait4(W_FULL, s + 2); \
        FENCE(); SB0(); \
        P1_READ(NT, xf0, wf0, 0); P1_MMA(NT, xf1, wf1); P1_ILV(NT); \
      } \
      P1_READ(NT, xf1, wf1, 1); \
      FENCE(); \
      pubv(vfree, 10); \
      P1_MMA(NT, xf0, wf0); P1_ILV2(NT); \
      STAMP(29); \
    } while (0)
    PRIO(2);
    if (isfull) { P1_LOOP(4); P1_MMA(4, xf1, wf1); } else { P1_LOOP(2); P1_MMA(2, xf1, wf1); }
    STAMP(4);
    wait2(W_AP + 8, 5);
    f32x4 cs[4];
#pragma unroll
    for (int mt = 0; mt < 4; ++mt) cs[mt] = __builtin_bit_cast(f32x4, lds128(lcs + (fg * 64 + mt * 16 + g * 4) * 4));
    const unsigned ho = WS_HF + ((8 * rt + (tok0 >> 4)) * 54 + 2 * (u0 + fg)) * 1024 + l16;
#pragma unroll
    for (int T = 0; T < 4; ++T) {
      if (T < 2 || isfull) {
        const f32x2 st = __builtin_bit_cast(f32x2, lds64(L_STATS + (tok0 + 16 * T + n) * 8));
#pragma unroll
        for (int pr = 0; pr < 2; ++pr) {
          unsigned o[4];
#pragma unroll
          for (int j = 0; j < 4; ++j) {
            const int mt = 2 * pr + (j >> 1), e = 2 * (j & 1);
            const f32x2 av = {acc[mt][T][e], acc[mt][T][e + 1]}, cv = {cs[mt][e], cs[mt][e + 1]};
            o[j] = pk_bf(silu_z(av * st[0] + cv * st[1]));
          }
          __builtin_amdgcn_raw_buffer_store_b128(v4u{o[0], o[1], o[2], o[3]}, rs, ho, T * (54 * 1024) + pr * 1024, AUX_SC1);
          SB0();
        }
      }
    }
    pubv(vfree, 11);
    STAMP(6);
    WAITV(0);
    if (lane < (isfull ? 2 : 1)) flag_set(p.ws + WS_FH, ((4 * rt + (tok0 >> 5) + lane) * 32 + u0 + fg) * 4, epoch);
    STAMP(7);
  } else {
    const int aw = w - 8;
    PRIO(3);
    if (aw == 0 && lane < 28) {
      const int hn = rt * 5 + half * 2 + cti - nfull;
      const bool cvon = lane < 12 || lane >= 20 || (isfull ? lane >= 16 : hn < 72);
      sts32(L_SYNC + lane * 4, cvon ? 0u : 99u);
    }
    const int srow = 128 * rt + 8 * (half * 8 + cti) + 2 * aw;
    cptr xr = (cptr)p.x + (size_t)srow * 1280;
    const v4u xa = *(const GAS v4u*)(xr + l16), xc = *(const GAS v4u*)(xr + 1280 + l16);
    const unsigned xb = *(const GAS unsigned*)(xr + 1024 + lane * 4), xd = *(const GAS unsigned*)(xr + 2304 + lane * 4);
    const int tg8 = (lane & 7) + 8 * (aw & 1), cpl = (lane >> 3) + 8 * (aw >> 1);
    unsigned halo[2][3] = {{0u, 0u, 0u}, {0u, 0u, 0u}};
    v4u shk4[2] = {{0u, 0u, 0u, 0u}, {0u, 0u, 0u, 0u}};
    unsigned vlast[2] = {0u, 0u};
    if (kvrole) {
      if (tg8 == 15) {
#pragma unroll
        for (int it = 0; it < 2; ++it) vlast[it] = *(const GAS unsigned*)((cptr)p.v + (size_t)(128 * kt + 127) * 1280 + 128 * kc + 4 * cpl + 64 * it);
      }
#pragma unroll
      for (int it = 0; it < 2; ++it) shk4[it] = *(const GAS v4u*)((cptr)p.shk + (size_t)(64 * kc + 2 * cpl + 32 * it) * 8);
      if (tg8 == 0 && kt > 0) {
#pragma unroll
        for (int it = 0; it < 2; ++it)
#pragma unroll
          for (int i = 0; i < 3; ++i) halo[it][i] = *(const GAS unsigned*)((cptr)p.k + (size_t)(128 * kt - 3 + i) * 1280 + 128 * kc + 4 * cpl + 64 * it);
      }
    }
    SB0();
    BAR0();
    PRIO(APRIO);
    EPOCH_FIN();
    const unsigned myap = W_AP + 4 * aw;
    const unsigned ka = L_P0 + tg8 * 1024 + ((((cpl >> 2) ^ tg8) & 7) << 4) + (cpl & 3) * 4;
    const unsigned kp = tg8 ? L_P0 + (tg8 - 1) * 1024 + ((((cpl >> 2) ^ (tg8 - 1)) & 7) << 4) + (cpl & 3) * 4 : ka;
    if (!kvrole) {
      wait4(W_PL, 1);
      if (wn < 90) {
      const int g = lane >> 4, q = (lane & 15) >> 2, pp = lane & 3;
#pragma unroll
      for (int it = 0; it < 2; ++it) {
        const int dt = 8 * (wn % 5) + aw + 4 * it;
#pragma unroll
        for (int u = 0; u < 3; ++u) {
          v2u h2[2];
#pragma unroll
          for (int t = 0; t < 2; ++t)
            h2[t] = ldstr(L_P0 + (32 * u + 16 * t + 4 * g + q) * 256 + (((2 * (aw + 4 * it) + (pp >> 1)) ^ ((q << 2) | (g & 3))) << 4) + 8 * (pp & 1));
          __builtin_amdgcn_raw_buffer_store_b128(v4u{h2[0].x, h2[0].y, h2[1].x, h2[1].y}, rs, WS_W2T + (((dt / 5) * 54 + 3 * (wn / 5) + u) * 5 + dt % 5) * 1024 + l16, 0, AUX_SC1);
        }
      }
    }
      SB0();
    }
    {
      f32x2 a[5] = {bf2(xa.x), bf2(xa.y), bf2(xa.z), bf2(xa.w), bf2(xb)}, c[5] = {bf2(xc.x), bf2(xc.y), bf2(xc.z), bf2(xc.w), bf2(xd)};
      f32x2 sa = (a[0] + a[1]) + (a[2] + a[3]) + a[4], sc = (c[0] + c[1]) + (c[2] + c[3]) + c[4];
      const float ma = wave_sum(sa[0] + sa[1]) * (1.0f / D), mc = wave_sum(sc[0] + sc[1]) * (1.0f / D);
      f32x2 qa = {0.f, 0.f}, qc = {0.f, 0.f};
#pragma unroll
      for (int i = 0; i < 5; ++i) { f32x2 e = a[i] - ma; qa += e * e; f32x2 f = c[i] - mc; qc += f * f; }
      const float va = wave_sum(qa[0] + qa[1]) * (1.0f / D), vc = wave_sum(qc[0] + qc[1]) * (1.0f / D);
      const unsigned vb = pk_bf(bf2(pk_bf(va, vc)) + 1e-5f);
      const v4u o = {pk_bf(ma, __builtin_amdgcn_sqrtf(bf_lo(vb))), epoch, pk_bf(mc, __builtin_amdgcn_sqrtf(bf_hi(vb))), epoch};
      if (lane < 2) __builtin_amdgcn_raw_buffer_store_b64(lane ? v2u{o.z, o.w} : v2u{o.x, o.y}, rs, WS_STATS + (srow + lane) * 8, 0, AUX_SC1);
    }
    if (!kvrole) {
      WAITV(1);
      pub(W_MK + 4 * aw, 1, lane);
      if (aw == 0) { wait4(W_MK, 1); if (lane == 0) flag_set(p.ws + WS_FW, wn * 4, epoch); }
    }
    if (rt < 10) {
      wait4(W_FULL, 1);
      const int g = lane >> 4, q = (lane & 15) >> 2, pp = lane & 3;
      float c0 = 0.f, c1 = 0.f, c2 = 0.f, c3 = 0.f;
#pragma unroll
      for (int t = 0; t < 2; ++t) {
        const unsigned rowb = 16384 + (8 * g + 4 * t + q) * 256 + 8 * (pp & 1), swz = (q << 2) | ((2 * g + t) & 3);
        const unsigned a0 = rowb + (((4 * aw + (pp >> 1)) ^ swz) << 4), a1 = rowb + (((4 * aw + 2 + (pp >> 1)) ^ swz) << 4);
#pragma unroll
        for (int u = 0; u < 2; ++u) {
          v2u r0 = ldstr(a0 + 8192 * u), r1 = ldstr(a1 + 8192 * u);
          c0 = dot2ones(r0.x, c0); c1 = dot2ones(r1.x, c1); c2 = dot2ones(r0.y, c2); c3 = dot2ones(r1.y, c3);
        }
      }
      FENCE();
      pub(myap, 1, lane);
      c0 = xsum16_32(c0 + c2); c1 = xsum16_32(c1 + c3);
      if (lane < 32)
        __builtin_amdgcn_raw_buffer_store_b64(v2u{asu(lane < 16 ? c0 : c1), epoch}, rs, WS_CSP + (rt * 1792 + 64 * u0 + 32 * (isfull ? aw : aw & 1) + lane) * 8, 0, AUX_SC1);
      SB0();
    }
    STAMP(6);
    wait4(W_PL, 1);
    STAMP(1);
    if (kvrole) {
      unsigned kk[2][11], vv[2][8];
#pragma unroll
      for (int it = 0; it < 2; ++it) {
#pragma unroll
        for (int i = 0; i < 3; ++i) kk[it][i] = lds32((kp ^ (64 * it)) + (5 + i) * 128);
#pragma unroll
        for (int i = 0; i < 8; ++i) { kk[it][3 + i] = lds32((ka ^ (64 * it)) + i * 128); vv[it][i] = lds32((ka ^ (64 * it)) + 16384 + i * 128); }
      }
      const unsigned kvo = WS_KVR + (64 * kc + 2 * cpl) * KVR_ROW + (2040 - 128 * kt - 8 * tg8) * 2;
      v4u ra[2], rb[2];
#pragma unroll
      for (int it = 0; it < 2; ++it) {
        const v4u sh = shk4[it];
        const f32x2 s0 = {bf_lo(sh.x), bf_lo(sh.z)}, s1 = {bf_hi(sh.x), bf_hi(sh.z)}, s2 = {bf_lo(sh.y), bf_lo(sh.w)}, s3 = {bf_hi(sh.y), bf_hi(sh.w)};
        f32x2 kf[11], o[8];
#pragma unroll
        for (int i = 0; i < 11; ++i) kf[i] = bf2(i < 3 ? (tg8 ? kk[it][i] : halo[it][i]) : kk[it][i]);
#pragma unroll
        for (int i = 0; i < 8; ++i) {
          f32x2 s = kf[i] * s3 + kf[i + 1] * s2 + kf[i + 2] * s1 + kf[i + 3] * s0;
          o[i] = bf2(pk_bf(s)) * bf2(i == 7 ? (tg8 == 15 ? vlast[it] : vv[it][i]) : vv[it][i]);
        }
        ra[it] = v4u{pk_bf(o[7][0], o[6][0]), pk_bf(o[5][0], o[4][0]), pk_bf(o[3][0], o[2][0]), pk_bf(o[1][0], o[0][0])};
        rb[it] = v4u{pk_bf(o[7][1], o[6][1]), pk_bf(o[5][1], o[4][1]), pk_bf(o[3][1], o[2][1]), pk_bf(o[1][1], o[0][1])};
        USE(ra[it]); USE(rb[it]);
      }
      STAMP(7);
#pragma unroll
      for (int it = 0; it < 2; ++it) {
        const unsigned o2 = kvo + it * (32 * KVR_ROW);
        __builtin_amdgcn_raw_buffer_store_b128(ra[it], rs, o2, 0, AUX_SC1);
        __builtin_amdgcn_raw_buffer_store_b128(rb[it], rs, o2 + KVR_ROW, 0, AUX_SC1);
        if (kt == 0 && tg8 == 0) {
          __builtin_amdgcn_raw_buffer_store_b128(v4u{0u, 0u, 0u, 0u}, rs, o2 + 16, 0, AUX_SC1);
          __builtin_amdgcn_raw_buffer_store_b128(v4u{0u, 0u, 0u, 0u}, rs, o2 + KVR_ROW + 16, 0, AUX_SC1);
        }
      }
    }
    SB0();
    STAMP(2);
    WAITV(0);
    pub(myap, 3, lane);
    wait4(W_AP, 3);
    if (aw == 0) {
      wait4(W_FREE, 1);
      if (lane == 32 || (kvrole && lane < 32))
        flag_set(p.ws, lane == 32 ? WS_FP0 + (kvrole ? kvn : 160 + wn) * 4 : WS_MBKV + (kc * 32 + lane) * 128 + kt * 4, epoch);
    }
    PRIO(1);
    STAMP(3);
    LATE_CONSTS();
    H_DMA(); CONV_PADS();
    if (aw == 1) {
      if (idx == 31) {
#pragma unroll 1
        for (unsigned spins = 0; spins < SPIN_CAP; ++spins) {
          __builtin_amdgcn_s_sleep(8);
          const v2u f = __builtin_amdgcn_raw_buffer_load_b64(rs, WS_FW + (lane < 48 ? lane : 0) * 8, 0, AUX_SC1);
          if (__all(f.x == epoch && f.y == epoch)) break;
        }
        if (lane < 4) flag_set(p.ws + WS_W2R, (xcd * 4 + lane) * 128, epoch);
      } else {
        wait4(W_FREE, 5);
        poll1(p.ws, WS_W2R + (xcd * 4 + (idx & 3)) * 128, epoch);
      }
      pub(myap, 4, lane);
    }
    STAMP(19);
    if (aw < 2) {
      if (aw == 0) {
        wait4(W_FREE, 6);
        poll1(p.ws, WS_MBKV + ((chb + 3) >> 1) * 128 + (lane & 15) * 4, epoch);
        pub(myap, 4, lane);
      }
      wait4(W_FREE, 7);
    } else {
      wait4(W_FREE, 6);
      v4u gr[10];
      const unsigned gb = aw == 2 ? WS_STATS + 128 * rt * 8 + l16 : WS_CSP + 64 * u0 * 8 + (isfull ? l16 : (l16 & 511));
#pragma unroll 1
      for (unsigned spins = 0; spins < SPIN_CAP; ++spins) {
        gr[0] = __builtin_amdgcn_raw_buffer_load_b128(rs, gb, 0, AUX_SC1);
        unsigned bad = 0;
        if (aw == 3) {
#pragma unroll
          for (int i = 1; i < 10; ++i) gr[i] = __builtin_amdgcn_raw_buffer_load_b128(rs, gb, i * (1792 * 8), AUX_SC1);
          float c0 = asf(gr[0].x), c1 = asf(gr[0].z);
#pragma unroll
          for (int i = 1; i < 10; ++i) { bad |= (gr[i].y ^ epoch) | (gr[i].w ^ epoch); c0 += asf(gr[i].x); c1 += asf(gr[i].z); }
          gr[0].x = asu(c0); gr[0].z = asu(c1);
        }
        bad |= (gr[0].y ^ epoch) | (gr[0].w ^ epoch);
        if (__all(bad == 0u)) break;
        __builtin_amdgcn_s_sleep(4);
      }
      stq = v2u{gr[0].x, gr[0].z};
      wait4(W_FREE, 8);
      if (aw == 2) {
        const float r0 = __builtin_amdgcn_rcpf(bf_hi(gr[0].x)) * LOG2E, r1 = __builtin_amdgcn_rcpf(bf_hi(gr[0].z)) * LOG2E;
        sts128(L_STATS + l16, __builtin_bit_cast(v4u, f32x4{-r0, bf_lo(gr[0].x) * r0, -r1, bf_lo(gr[0].z) * r1}));
      } else if (isfull || lane < 32) sts64(lcs + lane * 8, v2u{gr[0].x, gr[0].z});
      FENCE();
      pub(myap, 5, lane);
    }
    STAMP(4);
    wait1(W_AP, 4);
    R_DMA();
    WAITV(0);
    STAMP(5);
  }
  LATE_CONSTS();
  STAMP(8);
  if (convw) {
    const int rh = w & 1;
    const unsigned cvp = W_CV + area * 8, mycv = cvp + rh * 4;
    pub(mycv, 1, lane);
    const int i16 = lane & 15, kg = lane >> 4;
    const unsigned rbase = cb + ((i16 & 1) ? CH_R1 : CH_R0);
    unsigned va = rbase + 4032 - 3584 - 64 * (i16 >> 1) + 16 * kg + 256 * rh;
    const unsigned vamax = rbase + 4096;
    const unsigned hbase = cb + ((i16 & 1) ? CH_H0 : CH_H1);
    unsigned vb = hbase + 4 * i16 - ((i16 & 1) ? 60 : 64) + 16 * kg + 256 * rh;
    const unsigned vbmin = hbase - 16;
    USE(va); USE(vb);
    f32x4 acc[8];
#pragma unroll
    for (int i = 0; i < 8; ++i) acc[i] = f32x4{0.f, 0.f, 0.f, 0.f};
    const float dD = bf_lo(*(kptr)((cptr)p.sD + (cch >> 1) * 4) >> ((cch & 1) * 16));
    if (w >= 8) { wait2(cvp, 1); wait4(W_FREE, 11); } else wait4(W_FULL, 12);
    PRIO(3);
#define LDA_(i, o) lds128(va + 3584 - 512 * (i) + (o))
#define LDB_(dst, i, o) do { v2u lo = lds64v(vb + 512 * (i) + (o)), hi = lds64v(vb + 512 * (i) + (o) + 8); dst = v4u{lo.x, lo.y, hi.x, hi.y}; } while (0)
#define LDP(i, o) do { A[i] = LDA_(i, o); LDB_(B[i], i, o); } while (0)
#define STEP(m) do { _Pragma("unroll") for (int o = 0; o < (m); ++o) if ((m) + o <= 7) acc[(m) + o] = mfma(A[m], B[o], acc[(m) + o]); \
      if (2 * (m) <= 7) acc[2 * (m)] = mfma(A[m], B[m], acc[2 * (m)]); \
      _Pragma("unroll") for (int o = 0; o < (m); ++o) if ((m) + o <= 7) acc[(m) + o] = mfma(A[o], B[m], acc[(m) + o]); } while (0)
#define ILV(n) do { _Pragma("unroll") for (int i_ = 0; i_ < (n); ++i_) { SGB(0x8, 1); SGB(0x100, 1); } SGB(0x8, 16); SB0(); } while (0)
    v4u A[8], B[8];
    {
      A[0] = lds128(min(va + 3584, vamax));
      v2u lo = lds64v(max(vb, vbmin)), hi = lds64v(max(vb + 8, vbmin));
      B[0] = v4u{lo.x, lo.y, hi.x, hi.y};
      LDP(1, 0); LDP(2, 0); LDP(3, 0);
    }
    SB0();
#pragma unroll 1
    for (int r = 0; r < 4; ++r) {
      v4u nA0, nB0, nA1, nB1;
      LDP(4, 0); STEP(0); STEP(1); ILV(3);
      LDP(5, 0); STEP(2); ILV(3);
      LDP(6, 0); LDP(7, 0); STEP(3); ILV(6);
      nA0 = lds128(min(va + 3584 + 64, vamax)); LDB_(nB0, 0, 64); nA1 = LDA_(1, 64); LDB_(nB1, 1, 64); STEP(4); ILV(6);
      LDP(3, 64); STEP(5); ILV(3);
      LDP(2, 64); STEP(6); STEP(7); ILV(3);
      A[0] = nA0; B[0] = nB0; A[1] = nA1; B[1] = nB1;
      va += 64; vb += 64; USE(va); USE(vb);
    }
    const int chunk = 128 * rh + lane;
    const v4u kva = lds128(cb + CH_R1 + (255 - chunk) * 16), kvb = lds128(cb + CH_R1 + (191 - chunk) * 16);
    STAMP(9);
    PRIO(3);
    FENCE();
    pub(mycv, 2, lane); wait2(cvp, 2);
    {
      const unsigned a = cb + rh * 8192 + (64 * (lane >> 4) + 2 * (lane & 15)) * 4;
#pragma unroll
      for (int i = 0; i < 8; ++i) {
        sts64(a + 1024 * i, __builtin_bit_cast(v2u, f32x2{acc[i][0], acc[i][1]}));
        sts64(a + 1024 * i + 128, __builtin_bit_cast(v2u, f32x2{acc[i][2], acc[i][3]}));
      }
    }
    FENCE();
    pub(mycv, 3, lane); wait2(cvp, 3);
    conv_final(rs, cb, cch, chunk, kva, dD);
    conv_final(rs, cb, cch, chunk + 64, kvb, dD);
    FENCE();
    pub(mycv, 4, lane);
    if (w >= 8) { WAITV(0); if (lane == 0) flag_set(p.ws + WS_FY, (rh * 640 + cch) * 4, epoch); }
  }
  STAMP(10);
  if (w < 4) {
    const unsigned vfull = pub_addr(W_FULL + 4 * w, lane);
    if (!isfull) {
      wait4(W_FREE, 8);
      H_DMA(); CONV_PADS();
    }
    wait1(W_AP + 4, 4);
    v4u RG[4][8];
#define RG_LD(i, g) do { _Pragma("unroll") for (int j = 0; j < 8; ++j) { if ((j & 3) == 0) WAITV(8); \
      RG[i][j] = __builtin_amdgcn_raw_buffer_load_b128(rs, (unsigned)(w2s - p.ws) + l16, (g) * 15360 + j * 1024, AUX_SC1); } SB0(); } while (0)
#define RG_ST(i, g) do { wait4(W_FREE, 21 + (g) - 4); unsigned a_ = w2d + wslot(g) + l16; USE(a_); \
      _Pragma("unroll") for (int j = 0; j < 8; ++j) sts128(a_ + j * 1024, RG[i][j]); FENCE(); pubv(vfull, 21 + (g)); } while (0)
    wait4(W_FREE, 8);
    W2GROUP(2);
    if (isfull) W2GROUP(3); else { wait1(W_AP, 4); R_DMA(); }
    wait4(W_FREE, 9); W2GROUP(0);
    wait4(W_FREE, 10); W2GROUP(1);
    pubv(vfull, 12);
    RG_LD(0, 4);
    WAITV(8); pubv(vfull, isfull ? 24 : 23);
    RG_LD(1, 5); RG_LD(2, 6); RG_LD(3, 7);
    STAMP(11);
    if (!isfull) { wait4(W_CV, 4); W2GROUP(3); WAITV(0); pubv(vfull, 24); }
    PRIO(3);
    RG_ST(0, 4);
    RG_LD(0, 8);
    RG_ST(1, 5); RG_ST(2, 6); RG_ST(3, 7); RG_ST(0, 8);
    if (b == 0 && w == 3) {
      poll4(p.ws + WS_FP0, l16, epoch);
      if (lane == 0) __builtin_amdgcn_raw_buffer_store_b32(epoch, rs, WS_EPOCH, 0, AUX_SC1);
    }
    PRIO(0);
  } else if (w < 8) {
    const int cw = w - 4, tp = cw & 1, kh = cw >> 1;
    const unsigned vfree = pub_addr(W_FREE + 4 * cw, lane);
    const unsigned hoff = WS_HF + ((4 * tt64 + 2 * tp) * 54 + 27 * kh) * 1024 + l16;
    constexpr int HW = HWIN;
    v4u hf[27][2];
    f32x4 acc[2][5];
#pragma unroll
    for (int i = 0; i < 5; ++i) acc[0][i] = acc[1][i] = f32x4{0.f, 0.f, 0.f, 0.f};
    unsigned wa[2] = {kh * 15360 + l16, kh * 15360 + l16 + 65536};
    USE(wa[0]); USE(wa[1]);
    PRIO(2);
    {
      const unsigned fho = ((2 * tt64 + tp) * 32 + 13 * kh + lane) * 4;
      const unsigned v0 = atom_rd(p.ws + WS_FH, fho, 0x3fffull, epoch);
      const bool ok0 = wait_eq1<0>(v0, epoch);
      if (convw && lane == 0) flag_set(p.ws + WS_FY, ((w & 1) * 640 + cch) * 4, epoch);
      if (!ok0) poll_atomic(p.ws + WS_FH, fho, 0x3fffull, epoch);
    }
#define H_LD(i) do { hf[i][0] = __builtin_amdgcn_raw_buffer_load_b128(rs, hoff, (i) * 1024, AUX_SC1); \
      hf[i][1] = __builtin_amdgcn_raw_buffer_load_b128(rs, hoff, (i) * 1024 + 54 * 1024, AUX_SC1); } while (0)
#pragma unroll
    for (int i = 0; i < HW; ++i) H_LD(i);
    STAMP(11);
    v4u a0[5], a1[5];
    v4u fl;
    unsigned flm = 0;
#define P3_AD(ks, i) (wa[wslot((ks) / 3) >> 16] + (wslot((ks) / 3) & 65535) + (((ks) % 3) * 5 + (i)) * 1024)
#define P3_STEP(A, N, ks) do { \
      if (((ks) + 1) % 3 == 0 && (ks) < 26) { if (__builtin_expect(flm < 21 + ((ks) + 1) / 3, 0)) wait4(W_FULL, 21 + ((ks) + 1) / 3); FENCE(); } \
      _Pragma("unroll") for (int i = 0; i < 5; ++i) { \
        mfma_ip(acc[0][i], A[i], hf[ks][0]); \
        if ((ks) < 26) N[i] = lds128v(P3_AD((ks) + 1, i)); \
        mfma_ip(acc[1][i], A[i], hf[ks][1]); \
        if (i == 1 && (ks) % 3 == 0 && (ks) < 24) fl = peek4(W_FULL); \
        if (i == 3 && (ks) % 3 == 1 && (ks) < 24) flm = min4u(fl); \
        if (i == 1 && (ks) == 25) fl = peek4(W_FREE); \
      } \
      if (((ks) + 1) % 3 == 2 && (ks) < 25) pubv(vfree, 21 + ((ks) + 1) / 3); \
      SB0(); \
      if ((ks) + HW < 27) H_LD((ks) + HW); \
      SB0(); \
    } while (0)
    wait4(W_FULL, 21);
#pragma unroll
    for (int i = 0; i < 5; ++i) a0[i] = lds128v(P3_AD(0, i));
    SB0();
#pragma unroll
    for (int ks = 0; ks < 26; ks += 2) {
      P3_STEP(a0, a1, ks);
      P3_STEP(a1, a0, ks + 1);
      if (ks == 2) STAMP(12);
      if (ks == 12) STAMP(13);
    }
    P3_STEP(a0, a1, 26);
    const v4u fr = fl;
    asm volatile("s_nop 7\n\ts_nop 7" ::: "memory");
    STAMP(14);
    if (min4u(fr) < 27) wait4(W_FREE, 27);
    FENCE();
    {
      const unsigned oa = L_O + kh * (64 * OROW) + (32 * tp + (lane & 15)) * OROW + (lane >> 4) * 16;
#pragma unroll
      for (int i = 0; i < 5; ++i) { sts128(oa + i * 64, __builtin_bit_cast(v4u, acc[0][i])); sts128(oa + 16 * OROW + i * 64, __builtin_bit_cast(v4u, acc[1][i])); }
    }
    PRIO(0);
  } else {
    const int aw = w - 8;
    PRIO(1);
    wait4(W_CV + 16, 4);
    if (!convw) { pub(W_AP + 4 * aw, 6, lane); wait4(W_AP, 6); }
    const int npc = aw < 3 ? 3 : 1;
#pragma unroll 1
    for (int i = 0; i < npc; ++i) {
      const int it = (3 * aw + i) * 64 + lane;
      dma16((cptr)p.x + (size_t)(64 * tt64) * 1280 + c80 * 160, (it / 10) * 1280 + (it % 10) * 16, L_X + (3 * aw + i) * 1024);
    }
    if (aw == 2) sts64(L_XN + lane * 8, stq);
    STAMP(11);
    wait4(W_FREE, 23);
    poll_atomic(p.ws + WS_FY, ((tt64 >> 4) * 640 + c80 * 80 + aw * 24 + lane) * 4, aw < 3 ? 0xffffffull : 0xffull, epoch);
    STAMP(12);
#pragma unroll 1
    for (int i = 0; i < npc; ++i) {
      const int pc = 3 * aw + i;
      dma16s(p.ws + WS_YT + (size_t)(c80 * 80 + 8 * pc) * 4096 + tt64 * 128, (lane >> 3) * 4096 + (((lane ^ pc) & 7) << 4), L_Y + pc * 1024);
    }
    WAITV(0);
    PRIO(0);
  }
  STAMP(15);
  BAR0();
  STAMP(16);
  if (tid < 640) {
    const int ftok = tid / 10, fc8 = tid % 10;
    const unsigned oa = L_O + ftok * OROW + fc8 * 32;
    f32x4 o0 = __builtin_bit_cast(f32x4, lds128(oa)) + __builtin_bit_cast(f32x4, lds128(oa + 64 * OROW));
    f32x4 o1 = __builtin_bit_cast(f32x4, lds128(oa + 16)) + __builtin_bit_cast(f32x4, lds128(oa + 64 * OROW + 16));
    const v4u xv = lds128(L_X + tid * 16);
    const unsigned fs = lds32(L_XN + (64 * (tt64 & 1) + ftok) * 4);
    const float fm = bf_lo(fs), fr = __builtin_amdgcn_rcpf(bf_hi(fs));
    const unsigned ya = L_Y + fc8 * 1024 + ((((ftok >> 3) ^ fc8) & 7) << 4) + (ftok & 7) * 2;
    f32x2 o[4] = {{o0[0], o0[1]}, {o0[2], o0[3]}, {o1[0], o1[1]}, {o1[2], o1[3]}};
    unsigned xw[4] = {xv.x, xv.y, xv.z, xv.w}, rw[4];
#pragma unroll
    for (int j = 0; j < 4; ++j) {
      const f32x2 y = {bf_lo(*(const LAS u16*)(size_t)(ya + 256 * j)), bf_lo(*(const LAS u16*)(size_t)(ya + 256 * j + 128))};
      const f32x2 gb = silu_z(o[j] * -LOG2E);
      const f32x2 xf = bf2(xw[j]);
      rw[j] = pk_bf(xf + bf2(pk_bf((xf - fm) * fr)) * y * gb);
    }
    { const __amdgpu_buffer_rsrc_t ro = __builtin_amdgcn_make_buffer_rsrc((void*)p.out, 0, S * D * 2, 0x00020000);
      __builtin_amdgcn_raw_buffer_store_b128(v4u{rw[0], rw[1], rw[2], rw[3]}, ro, (64 * tt64 + ftok) * 1280 + c80 * 160 + fc8 * 16, 0, OUTAUX); }
  }
}

extern "C" void kernel_launch(void* const* d_in, const int* in_sizes, int n_in, void* d_out, int out_size, void* d_ws, size_t ws_size,
                              hipStream_t stream) {
  Params p;
  p.x = (const u16*)d_in[0]; p.k = (const u16*)d_in[1]; p.v = (const u16*)d_in[2];
  p.shk = (const u16*)d_in[3]; p.sk = (const u16*)d_in[4]; p.sD = (const u16*)d_in[5];
  p.w1 = (const u16*)d_in[7]; p.w2 = (const u16*)d_in[8];
  p.out = (u16*)d_out; p.ws = (unsigned char*)d_ws;
  fused_kernel<<<NWG, NTHR, 0, stream>>>(p);
}
```

```cpp
#define DIAG 0
#define DIAGW 4
#include <hip/hip_runtime.h>
#include <stdint.h>

typedef unsigned short u16;
typedef unsigned v4u __attribute__((ext_vector_type(4)));
typedef unsigned v2u __attribute__((ext_vector_type(2)));
typedef float f32x4 __attribute__((ext_vector_type(4)));
typedef float f32x2 __attribute__((ext_vector_type(2)));
typedef short bf16x8 __attribute__((ext_vector_type(8)));
typedef short v4s __attribute__((ext_vector_type(4)));
typedef __bf16 v2bf __attribute__((ext_vector_type(2)));
#define LAS __attribute__((address_space(3)))
#define GAS __attribute__((address_space(1)))
#define DEV __device__ __forceinline__
typedef const unsigned char* cptr;
typedef const __attribute__((address_space(4))) unsigned* kptr;

constexpr int S = 2048, D = 640, F = 1728;
constexpr int NWG = 256, NTHR = 768;
constexpr unsigned WS_EPOCH = 0;
constexpr unsigned WS_FP0 = 1024;
constexpr unsigned WS_FW = 3072;
constexpr unsigned WS_FH = 4096;
constexpr unsigned WS_FY = 16384;
constexpr unsigned WS_STATS = 32768;
constexpr unsigned KVR_ROW = 4224;
constexpr unsigned WS_KVR = 65536;
constexpr unsigned WS_W2T = WS_KVR + 640 * KVR_ROW;
constexpr unsigned WS_HF = WS_W2T + 8 * 54 * 5 * 1024;
constexpr unsigned WS_YT = WS_HF + 128 * 54 * 1024;
constexpr unsigned WS_CSP = WS_YT + 640 * 4096;
constexpr unsigned WS_MBKV = WS_CSP + 147456;
constexpr unsigned WS_W2R = WS_MBKV + 40960;
constexpr unsigned WS_END = WS_W2R + 4096;
constexpr unsigned WS_DBG = WS_YT + 640 * 4096 + 262144;
constexpr unsigned L_E = 131072;
constexpr unsigned L_STATS = 65536 + 30720;
constexpr unsigned L_CS = 98304 + 30720;
constexpr unsigned CH_R1 = 0, CH_Z1 = 4096, CH_R0 = 4128, CH_Z0 = 8224, CH_H0 = 8240, CH_PF = 12416, CH_H1 = 12432, CH_BYTES = 16528;
constexpr unsigned L_SYNC = 163840 - 128;
constexpr unsigned W_FULL = L_SYNC, W_FREE = L_SYNC + 16, W_AP = L_SYNC + 32, W_CV = L_SYNC + 48, W_PL = L_SYNC + 80, W_MK = L_SYNC + 96;
constexpr unsigned L_AREA0 = L_SYNC - 4 * CH_BYTES;
constexpr unsigned L_P0 = L_E;
constexpr unsigned L_Y = L_E;
constexpr unsigned L_X = L_E + 10240;
constexpr unsigned L_XN = L_E + 20480;
constexpr unsigned L_O = 32768;
constexpr unsigned OROW = 336;
constexpr unsigned SPIN_CAP = 1u << 15;
constexpr int AUX_SC1 = 16;
constexpr unsigned wslot(int g) { return (g & 3) * 32768u; }

DEV float asf(unsigned u) { return __builtin_bit_cast(float, u); }
DEV unsigned asu(float f) { return __builtin_bit_cast(unsigned, f); }
DEV float bf_lo(unsigned u) { return __builtin_bit_cast(float, u << 16); }
DEV float bf_hi(unsigned u) { return __builtin_bit_cast(float, u & 0xffff0000u); }
DEV f32x2 bf2(unsigned u) { return f32x2{bf_lo(u), bf_hi(u)}; }
DEV unsigned pk_bf(float a, float b) { v2bf r = {(__bf16)a, (__bf16)b}; return __builtin_bit_cast(unsigned, r); }
DEV unsigned pk_bf(f32x2 a) { return pk_bf(a[0], a[1]); }
DEV float rbf(float a) { return bf_lo(pk_bf(a, a) & 0xffffu); }
DEV f32x4 mfma(v4u a, v4u b, f32x4 c) {
  return __builtin_amdgcn_mfma_f32_16x16x32_bf16(__builtin_bit_cast(bf16x8, a), __builtin_bit_cast(bf16x8, b), c, 0, 0, 0);
}
DEV void mfma_ip(f32x4& c, v4u a, v4u b) { asm volatile("v_mfma_f32_16x16x32_bf16 %0, %1, %2, %0" : "+v"(c) : "v"(a), "v"(b)); }
DEV v4u lds128v(unsigned a) { return *(const volatile LAS v4u*)(size_t)a; }
constexpr float LOG2E = 1.44269504088896f;
DEV f32x2 silu_z(f32x2 z) {
  f32x2 e = {__builtin_amdgcn_exp2f(z[0]), __builtin_amdgcn_exp2f(z[1])};
  f32x2 d = e * -LOG2E - LOG2E;
  f32x2 r = {__builtin_amdgcn_rcpf(d[0]), __builtin_amdgcn_rcpf(d[1])};
  return z * r;
}
DEV float dot2ones(unsigned u, float acc) {
  return __builtin_amdgcn_fdot2_f32_bf16(__builtin_bit_cast(v2bf, u), __builtin_bit_cast(v2bf, 0x3f803f80u), acc, false);
}

DEV void dma16(const void* sbase, unsigned voff, unsigned ldsoff) {
  asm volatile("s_mov_b32 m0, %2\n\ts_nop 0\n\tglobal_load_lds_dwordx4 %0, %1" ::"v"(voff), "s"(sbase), "s"(ldsoff) : "memory");
}
DEV void dma16s(const void* sbase, unsigned voff, unsigned ldsoff) {
  asm volatile("s_mov_b32 m0, %2\n\ts_nop 0\n\tglobal_load_lds_dwordx4 %0, %1 sc1" ::"v"(voff), "s"(sbase), "s"(ldsoff) : "memory");
}
DEV void dma4k(const void* sbase, unsigned voff, unsigned ldsoff) {
  asm volatile("s_mov_b32 m0, %2\n\ts_nop 0\n\tglobal_load_lds_dwordx4 %0, %1\n\tglobal_load_lds_dwordx4 %0, %1 offset:1024\n\t"
               "global_load_lds_dwordx4 %0, %1 offset:2048\n\tglobal_load_lds_dwordx4 %0, %1 offset:3072" ::"v"(voff), "s"(sbase), "s"(ldsoff) : "memory");
}
DEV void dma4ks(const void* sbase, unsigned voff, unsigned ldsoff) {
  asm volatile("s_mov_b32 m0, %2\n\ts_nop 0\n\tglobal_load_lds_dwordx4 %0, %1 sc1\n\tglobal_load_lds_dwordx4 %0, %1 offset:1024 sc1\n\t"
               "global_load_lds_dwordx4 %0, %1 offset:2048 sc1\n\tglobal_load_lds_dwordx4 %0, %1 offset:3072 sc1" ::"v"(voff), "s"(sbase), "s"(ldsoff) : "memory");
}
DEV void dma4v(const void* sbase, const unsigned (&vo)[4], unsigned ldsoff) {
  asm volatile("s_mov_b32 m0, %5\n\ts_nop 0\n\tglobal_load_lds_dwordx4 %0, %4\n\tglobal_load_lds_dwordx4 %1, %4 offset:1024\n\t"
               "global_load_lds_dwordx4 %2, %4 offset:2048\n\tglobal_load_lds_dwordx4 %3, %4 offset:3072"
               ::"v"(vo[0]), "v"(vo[1]), "v"(vo[2]), "v"(vo[3]), "s"(sbase), "s"(ldsoff) : "memory");
}
DEV void dma4vm(const void* sbase, const unsigned (&vo)[4], unsigned ldsoff, unsigned long long lastmask) {
  asm volatile("s_mov_b32 m0, %5\n\ts_nop 0\n\tglobal_load_lds_dwordx4 %0, %4\n\tglobal_load_lds_dwordx4 %1, %4 offset:1024\n\t"
               "global_load_lds_dwordx4 %2, %4 offset:2048\n\ts_mov_b64 exec, %6\n\tglobal_load_lds_dwordx4 %3, %4 offset:3072\n\ts_mov_b64 exec, -1"
               ::"v"(vo[0]), "v"(vo[1]), "v"(vo[2]), "v"(vo[3]), "s"(sbase), "s"(ldsoff), "s"(lastmask) : "memory");
}
DEV void touch(const void* sbase, unsigned voff) {
  asm volatile("s_mov_b32 m0, 0x28000\n\ts_nop 0\n\tglobal_load_lds_dword %0, %1" ::"v"(voff), "s"(sbase) : "memory");
}
DEV v4u ld128a(const void* sbase, unsigned voff) { v4u r; asm volatile("global_load_dwordx4 %0, %1, %2 sc1" : "=v"(r) : "v"(voff), "s"(sbase) : "memory"); return r; }
DEV v2u ld64a(const void* sbase, unsigned voff) { v2u r; asm volatile("global_load_dwordx2 %0, %1, %2 sc1" : "=v"(r) : "v"(voff), "s"(sbase) : "memory"); return r; }
DEV unsigned atom_rd(const void* sbase, unsigned voff, unsigned long long mask, unsigned dflt) {
  unsigned r; unsigned long long sv;
  asm volatile("v_mov_b32 %0, %6\n\ts_mov_b64 %1, exec\n\ts_mov_b64 exec, %5\n\tglobal_atomic_or %0, %2, %3, %4 sc0 sc1\n\ts_mov_b64 exec, %1"
               : "=&v"(r), "=&s"(sv) : "v"(voff), "v"(0u), "s"(sbase), "s"(mask), "s"(dflt) : "memory");
  return r;
}
DEV unsigned ld32a(const void* sbase, unsigned voff) { unsigned r; asm volatile("global_load_dword %0, %1, %2 sc1" : "=v"(r) : "v"(voff), "s"(sbase) : "memory"); return r; }
#define WAITV(n) asm volatile("s_waitcnt vmcnt(" #n ")" ::: "memory")
#define USE(x) asm volatile("" : "+v"(x))
#define BAR0() do { asm volatile("s_waitcnt lgkmcnt(0)" ::: "memory"); __builtin_amdgcn_s_barrier(); asm volatile("" ::: "memory"); } while (0)
#define SGB(m, n) __builtin_amdgcn_sched_group_barrier(m, n, 0)
#define SB0() __builtin_amdgcn_sched_barrier(0)

DEV v4u lds128(unsigned a) { return *(const LAS v4u*)(size_t)a; }
DEV v2u lds64(unsigned a) { return *(const LAS v2u*)(size_t)a; }
DEV v2u lds64v(unsigned a) { return *(const volatile LAS v2u*)(size_t)a; }
DEV unsigned lds32(unsigned a) { return *(const LAS unsigned*)(size_t)a; }
DEV void sts128(unsigned a, v4u v) { *(LAS v4u*)(size_t)a = v; }
DEV void sts64(unsigned a, v2u v) { *(LAS v2u*)(size_t)a = v; }
DEV void sts32(unsigned a, unsigned v) { *(LAS unsigned*)(size_t)a = v; }
DEV void sts16(unsigned a, unsigned v) { *(LAS u16*)(size_t)a = (u16)v; }
DEV v2u ldstr(unsigned a) { return __builtin_bit_cast(v2u, __builtin_amdgcn_ds_read_tr16_b64_v4i16((LAS v4s*)(size_t)a)); }

#define SLP 2
DEV void pub(unsigned a, unsigned v, int lane) { if (lane == 0) *(volatile LAS unsigned*)(size_t)a = v; }
DEV unsigned pub_addr(unsigned a, int lane) { unsigned r = lane == 0 ? a : 0x30000u + ((a + lane * 4) & 255); asm volatile("" : "+v"(r)); return r; }
DEV void pubv(unsigned va, unsigned v) { *(volatile LAS unsigned*)(size_t)va = v; }
DEV unsigned min4u(v4u v) { return (unsigned)__builtin_amdgcn_readfirstlane(min(min(v.x, v.y), min(v.z, v.w))); }
DEV v4u peek4(unsigned a) { return *(const volatile LAS v4u*)(size_t)a; }
DEV bool ge4(v4u v, unsigned t) { return (unsigned)__builtin_amdgcn_readfirstlane(min(min(v.x, v.y), min(v.z, v.w))) >= t; }
DEV void wait4(unsigned a, unsigned t) {
#pragma unroll 1
  for (unsigned i = 0; i < (1u << 22); ++i) { if (ge4(peek4(a), t)) break; __builtin_amdgcn_s_sleep(SLP); }
  asm volatile("" ::: "memory");
}
DEV void wait2(unsigned a, unsigned t) {
#pragma unroll 1
  for (unsigned i = 0; i < (1u << 22); ++i) {
    v2u v = *(const volatile LAS v2u*)(size_t)a;
    if ((unsigned)__builtin_amdgcn_readfirstlane(min(v.x, v.y)) >= t) break;
    __builtin_amdgcn_s_sleep(1);
  }
  asm volatile("" ::: "memory");
}
DEV void wait1(unsigned a, unsigned t) {
#pragma unroll 1
  for (unsigned i = 0; i < (1u << 22); ++i) {
    if ((unsigned)__builtin_amdgcn_readfirstlane(*(const volatile LAS unsigned*)(size_t)a) >= t) break;
    __builtin_amdgcn_s_sleep(1);
  }
  asm volatile("" ::: "memory");
}
DEV float xsum16_32(float v) {
  float a = v, c = v;
  asm volatile("s_nop 1\n\tv_permlane16_swap_b32 %0, %1\n\ts_nop 0" : "+v"(a), "+v"(c));
  a += c; c = a;
  asm volatile("s_nop 1\n\tv_permlane32_swap_b32 %0, %1\n\ts_nop 0" : "+v"(a), "+v"(c));
  return a + c;
}
#define WAITL() asm volatile("s_waitcnt lgkmcnt(0)" ::: "memory")
template <int CTRL, int RM> DEV float dpp_add(float v) {
  return v + __builtin_bit_cast(float, __builtin_amdgcn_update_dpp(0, __builtin_bit_cast(int, v), CTRL, RM, 0xf, false));
}
DEV float wave_sum(float v) {
  v = dpp_add<0x111, 0xf>(v); v = dpp_add<0x112, 0xf>(v); v = dpp_add<0x114, 0xf>(v); v = dpp_add<0x118, 0xf>(v);
  v = dpp_add<0x142, 0xa>(v); v = dpp_add<0x143, 0xc>(v);
  return __builtin_bit_cast(float, __builtin_amdgcn_readlane(__builtin_bit_cast(int, v), 63));
}

#define BAR() BAR0()
#define STAMP(i) do { if ((i) == 0) dg0 = (unsigned)tf_; if ((i) == DIAG) dg1 = dg0; } while (0)

#define ROTC 0
#define LPRIO 1
#define HWIN 8
#define APRIO 3
struct Params {
  const u16 *x, *k, *v, *shk, *sk, *sD, *w1, *w2;
  u16* out;
  unsigned char* ws;
};

template <int N> DEV bool wait_eq1(unsigned v, unsigned epoch) {
  unsigned d;
  asm volatile("s_waitcnt vmcnt(%3)\n\tv_xor_b32 %0, %2, %1" : "=v"(d) : "v"(v), "s"(epoch), "i"(N) : "memory");
  return __all(d == 0u);
}
template <int N> DEV bool wait_eq4(v4u v, unsigned epoch) {
  unsigned d, e;
  asm volatile("s_waitcnt vmcnt(%7)\n\tv_xor_b32 %0, %6, %2\n\tv_xor_b32 %1, %6, %3\n\tv_or_b32 %0, %0, %1\n\tv_xor_b32 %1, %6, %4\n\t"
               "v_or_b32 %0, %0, %1\n\tv_xor_b32 %1, %6, %5\n\tv_or_b32 %0, %0, %1"
               : "=&v"(d), "=&v"(e) : "v"(v.x), "v"(v.y), "v"(v.z), "v"(v.w), "s"(epoch), "i"(N) : "memory");
  return __all(d == 0u);
}
DEV void poll1(cptr base, unsigned off, unsigned epoch) {
#pragma unroll 1
  for (unsigned spins = 0; spins < SPIN_CAP; ++spins) {
    unsigned v = ld32a(base, off);
    if (wait_eq1<0>(v, epoch)) break;
    __builtin_amdgcn_s_sleep(6);
  }
}
DEV void poll4(cptr base, unsigned off, unsigned epoch) {
#pragma unroll 1
  for (unsigned spins = 0; spins < SPIN_CAP; ++spins) {
    v4u v = ld128a(base, off);
    if (wait_eq4<0>(v, epoch)) break;
    __builtin_amdgcn_s_sleep(6);
  }
}
DEV void flag_set(const void* sbase, unsigned voff, unsigned epoch) {
  asm volatile("global_atomic_swap %0, %1, %2 sc1" ::"v"(voff), "v"(epoch), "s"(sbase) : "memory");
}
DEV void poll_atomic(cptr base, unsigned off, unsigned long long mask, unsigned epoch) {
#pragma unroll 1
  for (unsigned spins = 0; spins < SPIN_CAP; ++spins) {
    const unsigned v = atom_rd(base, off, mask, epoch);
    if (wait_eq1<0>(v, epoch)) break;
    __builtin_amdgcn_s_sleep(2);
  }
}
template <int N> DEV unsigned epoch_fin(unsigned ev) {
  unsigned e;
  asm volatile("s_waitcnt vmcnt(%2)\n\tv_readfirstlane_b32 %0, %1\n\ts_nop 4" : "=s"(e) : "v"(ev), "i"(N) : "memory");
  return e * 2654435761u + 0x9E3779B9u;
}

DEV void conv_final(__amdgpu_buffer_rsrc_t rs, unsigned db, int ch, int chunk, v4u kvr, float dD) {
  const unsigned a = db + chunk * 32;
  f32x4 p0 = __builtin_bit_cast(f32x4, lds128(a)), p1 = __builtin_bit_cast(f32x4, lds128(a + 16));
  f32x4 q0 = __builtin_bit_cast(f32x4, lds128(a + 8192)), q1 = __builtin_bit_cast(f32x4, lds128(a + 8192 + 16));
  p0 += q0; p1 += q1;
  p0 += f32x4{bf_hi(kvr.w), bf_lo(kvr.w), bf_hi(kvr.z), bf_lo(kvr.z)} * dD;
  p1 += f32x4{bf_hi(kvr.y), bf_lo(kvr.y), bf_hi(kvr.x), bf_lo(kvr.x)} * dD;
  const v4u o = {pk_bf(p0[0], p0[1]), pk_bf(p0[2], p0[3]), pk_bf(p1[0], p1[1]), pk_bf(p1[2], p1[3])};
  __builtin_amdgcn_raw_buffer_store_b128(o, rs, WS_YT + ch * 4096 + chunk * 16, 0, AUX_SC1);
}

__global__ void __launch_bounds__(NTHR, 3) fused_kernel(Params p) {
  __shared__ __attribute__((aligned(16))) unsigned char smem[163840];
  unsigned long long tf_; asm volatile("s_memrealtime %0\n\ts_waitcnt lgkmcnt(0)" : "=s"(tf_) :: "memory");
  const int tid = threadIdx.x, lane = tid & 63;
  const int w = __builtin_amdgcn_readfirstlane(tid >> 6);
  const int b = blockIdx.x, xcd = b & 7, idx = b >> 3;
  const int half = xcd & 1, cti = idx & 7;
  const int rt = 4 * (xcd >> 1) + (idx >> 3);
  const int nfull = 6 - half;
  const bool isfull = cti < nfull;
  const int u0 = 14 * half + (isfull ? 2 * cti : nfull + cti);
  const unsigned l16 = lane * 16;
  { unsigned lb = (unsigned)(size_t)(LAS unsigned char*)smem; asm volatile("" ::"s"(lb)); }
  unsigned stv = 0; (void)stv; unsigned dg0 = 0, dg1 = 0;
  STAMP(0);
  unsigned epoch, epraw;
  asm volatile("s_load_dword %0, %1, 0x0" : "=s"(epraw) : "s"(p.ws + WS_EPOCH) : "memory");
#define EPOCH_FIN() asm volatile("s_waitcnt lgkmcnt(0)\n\ts_mul_i32 %0, %1, 0x9E3779B1\n\ts_add_u32 %0, %0, 0x9E3779B9" : "=s"(epoch) : "s"(epraw) : "memory", "scc")
  const bool kvrole = idx < 20;
  const int kvn = xcd * 20 + idx, kt = kvn / 10, kc = kvn % 10;
  const int wn = xcd * 12 + idx - 20;
  const __amdgpu_buffer_rsrc_t rs = __builtin_amdgcn_make_buffer_rsrc((void*)p.ws, 0, (int)WS_END, 0x00020000);
  const unsigned lcs = isfull ? L_CS : L_STATS + 1024;
#define LATE_CONSTS() \
  int rt_ = rt; asm volatile("" : "+s"(rt_)); \
  const int tt64 = 2 * rt_ + (cti >> 2), c80 = 4 * half + (cti & 3); \
  const int hn = rt_ * 5 + half * 2 + cti - nfull; \
  const int area = w >= 8 ? 2 + ((w >> 1) & 1) : (w >> 1) & 1; \
  const bool convw = isfull ? w >= 8 : (hn < 72 && w >= 4); \
  const int chb = isfull ? 2 * (rt_ * 11 + half * 6 + cti) - 2 : (hn < 72 ? 352 + 4 * hn : 0); \
  const int cch = (isfull && area < 2) ? chb + 2 : chb + area; \
  const unsigned cb = L_AREA0 + area * CH_BYTES; \
  cptr w2s = p.ws + WS_W2T + c80 * (54 * 5 * 1024) + ((w >> 1) & 1) * (27 * 5 * 1024) + (w & 1) * 7168; \
  const unsigned w2d = ((w >> 1) & 1) * 15360 + (w & 1) * 7168; \
  (void)tt64; (void)convw; (void)cch; (void)cb; (void)w2s; (void)w2d; (void)chb; (void)hn;
#define H_DMA() dma4k((cptr)p.sk + (size_t)cch * 4096 + (w & 1) * 4, l16, cb + ((w & 1) ? CH_H1 : CH_H0))
#define R_DMA() dma4ks(p.ws + WS_KVR + (size_t)cch * KVR_ROW + (w & 1) * 2, l16, cb + ((w & 1) ? CH_R0 : CH_R1))
#define CONV_PADS() do { const unsigned h01 = *(kptr)((cptr)p.sk + (size_t)cch * 4096); \
    if (!(w & 1) && lane < 3) sts128(cb + (lane == 0 ? CH_Z1 : lane == 1 ? CH_Z0 : CH_PF), v4u{0u, 0u, 0u, lane == 2 ? h01 : 0u}); } while (0)
#define W2GROUP(g) do { WAITV(8); dma4ks(w2s + (g) * 15360, l16, w2d + wslot(g)); WAITV(8); dma4ks(w2s + (g) * 15360 + 4096, l16, w2d + wslot(g) + 4096); } while (0)
#define FENCE() asm volatile("" ::: "memory")
#define PRIO(x) __builtin_amdgcn_s_setprio(x)

  v2u stq = {0u, 0u};
  if (w < 4) {
    const int L = w;
    PRIO(LPRIO);
    const unsigned bx = (lane >> 3) * 1280, cx = lane & 7;
    const unsigned cwz = (lane & 15) ^ ((lane >> 4) << 2);
    const int cmask = isfull ? 15 : 7;
    unsigned vx[4], vw[4];
#pragma unroll
    for (int j = 0; j < 4; ++j) {
      vx[j] = bx + ((cx ^ (lane >> 3)) << 4) + j * (8 * 1280 - 1024);
      vw[j] = (lane >> 4) * 3456 + (((cwz ^ j) & cmask) << 4) + j * (4 * 3456 - 1024);
    }
    int chunk = (rt + ROTC * cti) % 10;
    cptr xb0 = (cptr)p.x + (size_t)(128 * rt + 32 * L) * 1280;
    cptr wb0 = (cptr)p.w1 + (size_t)(16 * L) * 3456 + 128 * u0;
    unsigned reg = L * 4096, rx = 32768;
#define STAGE_A() dma4v(xb0 + 128 * chunk, vx, reg)
#define STAGE_B() do { dma4v(wb0 + (size_t)chunk * (64 * 3456), vw, reg + 16384); chunk = chunk == 9 ? 0 : chunk + 1; reg ^= rx; rx ^= 98304; } while (0)
    unsigned vo[4];
    const unsigned myfull = W_FULL + 4 * L, vfull = pub_addr(myfull, lane);
    if (kvrole) {
      STAGE_A(); STAGE_B();
      BAR0();
#pragma unroll
      for (int j = 0; j < 4; ++j) vo[j] = bx + ((cx ^ ((4 * L + j) & 7)) << 4) + j * (8 * 1280 - 1024);
      const size_t go = (size_t)(128 * kt + 32 * L) * 1280 + 128 * kc;
      STAGE_A();
      WAITV(4); pubv(vfull, 1);
      STAGE_B();
      EPOCH_FIN();
      WAITV(4); dma4v((cptr)p.k + go, vo, L_P0 + L * 4096);
      WAITV(4); pubv(vfull, 2);
      dma4vm((cptr)p.v + go, vo, L_P0 + 16384 + L * 4096, L == 3 ? 0x00ffffffffffffffull : ~0ull);
      WAITV(4); STAGE_A();
      WAITV(4); pub(W_PL + 4 * L, 1, lane);
    } else {
#pragma unroll
      for (int j = 0; j < 4; ++j) vo[j] = (lane >> 4) * 1280 + ((cwz ^ j) << 4) + j * (4 * 1280 - 1024);
      const int wnc = wn < 90 ? wn : 0;
      const int g0 = L < 3 ? 2 * L : 4;
      cptr p0a = (cptr)p.w2 + (size_t)(96 * (wnc / 5) + 16 * g0) * 1280 + 256 * (wnc % 5);
      dma4v(p0a, vo, L_P0 + g0 * 4096); dma4v(p0a + 16 * 1280, vo, L_P0 + g0 * 4096 + 4096);
      BAR0();
      STAGE_A();
      EPOCH_FIN();
      WAITV(4); pub(W_PL + 4 * L, 1, lane);
      STAGE_B();
      WAITV(4); STAGE_A();
      WAITV(4); pubv(vfull, 1);
      STAGE_B();
      WAITV(4); STAGE_A();
      WAITV(4); pubv(vfull, 2);
    }
    chunk = __builtin_amdgcn_readfirstlane(chunk); reg = __builtin_amdgcn_readfirstlane(reg); rx = __builtin_amdgcn_readfirstlane(rx);
    STAMP(1);
    STAGE_B();
    WAITV(4); STAGE_A();
    WAITV(4); pubv(vfull, 3);
    STAGE_B();
    STAMP(2);
    if (rt < 10) wait4(W_AP, 1);
    {
      unsigned frm = min4u(peek4(W_FREE));
#pragma unroll 1
      for (int s = 1; s <= 6; ++s) {
        if (frm < (unsigned)s) { WAITV(0); pubv(vfull, s + 3); wait4(W_FREE, s); }
        WAITV(4); STAGE_A();
        WAITV(4); pubv(vfull, s + 3);
        STAGE_B();
        frm = min4u(peek4(W_FREE));
      }
    }
    WAITV(0); pubv(vfull, 10);
    STAMP(3);
    STAMP(5);
  } else if (w < 8) {
    const int cw = w - 4;
    const int n = lane & 15, g = lane >> 4, q = (lane & 15) >> 2, pp = lane & 3;
    const int tok0 = isfull ? 64 * (cw & 1) : 32 * cw, fg = isfull ? cw >> 1 : 0;
    BAR0();
    unsigned ax[2], aw[4][2];
#pragma unroll
    for (int u = 0; u < 2; ++u) ax[u] = (tok0 + n) * 128 + ((((4 * u + g) ^ n) & 7) << 4);
#pragma unroll
    for (int mt = 0; mt < 4; ++mt)
#pragma unroll
      for (int t = 0; t < 2; ++t)
        aw[mt][t] = 16384 + (8 * g + 4 * t + q) * 256 + (((2 * (4 * fg + mt) + (pp >> 1)) ^ ((q << 2) | ((2 * g + t) & 3))) << 4) + 8 * (pp & 1);
    f32x4 acc[4][4];
#pragma unroll
    for (int mt = 0; mt < 4; ++mt)
#pragma unroll
      for (int T = 0; T < 4; ++T) acc[mt][T] = f32x4{0.f, 0.f, 0.f, 0.f};
    v4u xf0[4], wf0[4], xf1[4], wf1[4];
    EPOCH_FIN();
    const unsigned vfree = pub_addr(W_FREE + 4 * cw, lane);
#define P1_READ(NT, XF, WF, U) do { \
      { v2u lo = ldstr(aw[0][0] + 8192 * U), hi = ldstr(aw[0][1] + 8192 * U); WF[0] = v4u{lo.x, lo.y, hi.x, hi.y}; } \
      _Pragma("unroll") for (int T = 0; T < NT; ++T) XF[T] = lds128(ax[U] + 2048 * T); \
      _Pragma("unroll") for (int mt = 1; mt < 4; ++mt) { v2u lo = ldstr(aw[mt][0] + 8192 * U), hi = ldstr(aw[mt][1] + 8192 * U); WF[mt] = v4u{lo.x, lo.y, hi.x, hi.y}; } \
    } while (0)
#define P1_MMA(NT, XF, WF) do { \
      _Pragma("unroll") for (int mt = 0; mt < 4; ++mt) _Pragma("unroll") for (int T = 0; T < NT; ++T) acc[mt][T] = mfma(WF[mt], XF[T], acc[mt][T]); \
    } while (0)
#define P1_ILV(NT) do { _Pragma("unroll") for (int i = 0; i < 4 * NT; ++i) { SGB(0x8, 1); SGB(0x100, NT == 4 ? 1 : 2); } SB0(); } while (0)
#define P1_ILV2(NT) do { _Pragma("unroll") for (int i = 0; i < 4 * NT; ++i) { SGB(0x8, 1); SGB(0x180, NT == 4 ? 1 : 2); SGB(0x2, NT == 4 ? 1 : 2); } SB0(); } while (0)
#define P1_LOOP(NT) do { \
      wait4(W_FULL, 1); \
      P1_READ(NT, xf0, wf0, 0); SB0(); \
      _Pragma("unroll 1") for (int s = 0; s < 9; ++s) { \
        const v4u fl = peek4(W_FULL); \
        P1_READ(NT, xf1, wf1, 1); \
        FENCE(); \
        pubv(vfree, s + 1); \
        const unsigned dx = (s & 1) ? 65536u : 32768u; \
        ax[0] ^= dx; ax[1] ^= dx; \
        _Pragma("unroll") for (int mt = 0; mt < 4; ++mt) { aw[mt][0] ^= dx; aw[mt][1] ^= dx; } \
        const unsigned flm = min4u(fl); \
        P1_MMA(NT, xf0, wf0); P1_ILV2(NT); \
        if (s == 0 || s == 2 || s == 5) STAMP(20 + s); \
        if (__builtin_expect(flm < (unsigned)s + 2, 0)) wait4(W_FULL, s + 2); \
        FENCE(); SB0(); \
        P1_READ(NT, xf0, wf0, 0); P1_MMA(NT, xf1, wf1); P1_ILV(NT); \
      } \
      P1_READ(NT, xf1, wf1, 1); \
      FENCE(); \
      pubv(vfree, 10); \
      P1_MMA(NT, xf0, wf0); P1_ILV2(NT); \
      STAMP(29); \
    } while (0)
    PRIO(2);
    if (isfull) { P1_LOOP(4); P1_MMA(4, xf1, wf1); } else { P1_LOOP(2); P1_MMA(2, xf1, wf1); }
    STAMP(4);
    wait2(W_AP + 8, 5);
    f32x4 cs[4];
#pragma unroll
    for (int mt = 0; mt < 4; ++mt) cs[mt] = __builtin_bit_cast(f32x4, lds128(lcs + (fg * 64 + mt * 16 + g * 4) * 4));
    const unsigned ho = WS_HF + ((8 * rt + (tok0 >> 4)) * 54 + 2 * (u0 + fg)) * 1024 + l16;
#pragma unroll
    for (int T = 0; T < 4; ++T) {
      if (T < 2 || isfull) {
        const f32x2 st = __builtin_bit_cast(f32x2, lds64(L_STATS + (tok0 + 16 * T + n) * 8));
#pragma unroll
        for (int pr = 0; pr < 2; ++pr) {
          unsigned o[4];
#pragma unroll
          for (int j = 0; j < 4; ++j) {
            const int mt = 2 * pr + (j >> 1), e = 2 * (j & 1);
            const f32x2 av = {acc[mt][T][e], acc[mt][T][e + 1]}, cv = {cs[mt][e], cs[mt][e + 1]};
            o[j] = pk_bf(silu_z(av * st[0] + cv * st[1]));
          }
          __builtin_amdgcn_raw_buffer_store_b128(v4u{o[0], o[1], o[2], o[3]}, rs, ho, T * (54 * 1024) + pr * 1024, AUX_SC1);
          SB0();
        }
      }
    }
    pubv(vfree, 11);
    STAMP(6);
    WAITV(0);
    if (lane < (isfull ? 2 : 1)) flag_set(p.ws + WS_FH, ((4 * rt + (tok0 >> 5) + lane) * 32 + u0 + fg) * 4, epoch);
    STAMP(7);
  } else {
    const int aw = w - 8;
    PRIO(3);
    if (aw == 0 && lane < 28) {
      const int hn = rt * 5 + half * 2 + cti - nfull;
      const bool cvon = lane < 12 || lane >= 20 || (isfull ? lane >= 16 : hn < 72);
      sts32(L_SYNC + lane * 4, cvon ? 0u : 99u);
    }
    const int srow = 128 * rt + 8 * (half * 8 + cti) + 2 * aw;
    cptr xr = (cptr)p.x + (size_t)srow * 1280;
    const v4u xa = *(const GAS v4u*)(xr + l16), xc = *(const GAS v4u*)(xr + 1280 + l16);
    const unsigned xb = *(const GAS unsigned*)(xr + 1024 + lane * 4), xd = *(const GAS unsigned*)(xr + 2304 + lane * 4);
    const int tg8 = (lane & 7) + 8 * (aw & 1), cpl = (lane >> 3) + 8 * (aw >> 1);
    unsigned halo[2][3] = {{0u, 0u, 0u}, {0u, 0u, 0u}};
    v4u shk4[2] = {{0u, 0u, 0u, 0u}, {0u, 0u, 0u, 0u}};
    unsigned vlast[2] = {0u, 0u};
    if (kvrole) {
      if (tg8 == 15) {
#pragma unroll
        for (int it = 0; it < 2; ++it) vlast[it] = *(const GAS unsigned*)((cptr)p.v + (size_t)(128 * kt + 127) * 1280 + 128 * kc + 4 * cpl + 64 * it);
      }
#pragma unroll
      for (int it = 0; it < 2; ++it) shk4[it] = *(const GAS v4u*)((cptr)p.shk + (size_t)(64 * kc + 2 * cpl + 32 * it) * 8);
      if (tg8 == 0 && kt > 0) {
#pragma unroll
        for (int it = 0; it < 2; ++it)
#pragma unroll
          for (int i = 0; i < 3; ++i) halo[it][i] = *(const GAS unsigned*)((cptr)p.k + (size_t)(128 * kt - 3 + i) * 1280 + 128 * kc + 4 * cpl + 64 * it);
      }
    }
    SB0();
    BAR0();
    PRIO(APRIO);
    EPOCH_FIN();
    const unsigned myap = W_AP + 4 * aw;
    const unsigned ka = L_P0 + tg8 * 1024 + ((((cpl >> 2) ^ tg8) & 7) << 4) + (cpl & 3) * 4;
    const unsigned kp = tg8 ? L_P0 + (tg8 - 1) * 1024 + ((((cpl >> 2) ^ (tg8 - 1)) & 7) << 4) + (cpl & 3) * 4 : ka;
    if (!kvrole) {
      wait4(W_PL, 1);
      if (wn < 90) {
      const int g = lane >> 4, q = (lane & 15) >> 2, pp = lane & 3;
#pragma unroll
      for (int it = 0; it < 2; ++it) {
        const int dt = 8 * (wn % 5) + aw + 4 * it;
#pragma unroll
        for (int u = 0; u < 3; ++u) {
          v2u h2[2];
#pragma unroll
          for (int t = 0; t < 2; ++t)
            h2[t] = ldstr(L_P0 + (32 * u + 16 * t + 4 * g + q) * 256 + (((2 * (aw + 4 * it) + (pp >> 1)) ^ ((q << 2) | (g & 3))) << 4) + 8 * (pp & 1));
          __builtin_amdgcn_raw_buffer_store_b128(v4u{h2[0].x, h2[0].y, h2[1].x, h2[1].y}, rs, WS_W2T + (((dt / 5) * 54 + 3 * (wn / 5) + u) * 5 + dt % 5) * 1024 + l16, 0, AUX_SC1);
        }
      }
    }
      SB0();
    }
    {
      f32x2 a[5] = {bf2(xa.x), bf2(xa.y), bf2(xa.z), bf2(xa.w), bf2(xb)}, c[5] = {bf2(xc.x), bf2(xc.y), bf2(xc.z), bf2(xc.w), bf2(xd)};
      f32x2 sa = (a[0] + a[1]) + (a[2] + a[3]) + a[4], sc = (c[0] + c[1]) + (c[2] + c[3]) + c[4];
      const float ma = wave_sum(sa[0] + sa[1]) * (1.0f / D), mc = wave_sum(sc[0] + sc[1]) * (1.0f / D);
      f32x2 qa = {0.f, 0.f}, qc = {0.f, 0.f};
#pragma unroll
      for (int i = 0; i < 5; ++i) { f32x2 e = a[i] - ma; qa += e * e; f32x2 f = c[i] - mc; qc += f * f; }
      const float va = wave_sum(qa[0] + qa[1]) * (1.0f / D), vc = wave_sum(qc[0] + qc[1]) * (1.0f / D);
      const unsigned vb = pk_bf(bf2(pk_bf(va, vc)) + 1e-5f);
      const v4u o = {pk_bf(ma, __builtin_amdgcn_sqrtf(bf_lo(vb))), epoch, pk_bf(mc, __builtin_amdgcn_sqrtf(bf_hi(vb))), epoch};
      if (lane < 2) __builtin_amdgcn_raw_buffer_store_b64(lane ? v2u{o.z, o.w} : v2u{o.x, o.y}, rs, WS_STATS + (srow + lane) * 8, 0, AUX_SC1);
    }
    if (!kvrole) {
      WAITV(1);
      pub(W_MK + 4 * aw, 1, lane);
      if (aw == 0) { wait4(W_MK, 1); if (lane == 0) flag_set(p.ws + WS_FW, wn * 4, epoch); }
    }
    if (rt < 10) {
      wait4(W_FULL, 1);
      const int g = lane >> 4, q = (lane & 15) >> 2, pp = lane & 3;
      float c0 = 0.f, c1 = 0.f, c2 = 0.f, c3 = 0.f;
#pragma unroll
      for (int t = 0; t < 2; ++t) {
        const unsigned rowb = 16384 + (8 * g + 4 * t + q) * 256 + 8 * (pp & 1), swz = (q << 2) | ((2 * g + t) & 3);
        const unsigned a0 = rowb + (((4 * aw + (pp >> 1)) ^ swz) << 4), a1 = rowb + (((4 * aw + 2 + (pp >> 1)) ^ swz) << 4);
#pragma unroll
        for (int u = 0; u < 2; ++u) {
          v2u r0 = ldstr(a0 + 8192 * u), r1 = ldstr(a1 + 8192 * u);
          c0 = dot2ones(r0.x, c0); c1 = dot2ones(r1.x, c1); c2 = dot2ones(r0.y, c2); c3 = dot2ones(r1.y, c3);
        }
      }
      FENCE();
      pub(myap, 1, lane);
      c0 = xsum16_32(c0 + c2); c1 = xsum16_32(c1 + c3);
      if (lane < 32)
        __builtin_amdgcn_raw_buffer_store_b64(v2u{asu(lane < 16 ? c0 : c1), epoch}, rs, WS_CSP + (rt * 1792 + 64 * u0 + 32 * (isfull ? aw : aw & 1) + lane) * 8, 0, AUX_SC1);
      SB0();
    }
    STAMP(6);
    wait4(W_PL, 1);
    STAMP(1);
    if (kvrole) {
      unsigned kk[2][11], vv[2][8];
#pragma unroll
      for (int it = 0; it < 2; ++it) {
#pragma unroll
        for (int i = 0; i < 3; ++i) kk[it][i] = lds32((kp ^ (64 * it)) + (5 + i) * 128);
#pragma unroll
        for (int i = 0; i < 8; ++i) { kk[it][3 + i] = lds32((ka ^ (64 * it)) + i * 128); vv[it][i] = lds32((ka ^ (64 * it)) + 16384 + i * 128); }
      }
      const unsigned kvo = WS_KVR + (64 * kc + 2 * cpl) * KVR_ROW + (2040 - 128 * kt - 8 * tg8) * 2;
      v4u ra[2], rb[2];
#pragma unroll
      for (int it = 0; it < 2; ++it) {
        const v4u sh = shk4[it];
        const f32x2 s0 = {bf_lo(sh.x), bf_lo(sh.z)}, s1 = {bf_hi(sh.x), bf_hi(sh.z)}, s2 = {bf_lo(sh.y), bf_lo(sh.w)}, s3 = {bf_hi(sh.y), bf_hi(sh.w)};
        f32x2 kf[11], o[8];
#pragma unroll
        for (int i = 0; i < 11; ++i) kf[i] = bf2(i < 3 ? (tg8 ? kk[it][i] : halo[it][i]) : kk[it][i]);
#pragma unroll
        for (int i = 0; i < 8; ++i) {
          f32x2 s = kf[i] * s3 + kf[i + 1] * s2 + kf[i + 2] * s1 + kf[i + 3] * s0;
          o[i] = bf2(pk_bf(s)) * bf2(i == 7 ? (tg8 == 15 ? vlast[it] : vv[it][i]) : vv[it][i]);
        }
        ra[it] = v4u{pk_bf(o[7][0], o[6][0]), pk_bf(o[5][0], o[4][0]), pk_bf(o[3][0], o[2][0]), pk_bf(o[1][0], o[0][0])};
        rb[it] = v4u{pk_bf(o[7][1], o[6][1]), pk_bf(o[5][1], o[4][1]), pk_bf(o[3][1], o[2][1]), pk_bf(o[1][1], o[0][1])};
        USE(ra[it]); USE(rb[it]);
      }
      STAMP(7);
#pragma unroll
      for (int it = 0; it < 2; ++it) {
        const unsigned o2 = kvo + it * (32 * KVR_ROW);
        __builtin_amdgcn_raw_buffer_store_b128(ra[it], rs, o2, 0, AUX_SC1);
        __builtin_amdgcn_raw_buffer_store_b128(rb[it], rs, o2 + KVR_ROW, 0, AUX_SC1);
        if (kt == 0 && tg8 == 0) {
          __builtin_amdgcn_raw_buffer_store_b128(v4u{0u, 0u, 0u, 0u}, rs, o2 + 16, 0, AUX_SC1);
          __builtin_amdgcn_raw_buffer_store_b128(v4u{0u, 0u, 0u, 0u}, rs, o2 + KVR_ROW + 16, 0, AUX_SC1);
        }
      }
    }
    SB0();
    STAMP(2);
    WAITV(0);
    pub(myap, 3, lane);
    wait4(W_AP, 3);
    if (aw == 0) {
      wait4(W_FREE, 1);
      if (lane == 32 || (kvrole && lane < 32))
        flag_set(p.ws, lane == 32 ? WS_FP0 + (kvrole ? kvn : 160 + wn) * 4 : WS_MBKV + (kc * 32 + lane) * 128 + kt * 4, epoch);
    }
    PRIO(1);
    STAMP(3);
    LATE_CONSTS();
    H_DMA(); CONV_PADS();
    if (aw == 1) {
      if (idx == 31) {
#pragma unroll 1
        for (unsigned spins = 0; spins < SPIN_CAP; ++spins) {
          __builtin_amdgcn_s_sleep(8);
          const v2u f = __builtin_amdgcn_raw_buffer_load_b64(rs, WS_FW + (lane < 48 ? lane : 0) * 8, 0, AUX_SC1);
          if (__all(f.x == epoch && f.y == epoch)) break;
        }
        if (lane < 4) flag_set(p.ws + WS_W2R, (xcd * 4 + lane) * 128, epoch);
      } else {
        wait4(W_FREE, 5);
        poll1(p.ws, WS_W2R + (xcd * 4 + (idx & 3)) * 128, epoch);
      }
      pub(myap, 4, lane);
    }
    STAMP(19);
    if (aw < 2) {
      if (aw == 0) {
        wait4(W_FREE, 6);
        poll1(p.ws, WS_MBKV + ((chb + 3) >> 1) * 128 + (lane & 15) * 4, epoch);
        pub(myap, 4, lane);
      }
      wait4(W_FREE, 7);
    } else {
      wait4(W_FREE, 6);
      v4u gr[10];
      const unsigned gb = aw == 2 ? WS_STATS + 128 * rt * 8 + l16 : WS_CSP + 64 * u0 * 8 + (isfull ? l16 : (l16 & 511));
#pragma unroll 1
      for (unsigned spins = 0; spins < SPIN_CAP; ++spins) {
        gr[0] = __builtin_amdgcn_raw_buffer_load_b128(rs, gb, 0, AUX_SC1);
        unsigned bad = 0;
        if (aw == 3) {
#pragma unroll
          for (int i = 1; i < 10; ++i) gr[i] = __builtin_amdgcn_raw_buffer_load_b128(rs, gb, i * (1792 * 8), AUX_SC1);
          float c0 = asf(gr[0].x), c1 = asf(gr[0].z);
#pragma unroll
          for (int i = 1; i < 10; ++i) { bad |= (gr[i].y ^ epoch) | (gr[i].w ^ epoch); c0 += asf(gr[i].x); c1 += asf(gr[i].z); }
          gr[0].x = asu(c0); gr[0].z = asu(c1);
        }
        bad |= (gr[0].y ^ epoch) | (gr[0].w ^ epoch);
        if (__all(bad == 0u)) break;
        __builtin_amdgcn_s_sleep(4);
      }
      stq = v2u{gr[0].x, gr[0].z};
      wait4(W_FREE, 8);
      if (aw == 2) {
        const float r0 = __builtin_amdgcn_rcpf(bf_hi(gr[0].x)) * LOG2E, r1 = __builtin_amdgcn_rcpf(bf_hi(gr[0].z)) * LOG2E;
        sts128(L_STATS + l16, __builtin_bit_cast(v4u, f32x4{-r0, bf_lo(gr[0].x) * r0, -r1, bf_lo(gr[0].z) * r1}));
      } else if (isfull || lane < 32) sts64(lcs + lane * 8, v2u{gr[0].x, gr[0].z});
      FENCE();
      pub(myap, 5, lane);
    }
    STAMP(4);
    wait1(W_AP, 4);
    R_DMA();
    WAITV(0);
    STAMP(5);
  }
  LATE_CONSTS();
  STAMP(8);
  if (convw) {
    const int rh = w & 1;
    const unsigned cvp = W_CV + area * 8, mycv = cvp + rh * 4;
    pub(mycv, 1, lane);
    const int i16 = lane & 15, kg = lane >> 4;
    const unsigned rbase = cb + ((i16 & 1) ? CH_R1 : CH_R0);
    unsigned va = rbase + 4032 - 3584 - 64 * (i16 >> 1) + 16 * kg + 256 * rh;
    const unsigned vamax = rbase + 4096;
    const unsigned hbase = cb + ((i16 & 1) ? CH_H0 : CH_H1);
    unsigned vb = hbase + 4 * i16 - ((i16 & 1) ? 60 : 64) + 16 * kg + 256 * rh;
    const unsigned vbmin = hbase - 16;
    USE(va); USE(vb);
    f32x4 acc[8];
#pragma unroll
    for (int i = 0; i < 8; ++i) acc[i] = f32x4{0.f, 0.f, 0.f, 0.f};
    const float dD = bf_lo(*(kptr)((cptr)p.sD + (cch >> 1) * 4) >> ((cch & 1) * 16));
    if (w >= 8) { wait2(cvp, 1); wait4(W_FREE, 11); } else wait4(W_FULL, 12);
    PRIO(3);
#define LDA_(i, o) lds128(va + 3584 - 512 * (i) + (o))
#define LDB_(dst, i, o) do { v2u lo = lds64v(vb + 512 * (i) + (o)), hi = lds64v(vb + 512 * (i) + (o) + 8); dst = v4u{lo.x, lo.y, hi.x, hi.y}; } while (0)
#define LDP(i, o) do { A[i] = LDA_(i, o); LDB_(B[i], i, o); } while (0)
#define STEP(m) do { _Pragma("unroll") for (int o = 0; o < (m); ++o) if ((m) + o <= 7) acc[(m) + o] = mfma(A[m], B[o], acc[(m) + o]); \
      if (2 * (m) <= 7) acc[2 * (m)] = mfma(A[m], B[m], acc[2 * (m)]); \
      _Pragma("unroll") for (int o = 0; o < (m); ++o) if ((m) + o <= 7) acc[(m) + o] = mfma(A[o], B[m], acc[(m) + o]); } while (0)
#define ILV(n) do { _Pragma("unroll") for (int i_ = 0; i_ < (n); ++i_) { SGB(0x8, 1); SGB(0x100, 1); } SGB(0x8, 16); SB0(); } while (0)
    v4u A[8], B[8];
    {
      A[0] = lds128(min(va + 3584, vamax));
      v2u lo = lds64v(max(vb, vbmin)), hi = lds64v(max(vb + 8, vbmin));
      B[0] = v4u{lo.x, lo.y, hi.x, hi.y};
      LDP(1, 0); LDP(2, 0); LDP(3, 0);
    }
    SB0();
#pragma unroll 1
    for (int r = 0; r < 4; ++r) {
      v4u nA0, nB0, nA1, nB1;
      LDP(4, 0); STEP(0); STEP(1); ILV(3);
      LDP(5, 0); STEP(2); ILV(3);
      LDP(6, 0); LDP(7, 0); STEP(3); ILV(6);
      nA0 = lds128(min(va + 3584 + 64, vamax)); LDB_(nB0, 0, 64); nA1 = LDA_(1, 64); LDB_(nB1, 1, 64); STEP(4); ILV(6);
      LDP(3, 64); STEP(5); ILV(3);
      LDP(2, 64); STEP(6); STEP(7); ILV(3);
      A[0] = nA0; B[0] = nB0; A[1] = nA1; B[1] = nB1;
      va += 64; vb += 64; USE(va); USE(vb);
    }
    const int chunk = 128 * rh + lane;
    const v4u kva = lds128(cb + CH_R1 + (255 - chunk) * 16), kvb = lds128(cb + CH_R1 + (191 - chunk) * 16);
    STAMP(9);
    PRIO(3);
    FENCE();
    pub(mycv, 2, lane); wait2(cvp, 2);
    {
      const unsigned a = cb + rh * 8192 + (64 * (lane >> 4) + 2 * (lane & 15)) * 4;
#pragma unroll
      for (int i = 0; i < 8; ++i) {
        sts64(a + 1024 * i, __builtin_bit_cast(v2u, f32x2{acc[i][0], acc[i][1]}));
        sts64(a + 1024 * i + 128, __builtin_bit_cast(v2u, f32x2{acc[i][2], acc[i][3]}));
      }
    }
    FENCE();
    pub(mycv, 3, lane); wait2(cvp, 3);
    conv_final(rs, cb, cch, chunk, kva, dD);
    conv_final(rs, cb, cch, chunk + 64, kvb, dD);
    FENCE();
    pub(mycv, 4, lane);
    if (w >= 8) { WAITV(0); if (lane == 0) flag_set(p.ws + WS_FY, (rh * 640 + cch) * 4, epoch); }
  }
  STAMP(10);
  if (w < 4) {
    const unsigned vfull = pub_addr(W_FULL + 4 * w, lane);
    if (!isfull) {
      wait4(W_FREE, 8);
      H_DMA(); CONV_PADS();
    }
    wait1(W_AP + 4, 4);
    v4u RG[4][8];
#define RG_LD(i, g) do { _Pragma("unroll") for (int j = 0; j < 8; ++j) { if ((j & 3) == 0) WAITV(8); \
      RG[i][j] = __builtin_amdgcn_raw_buffer_load_b128(rs, (unsigned)(w2s - p.ws) + l16, (g) * 15360 + j * 1024, AUX_SC1); } SB0(); } while (0)
#define RG_ST(i, g) do { wait4(W_FREE, 21 + (g) - 4); unsigned a_ = w2d + wslot(g) + l16; USE(a_); \
      _Pragma("unroll") for (int j = 0; j < 8; ++j) sts128(a_ + j * 1024, RG[i][j]); FENCE(); pubv(vfull, 21 + (g)); } while (0)
    wait4(W_FREE, 8);
    W2GROUP(2);
    if (isfull) W2GROUP(3); else { wait1(W_AP, 4); R_DMA(); }
    wait4(W_FREE, 9); W2GROUP(0);
    wait4(W_FREE, 10); W2GROUP(1);
    pubv(vfull, 12);
    RG_LD(0, 4);
    WAITV(8); pubv(vfull, isfull ? 24 : 23);
    RG_LD(1, 5); RG_LD(2, 6); RG_LD(3, 7);
    STAMP(11);
    if (!isfull) { wait4(W_CV, 4); W2GROUP(3); WAITV(0); pubv(vfull, 24); }
    PRIO(3);
    RG_ST(0, 4);
    RG_LD(0, 8);
    RG_ST(1, 5); RG_ST(2, 6); RG_ST(3, 7); RG_ST(0, 8);
    if (b == 0 && w == 3) {
      poll4(p.ws + WS_FP0, l16, epoch);
      if (lane == 0) __builtin_amdgcn_raw_buffer_store_b32(epoch, rs, WS_EPOCH, 0, AUX_SC1);
    }
    PRIO(0);
  } else if (w < 8) {
    const int cw = w - 4, tp = cw & 1, kh = cw >> 1;
    const unsigned vfree = pub_addr(W_FREE + 4 * cw, lane);
    const unsigned hoff = WS_HF + ((4 * tt64 + 2 * tp) * 54 + 27 * kh) * 1024 + l16;
    constexpr int HW = HWIN;
    v4u hf[27][2];
    f32x4 acc[2][5];
#pragma unroll
    for (int i = 0; i < 5; ++i) acc[0][i] = acc[1][i] = f32x4{0.f, 0.f, 0.f, 0.f};
    unsigned wa[2] = {kh * 15360 + l16, kh * 15360 + l16 + 65536};
    USE(wa[0]); USE(wa[1]);
    PRIO(2);
    {
      const unsigned fho = ((2 * tt64 + tp) * 32 + 13 * kh + lane) * 4;
      const unsigned v0 = atom_rd(p.ws + WS_FH, fho, 0x3fffull, epoch);
      const bool ok0 = wait_eq1<0>(v0, epoch);
      if (convw && lane == 0) flag_set(p.ws + WS_FY, ((w & 1) * 640 + cch) * 4, epoch);
      if (!ok0) poll_atomic(p.ws + WS_FH, fho, 0x3fffull, epoch);
    }
#define H_LD(i) do { hf[i][0] = __builtin_amdgcn_raw_buffer_load_b128(rs, hoff, (i) * 1024, AUX_SC1); \
      hf[i][1] = __builtin_amdgcn_raw_buffer_load_b128(rs, hoff, (i) * 1024 + 54 * 1024, AUX_SC1); } while (0)
#pragma unroll
    for (int i = 0; i < HW; ++i) H_LD(i);
    STAMP(11);
    v4u a0[5], a1[5];
    v4u fl;
    unsigned flm = 0;
#define P3_AD(ks, i) (wa[wslot((ks) / 3) >> 16] + (wslot((ks) / 3) & 65535) + (((ks) % 3) * 5 + (i)) * 1024)
#define P3_STEP(A, N, ks) do { \
      if (((ks) + 1) % 3 == 0 && (ks) < 26) { if (__builtin_expect(flm < 21 + ((ks) + 1) / 3, 0)) wait4(W_FULL, 21 + ((ks) + 1) / 3); FENCE(); } \
      _Pragma("unroll") for (int i = 0; i < 5; ++i) { \
        mfma_ip(acc[0][i], A[i], hf[ks][0]); \
        if ((ks) < 26) N[i] = lds128v(P3_AD((ks) + 1, i)); \
        mfma_ip(acc[1][i], A[i], hf[ks][1]); \
        if (i == 1 && (ks) % 3 == 0 && (ks) < 24) fl = peek4(W_FULL); \
        if (i == 3 && (ks) % 3 == 1 && (ks) < 24) flm = min4u(fl); \
        if (i == 1 && (ks) == 25) fl = peek4(W_FREE); \
      } \
      if (((ks) + 1) % 3 == 2 && (ks) < 25) pubv(vfree, 21 + ((ks) + 1) / 3); \
      SB0(); \
      if ((ks) + HW < 27) H_LD((ks) + HW); \
      SB0(); \
    } while (0)
    wait4(W_FULL, 21);
#pragma unroll
    for (int i = 0; i < 5; ++i) a0[i] = lds128v(P3_AD(0, i));
    SB0();
#pragma unroll
    for (int ks = 0; ks < 26; ks += 2) {
      P3_STEP(a0, a1, ks);
      P3_STEP(a1, a0, ks + 1);
      if (ks == 2) STAMP(12);
      if (ks == 12) STAMP(13);
    }
    P3_STEP(a0, a1, 26);
    const v4u fr = fl;
    asm volatile("s_nop 7\n\ts_nop 7" ::: "memory");
    STAMP(14);
    if (min4u(fr) < 27) wait4(W_FREE, 27);
    FENCE();
    {
      const unsigned oa = L_O + kh * (64 * OROW) + (32 * tp + (lane & 15)) * OROW + (lane >> 4) * 16;
#pragma unroll
      for (int i = 0; i < 5; ++i) { sts128(oa + i * 64, __builtin_bit_cast(v4u, acc[0][i])); sts128(oa + 16 * OROW + i * 64, __builtin_bit_cast(v4u, acc[1][i])); }
    }
    PRIO(0);
  } else {
    const int aw = w - 8;
    PRIO(1);
    wait4(W_CV + 16, 4);
    if (!convw) { pub(W_AP + 4 * aw, 6, lane); wait4(W_AP, 6); }
    const int npc = aw < 3 ? 3 : 1;
#pragma unroll 1
    for (int i = 0; i < npc; ++i) {
      const int it = (3 * aw + i) * 64 + lane;
      dma16((cptr)p.x + (size_t)(64 * tt64) * 1280 + c80 * 160, (it / 10) * 1280 + (it % 10) * 16, L_X + (3 * aw + i) * 1024);
    }
    if (aw == 2) sts64(L_XN + lane * 8, stq);
    STAMP(11);
    wait4(W_FREE, 23);
    poll_atomic(p.ws + WS_FY, ((tt64 >> 4) * 640 + c80 * 80 + aw * 24 + lane) * 4, aw < 3 ? 0xffffffull : 0xffull, epoch);
    STAMP(12);
#pragma unroll 1
    for (int i = 0; i < npc; ++i) {
      const int pc = 3 * aw + i;
      dma16s(p.ws + WS_YT + (size_t)(c80 * 80 + 8 * pc) * 4096 + tt64 * 128, (lane >> 3) * 4096 + (((lane ^ pc) & 7) << 4), L_Y + pc * 1024);
    }
    WAITV(0);
    PRIO(0);
  }
  STAMP(15);
  BAR0();
  STAMP(16);
  if (tid < 640) {
    const int ftok = tid / 10, fc8 = tid % 10;
    const unsigned oa = L_O + ftok * OROW + fc8 * 32;
    f32x4 o0 = __builtin_bit_cast(f32x4, lds128(oa)) + __builtin_bit_cast(f32x4, lds128(oa + 64 * OROW));
    f32x4 o1 = __builtin_bit_cast(f32x4, lds128(oa + 16)) + __builtin_bit_cast(f32x4, lds128(oa + 64 * OROW + 16));
    const v4u xv = lds128(L_X + tid * 16);
    const unsigned fs = lds32(L_XN + (64 * (tt64 & 1) + ftok) * 4);
    const float fm = bf_lo(fs), fr = __builtin_amdgcn_rcpf(bf_hi(fs));
    const unsigned ya = L_Y + fc8 * 1024 + ((((ftok >> 3) ^ fc8) & 7) << 4) + (ftok & 7) * 2;
    f32x2 o[4] = {{o0[0], o0[1]}, {o0[2], o0[3]}, {o1[0], o1[1]}, {o1[2], o1[3]}};
    unsigned xw[4] = {xv.x, xv.y, xv.z, xv.w}, rw[4];
#pragma unroll
    for (int j = 0; j < 4; ++j) {
      const f32x2 y = {bf_lo(*(const LAS u16*)(size_t)(ya + 256 * j)), bf_lo(*(const LAS u16*)(size_t)(ya + 256 * j + 128))};
      const f32x2 gb = silu_z(o[j] * -LOG2E);
      const f32x2 xf = bf2(xw[j]);
      rw[j] = pk_bf(xf + bf2(pk_bf((xf - fm) * fr)) * y * gb);
    }
    *(GAS v4u*)((unsigned char*)p.out + (size_t)(64 * tt64 + ftok) * 1280 + c80 * 160 + fc8 * 16) = v4u{rw[0], rw[1], rw[2], rw[3]};
  }
  STAMP(17);
  if (w == DIAGW) { const unsigned tend = 4000u + 10u * (dg1 - dg0); while ((unsigned)__builtin_amdgcn_s_memrealtime() - dg0 < tend) __builtin_amdgcn_s_sleep(4); }
}

extern "C" void kernel_launch(void* const* d_in, const int* in_sizes, int n_in, void* d_out, int out_size, void* d_ws, size_t ws_size,
                              hipStream_t stream) {
  Params p;
  p.x = (const u16*)d_in[0]; p.k = (const u16*)d_in[1]; p.v = (const u16*)d_in[2];
  p.shk = (const u16*)d_in[3]; p.sk = (const u16*)d_in[4]; p.sD = (const u16*)d_in[5];
  p.w1 = (const u16*)d_in[7]; p.w2 = (const u16*)d_in[8];
  p.out = (u16*)d_out; p.ws = (unsigned char*)d_ws;
  fused_kernel<<<NWG, NTHR, 0, stream>>>(p);
}
```

```cpp
#include <hip/hip_runtime.h>
#include <stdint.h>

typedef unsigned short u16;
typedef unsigned v4u __attribute__((ext_vector_type(4)));
typedef unsigned v2u __attribute__((ext_vector_type(2)));
typedef float f32x4 __attribute__((ext_vector_type(4)));
typedef float f32x2 __attribute__((ext_vector_type(2)));
typedef short bf16x8 __attribute__((ext_vector_type(8)));
typedef short v4s __attribute__((ext_vector_type(4)));
typedef __bf16 v2bf __attribute__((ext_vector_type(2)));
#define LAS __attribute__((address_space(3)))
#define GAS __attribute__((address_space(1)))
#define DEV __device__ __forceinline__
typedef const unsigned char* cptr;
typedef const __attribute__((address_space(4))) unsigned* kptr;

constexpr int S = 2048, D = 640, F = 1728;
constexpr int NWG = 256, NTHR = 768;
constexpr unsigned WS_EPOCH = 0;
constexpr unsigned WS_FP0 = 1024;
constexpr unsigned WS_FH = 4096;
constexpr unsigned WS_FY = 16384;
constexpr unsigned WS_STATS = 32768;
constexpr unsigned KVR_ROW = 4224;
constexpr unsigned WS_KVR = 65536;
constexpr unsigned WS_W2T = WS_KVR + 640 * KVR_ROW;
constexpr unsigned WS_HF = WS_W2T + 8 * 54 * 5 * 1024;
constexpr unsigned WS_YT = WS_HF + 128 * 54 * 1024;
constexpr unsigned WS_CSP = WS_YT + 640 * 4096;
constexpr unsigned WS_MBKV = WS_CSP + 147456;
constexpr unsigned WS_W2R = WS_MBKV + 40960;
constexpr unsigned WS_END = WS_W2R + 4096;
constexpr unsigned L_E = 131072;
constexpr unsigned L_STATS = 65536 + 30720;
constexpr unsigned L_CS = 98304 + 30720;
constexpr unsigned CH_R1 = 0, CH_Z1 = 4096, CH_R0 = 4128, CH_Z0 = 8224, CH_H0 = 8240, CH_PF = 12416, CH_H1 = 12432, CH_BYTES = 16528;
constexpr unsigned L_SYNC = 163840 - 128;
constexpr unsigned W_FULL = L_SYNC, W_FREE = L_SYNC + 16, W_AP = L_SYNC + 32, W_CV = L_SYNC + 48, W_PL = L_SYNC + 80, W_MK = L_SYNC + 96;
constexpr unsigned L_AREA0 = L_SYNC - 4 * CH_BYTES;
constexpr unsigned L_P0 = L_E;
constexpr unsigned L_Y = L_E;
constexpr unsigned L_X = L_E + 10240;
constexpr unsigned L_XN = L_E + 20480;
constexpr unsigned L_O = 32768;
constexpr unsigned OROW = 336;
constexpr unsigned SPIN_CAP = 1u << 15;
constexpr int AUX_SC1 = 16, AUX_NT = 2;
constexpr unsigned CODE_PF_BYTES = 32768;
constexpr unsigned wslot(int g) { return (g & 3) * 32768u; }

DEV float asf(unsigned u) { return __builtin_bit_cast(float, u); }
DEV unsigned asu(float f) { return __builtin_bit_cast(unsigned, f); }
DEV float bf_lo(unsigned u) { return __builtin_bit_cast(float, u << 16); }
DEV float bf_hi(unsigned u) { return __builtin_bit_cast(float, u & 0xffff0000u); }
DEV f32x2 bf2(unsigned u) { return f32x2{bf_lo(u), bf_hi(u)}; }
DEV unsigned pk_bf(float a, float b) { v2bf r = {(__bf16)a, (__bf16)b}; return __builtin_bit_cast(unsigned, r); }
DEV unsigned pk_bf(f32x2 a) { return pk_bf(a[0], a[1]); }
DEV f32x4 mfma(v4u a, v4u b, f32x4 c) {
  return __builtin_amdgcn_mfma_f32_16x16x32_bf16(__builtin_bit_cast(bf16x8, a), __builtin_bit_cast(bf16x8, b), c, 0, 0, 0);
}
DEV void mfma_ip(f32x4& c, v4u a, v4u b) { asm volatile("v_mfma_f32_16x16x32_bf16 %0, %1, %2, %0" : "+v"(c) : "v"(a), "v"(b)); }
DEV v4u lds128v(unsigned a) { return *(const volatile LAS v4u*)(size_t)a; }
constexpr float LOG2E = 1.44269504088896f;
DEV f32x2 silu_z(f32x2 z) {
  f32x2 e = {__builtin_amdgcn_exp2f(z[0]), __builtin_amdgcn_exp2f(z[1])};
  f32x2 d = e * -LOG2E - LOG2E;
  f32x2 r = {__builtin_amdgcn_rcpf(d[0]), __builtin_amdgcn_rcpf(d[1])};
  return z * r;
}
DEV float dot2ones(unsigned u, float acc) {
  return __builtin_amdgcn_fdot2_f32_bf16(__builtin_bit_cast(v2bf, u), __builtin_bit_cast(v2bf, 0x3f803f80u), acc, false);
}

DEV void dma16(const void* sbase, unsigned voff, unsigned ldsoff) {
  asm volatile("s_mov_b32 m0, %2\n\ts_nop 0\n\tglobal_load_lds_dwordx4 %0, %1" ::"v"(voff), "s"(sbase), "s"(ldsoff) : "memory");
}
DEV void dma16s(const void* sbase, unsigned voff, unsigned ldsoff) {
  asm volatile("s_mov_b32 m0, %2\n\ts_nop 0\n\tglobal_load_lds_dwordx4 %0, %1 sc1" ::"v"(voff), "s"(sbase), "s"(ldsoff) : "memory");
}
DEV void dma4km(const void* sbase, unsigned voff, unsigned ldsoff, unsigned long long lastmask) {
  asm volatile("s_mov_b32 m0, %2\n\ts_nop 0\n\tglobal_load_lds_dwordx4 %0, %1\n\tglobal_load_lds_dwordx4 %0, %1 offset:1024\n\t"
               "global_load_lds_dwordx4 %0, %1 offset:2048\n\ts_mov_b64 exec, %3\n\tglobal_load_lds_dwordx4 %0, %1 offset:3072\n\ts_mov_b64 exec, -1"
               ::"v"(voff), "s"(sbase), "s"(ldsoff), "s"(lastmask) : "memory");
}
DEV void dma4ks(const void* sbase, unsigned voff, unsigned ldsoff) {
  asm volatile("s_mov_b32 m0, %2\n\ts_nop 0\n\tglobal_load_lds_dwordx4 %0, %1 sc1\n\tglobal_load_lds_dwordx4 %0, %1 offset:1024 sc1\n\t"
               "global_load_lds_dwordx4 %0, %1 offset:2048 sc1\n\tglobal_load_lds_dwordx4 %0, %1 offset:3072 sc1" ::"v"(voff), "s"(sbase), "s"(ldsoff) : "memory");
}
DEV void dma4v(const void* sbase, const unsigned (&vo)[4], unsigned ldsoff) {
  asm volatile("s_mov_b32 m0, %5\n\ts_nop 0\n\tglobal_load_lds_dwordx4 %0, %4\n\tglobal_load_lds_dwordx4 %1, %4 offset:1024\n\t"
               "global_load_lds_dwordx4 %2, %4 offset:2048\n\tglobal_load_lds_dwordx4 %3, %4 offset:3072"
               ::"v"(vo[0]), "v"(vo[1]), "v"(vo[2]), "v"(vo[3]), "s"(sbase), "s"(ldsoff) : "memory");
}
DEV void dma4vx(const void* sbase, const unsigned (&vo)[4], unsigned ldsoff, unsigned long long mask) {
  asm volatile("s_mov_b32 m0, %5\n\ts_mov_b64 exec, %6\n\tglobal_load_lds_dwordx4 %0, %4\n\tglobal_load_lds_dwordx4 %1, %4 offset:1024\n\t"
               "global_load_lds_dwordx4 %2, %4 offset:2048\n\tglobal_load_lds_dwordx4 %3, %4 offset:3072\n\ts_mov_b64 exec, -1"
               ::"v"(vo[0]), "v"(vo[1]), "v"(vo[2]), "v"(vo[3]), "s"(sbase), "s"(ldsoff), "s"(mask) : "memory");
}
DEV void dma4vm(const void* sbase, const unsigned (&vo)[4], unsigned ldsoff, unsigned long long lastmask) {
  asm volatile("s_mov_b32 m0, %5\n\ts_nop 0\n\tglobal_load_lds_dwordx4 %0, %4\n\tglobal_load_lds_dwordx4 %1, %4 offset:1024\n\t"
               "global_load_lds_dwordx4 %2, %4 offset:2048\n\ts_mov_b64 exec, %6\n\tglobal_load_lds_dwordx4 %3, %4 offset:3072\n\ts_mov_b64 exec, -1"
               ::"v"(vo[0]), "v"(vo[1]), "v"(vo[2]), "v"(vo[3]), "s"(sbase), "s"(ldsoff), "s"(lastmask) : "memory");
}
DEV v4u ld128a(const void* sbase, unsigned voff) { v4u r; asm volatile("global_load_dwordx4 %0, %1, %2 sc1" : "=v"(r) : "v"(voff), "s"(sbase) : "memory"); return r; }
DEV unsigned atom_rd(const void* sbase, unsigned voff, unsigned long long mask, unsigned dflt) {
  unsigned r; unsigned long long sv;
  asm volatile("v_mov_b32 %0, %6\n\ts_mov_b64 %1, exec\n\ts_mov_b64 exec, %5\n\tglobal_atomic_or %0, %2, %3, %4 sc0 sc1\n\ts_mov_b64 exec, %1"
               : "=&v"(r), "=&s"(sv) : "v"(voff), "v"(0u), "s"(sbase), "s"(mask), "s"(dflt) : "memory");
  return r;
}
DEV unsigned ld32a(const void* sbase, unsigned voff) { unsigned r; asm volatile("global_load_dword %0, %1, %2 sc1" : "=v"(r) : "v"(voff), "s"(sbase) : "memory"); return r; }
#define WAITV(n) asm volatile("s_waitcnt vmcnt(" #n ")" ::: "memory")
#define USE(x) asm volatile("" : "+v"(x))
#define BAR0() do { asm volatile("s_waitcnt lgkmcnt(0)" ::: "memory"); __builtin_amdgcn_s_barrier(); asm volatile("" ::: "memory"); } while (0)
#define SGB(m, n) __builtin_amdgcn_sched_group_barrier(m, n, 0)
#define SB0() __builtin_amdgcn_sched_barrier(0)

DEV v4u lds128(unsigned a) { return *(const LAS v4u*)(size_t)a; }
DEV v2u lds64(unsigned a) { return *(const LAS v2u*)(size_t)a; }
DEV v2u lds64v(unsigned a) { return *(const volatile LAS v2u*)(size_t)a; }
DEV unsigned lds32(unsigned a) { return *(const LAS unsigned*)(size_t)a; }
DEV void sts128(unsigned a, v4u v) { *(LAS v4u*)(size_t)a = v; }
DEV void sts64(unsigned a, v2u v) { *(LAS v2u*)(size_t)a = v; }
DEV void sts32(unsigned a, unsigned v) { *(LAS unsigned*)(size_t)a = v; }
DEV v2u ldstr(unsigned a) { return __builtin_bit_cast(v2u, __builtin_amdgcn_ds_read_tr16_b64_v4i16((LAS v4s*)(size_t)a)); }

DEV void pub(unsigned a, unsigned v, int lane) { if (lane == 0) *(volatile LAS unsigned*)(size_t)a = v; }
DEV unsigned pub_addr(unsigned a, int lane) { unsigned r = lane == 0 ? a : 0x30000u + ((a + lane * 4) & 255); asm volatile("" : "+v"(r)); return r; }
DEV void pubv(unsigned va, unsigned v) { *(volatile LAS unsigned*)(size_t)va = v; }
DEV unsigned min4u(v4u v) { return (unsigned)__builtin_amdgcn_readfirstlane(min(min(v.x, v.y), min(v.z, v.w))); }
DEV v4u peek4(unsigned a) { return *(const volatile LAS v4u*)(size_t)a; }
DEV bool ge4(v4u v, unsigned t) { return (unsigned)__builtin_amdgcn_readfirstlane(min(min(v.x, v.y), min(v.z, v.w))) >= t; }
DEV void wait4(unsigned a, unsigned t) {
#pragma unroll 1
  for (unsigned i = 0; i < (1u << 22); ++i) { if (ge4(peek4(a), t)) break; __builtin_amdgcn_s_sleep(1); }
  asm volatile("" ::: "memory");
}
DEV void wait2(unsigned a, unsigned t) {
#pragma unroll 1
  for (unsigned i = 0; i < (1u << 22); ++i) {
    v2u v = *(const volatile LAS v2u*)(size_t)a;
    if ((unsigned)__builtin_amdgcn_readfirstlane(min(v.x, v.y)) >= t) break;
    __builtin_amdgcn_s_sleep(1);
  }
  asm volatile("" ::: "memory");
}
DEV void wait1(unsigned a, unsigned t) {
#pragma unroll 1
  for (unsigned i = 0; i < (1u << 22); ++i) {
    if ((unsigned)__builtin_amdgcn_readfirstlane(*(const volatile LAS unsigned*)(size_t)a) >= t) break;
    __builtin_amdgcn_s_sleep(1);
  }
  asm volatile("" ::: "memory");
}
DEV float xsum16_32(float v) {
  float a = v, c = v;
  asm volatile("s_nop 1\n\tv_permlane16_swap_b32 %0, %1\n\ts_nop 0" : "+v"(a), "+v"(c));
  a += c; c = a;
  asm volatile("s_nop 1\n\tv_permlane32_swap_b32 %0, %1\n\ts_nop 0" : "+v"(a), "+v"(c));
  return a + c;
}
template <int CTRL, int RM> DEV float dpp_add(float v) {
  return v + __builtin_bit_cast(float, __builtin_amdgcn_update_dpp(0, __builtin_bit_cast(int, v), CTRL, RM, 0xf, false));
}
DEV float wave_sum(float v) {
  v = dpp_add<0x111, 0xf>(v); v = dpp_add<0x112, 0xf>(v); v = dpp_add<0x114, 0xf>(v); v = dpp_add<0x118, 0xf>(v);
  v = dpp_add<0x142, 0xa>(v); v = dpp_add<0x143, 0xc>(v);
  return __builtin_bit_cast(float, __builtin_amdgcn_readlane(__builtin_bit_cast(int, v), 63));
}

#define R5AP()
struct Params {
  const u16 *x, *k, *v, *shk, *sk, *sD, *w1, *w2;
  u16* out;
  unsigned char* ws;
};

template <int N> DEV bool wait_eq1(unsigned v, unsigned epoch) {
  unsigned d;
  asm volatile("s_waitcnt vmcnt(%3)\n\tv_xor_b32 %0, %2, %1" : "=v"(d) : "v"(v), "s"(epoch), "i"(N) : "memory");
  return __all(d == 0u);
}
template <int N> DEV bool wait_eq4(v4u v, unsigned epoch) {
  unsigned d, e;
  asm volatile("s_waitcnt vmcnt(%7)\n\tv_xor_b32 %0, %6, %2\n\tv_xor_b32 %1, %6, %3\n\tv_or_b32 %0, %0, %1\n\tv_xor_b32 %1, %6, %4\n\t"
               "v_or_b32 %0, %0, %1\n\tv_xor_b32 %1, %6, %5\n\tv_or_b32 %0, %0, %1"
               : "=&v"(d), "=&v"(e) : "v"(v.x), "v"(v.y), "v"(v.z), "v"(v.w), "s"(epoch), "i"(N) : "memory");
  return __all(d == 0u);
}
DEV void poll1(cptr base, unsigned off, unsigned epoch) {
#pragma unroll 1
  for (unsigned spins = 0; spins < SPIN_CAP; ++spins) {
    unsigned v = ld32a(base, off);
    if (wait_eq1<0>(v, epoch)) break;
    __builtin_amdgcn_s_sleep(6);
  }
}
DEV void poll4(cptr base, unsigned off, unsigned epoch) {
#pragma unroll 1
  for (unsigned spins = 0; spins < SPIN_CAP; ++spins) {
    v4u v = ld128a(base, off);
    if (wait_eq4<0>(v, epoch)) break;
    __builtin_amdgcn_s_sleep(6);
  }
}
DEV void flag_set(const void* sbase, unsigned voff, unsigned epoch) {
  asm volatile("global_atomic_swap %0, %1, %2 sc1" ::"v"(voff), "v"(epoch), "s"(sbase) : "memory");
}
DEV void poll_atomic(cptr base, unsigned off, unsigned long long mask, unsigned epoch) {
#pragma unroll 1
  for (unsigned spins = 0; spins < SPIN_CAP; ++spins) {
    const unsigned v = atom_rd(base, off, mask, epoch);
    if (wait_eq1<0>(v, epoch)) break;
    __builtin_amdgcn_s_sleep(2);
  }
}

DEV void conv_final(__amdgpu_buffer_rsrc_t rs, unsigned db, int ch, int chunk, v4u kvr, float dD) {
  const unsigned a = db + chunk * 32;
  f32x4 p0 = __builtin_bit_cast(f32x4, lds128(a)), p1 = __builtin_bit_cast(f32x4, lds128(a + 16));
  f32x4 q0 = __builtin_bit_cast(f32x4, lds128(a + 8192)), q1 = __builtin_bit_cast(f32x4, lds128(a + 8192 + 16));
  p0 += q0; p1 += q1;
  p0 += f32x4{bf_hi(kvr.w), bf_lo(kvr.w), bf_hi(kvr.z), bf_lo(kvr.z)} * dD;
  p1 += f32x4{bf_hi(kvr.y), bf_lo(kvr.y), bf_hi(kvr.x), bf_lo(kvr.x)} * dD;
  const v4u o = {pk_bf(p0[0], p0[1]), pk_bf(p0[2], p0[3]), pk_bf(p1[0], p1[1]), pk_bf(p1[2], p1[3])};
  __builtin_amdgcn_raw_buffer_store_b128(o, rs, WS_YT + ch * 4096 + chunk * 16, 0, AUX_SC1);
}

__global__ void __launch_bounds__(NTHR, 3) fused_kernel(Params p) {
  __shared__ __attribute__((aligned(16))) unsigned char smem[163840];
  const int tid = threadIdx.x, lane = tid & 63;
  const int w = __builtin_amdgcn_readfirstlane(tid >> 6);
  const int b = blockIdx.x, xcd = b & 7, idx = b >> 3;
  const int half = xcd & 1, cti = idx & 7;
  const int rt = 4 * (xcd >> 1) + (idx >> 3);
  const int nfull = 6 - half;
  const bool isfull = cti < nfull;
  const int u0 = 14 * half + (isfull ? 2 * cti : nfull + cti);
  const unsigned l16 = lane * 16;
  { unsigned lb = (unsigned)(size_t)(LAS unsigned char*)smem; asm volatile("" ::"s"(lb)); }
  {
    cptr kpc; asm volatile("s_getpc_b64 %0" : "=s"(kpc));
    if (w == 4) asm volatile("global_load_dword v167, %0, %1" ::"v"(min((unsigned)(lane * 8 + (idx & 7)) * 128u, CODE_PF_BYTES)), "s"(kpc) : "v167", "memory");
  }
  unsigned epoch, epraw;
#define EPOCH_LD() asm volatile("s_load_dword %0, %1, 0x0" : "=s"(epraw) : "s"(p.ws + WS_EPOCH) : "memory")
#define EPOCH_FIN() asm volatile("s_waitcnt lgkmcnt(0)\n\ts_mul_i32 %0, %1, 0x9E3779B1\n\ts_add_u32 %0, %0, 0x9E3779B9" : "=s"(epoch) : "s"(epraw) : "memory", "scc")
  const bool kvrole = rt < 10;
  const int kt = half * 8 + cti, kc = rt, kvn = rt * 16 + kt;
  const int wn = kvn - 160;
#define RS_DECL() unsigned char* wsq_ = p.ws; asm volatile("" : "+s"(wsq_)); const __amdgpu_buffer_rsrc_t rs = __builtin_amdgcn_make_buffer_rsrc((void*)wsq_, 0, (int)WS_END, 0x00020000)
  const unsigned lcs = isfull ? L_CS : L_STATS + 1024;
#define LATE_CONSTS() \
  int rt_ = rt; asm volatile("" : "+s"(rt_)); \
  const int tt64 = 2 * rt_ + (cti >> 2), c80 = 4 * half + (cti & 3); \
  const int hn = rt_ * 5 + half * 2 + cti - nfull; \
  const int area = w >= 8 ? 2 + ((w >> 1) & 1) : (w >> 1) & 1; \
  const bool convw = isfull ? w >= 8 : (hn < 72 && w >= 4); \
  const int chb = isfull ? 2 * (rt_ * 11 + half * 6 + cti) - 2 : (hn < 72 ? 352 + 4 * hn : 0); \
  const int cch = (isfull && area < 2) ? chb + 2 : chb + area; \
  const unsigned cb = L_AREA0 + area * CH_BYTES; \
  cptr w2s = p.ws + WS_W2T + c80 * (54 * 5 * 1024) + ((w >> 1) & 1) * (27 * 5 * 1024) + (w & 1) * 7168; \
  const unsigned w2d = ((w >> 1) & 1) * 15360 + (w & 1) * 7168; \
  (void)tt64; (void)convw; (void)cch; (void)cb; (void)w2s; (void)w2d; (void)chb; (void)hn;
#define H_DMA() dma4km((cptr)p.sk + (size_t)cch * 4096 + (w & 1) * 4, l16, cb + ((w & 1) ? CH_H1 : CH_H0), (w & 1) ? 0x7fffffffffffffffull : ~0ull)
#define R_DMA() dma4ks(p.ws + WS_KVR + (size_t)cch * KVR_ROW + (w & 1) * 2, l16, cb + ((w & 1) ? CH_R0 : CH_R1))
#define PADS_LD() kptr hr_ = (kptr)((cptr)p.sk + (size_t)cch * 4096); unsigned h01 = hr_[0], he0 = hr_[1021], he1 = hr_[1022]; \
    asm volatile("" : "+s"(h01), "+s"(he0), "+s"(he1))
#define PADS_ST() do { \
    if (!(w & 1) && lane < 4) sts128(cb + (lane == 0 ? CH_Z1 : lane == 1 ? CH_Z0 : lane == 2 ? CH_PF : CH_H1 + 4080), \
                                     v4u{lane == 3 ? he0 : 0u, lane == 3 ? he1 : 0u, 0u, lane == 2 ? h01 : 0u}); } while (0)
#define CONV_PADS() do { PADS_LD(); PADS_ST(); } while (0)
#define W2GROUP(g) do { WAITV(8); dma4ks(w2s + (g) * 15360, l16, w2d + wslot(g)); WAITV(8); dma4ks(w2s + (g) * 15360 + 4096, l16, w2d + wslot(g) + 4096); } while (0)
#define FENCE() asm volatile("" ::: "memory")
#define PRIO(x) __builtin_amdgcn_s_setprio(x)

  unsigned hfo = ~0u;
  unsigned hpv;
  v2u stq = {0u, 0u};
  if (__builtin_expect(w < 4, 1)) {
    const int L = w;
    PRIO(1);
    const unsigned bx = (lane >> 3) * 1280, cx = lane & 7;
    const unsigned cwz = (lane & 15) ^ ((lane >> 4) << 2);
    const int cmask = isfull ? 15 : 7;
    unsigned vx[4], vw[4];
#pragma unroll
    for (int j = 0; j < 4; ++j) {
      vx[j] = bx + ((cx ^ (lane >> 3)) << 4) + j * (8 * 1280 - 1024);
      vw[j] = (lane >> 4) * 3456 + (((cwz ^ j) & cmask) << 4) + j * (4 * 3456 - 1024);
    }
    int chunk = (rt & ~1) % 10;
    cptr xb0 = (cptr)p.x + (size_t)(128 * rt + 32 * L) * 1280;
    cptr wb0 = (cptr)p.w1 + (size_t)(16 * L) * 3456 + 128 * u0;
    unsigned reg = L * 4096, rx = 32768;
#define STAGE_A() dma4v(xb0 + 128 * chunk, vx, reg)
    const unsigned long long wmask = isfull ? ~0ull : 0xff00ff0000ff00ffull;
#define STAGE_B() do { dma4vx(wb0 + (size_t)chunk * (64 * 3456), vw, reg + 16384, wmask); chunk = chunk == 9 ? 0 : chunk + 1; reg ^= rx; rx ^= 98304; } while (0)
    unsigned vo[4];
    const unsigned myfull = W_FULL + 4 * L, vfull = pub_addr(myfull, lane);
    STAGE_A(); STAGE_B();
    EPOCH_LD();
    __builtin_amdgcn_s_barrier(); FENCE();
    cptr p0a, p0b; unsigned p0la, p0lb;
    if (kvrole) {
#pragma unroll
      for (int j = 0; j < 4; ++j) vo[j] = bx + ((cx ^ ((4 * L + j) & 7)) << 4) + j * (8 * 1280 - 1024);
      const size_t go = (size_t)(128 * kt + 32 * L) * 1280 + 128 * kc;
      p0a = (cptr)p.k + go; p0b = (cptr)p.v + go; p0la = L_P0 + L * 4096; p0lb = p0la + 16384;
    } else {
#pragma unroll
      for (int j = 0; j < 4; ++j) vo[j] = (lane >> 4) * 1280 + ((cwz ^ j) << 4) + j * (4 * 1280 - 1024);
      const int wnc = wn < 90 ? wn : 0;
      const int g0 = L < 3 ? 2 * L : 4;
      p0a = (cptr)p.w2 + (size_t)(96 * (wnc / 5) + 16 * g0) * 1280 + 256 * (wnc % 5); p0b = p0a + 16 * 1280; p0la = L_P0 + g0 * 4096; p0lb = p0la + 4096;
    }
#define P0A() dma4v(p0a, vo, p0la)
#define P0B() dma4vm(p0b, vo, p0lb, (kvrole && L == 3) ? 0x00ffffffffffffffull : ~0ull)
#define PUBPL() do { EPOCH_FIN(); pub(W_PL + 4 * L, 1, lane); } while (0)
    P0A(); WAITV(4); pubv(vfull, 1); P0B();
    WAITV(4); STAGE_A(); WAITV(4); PUBPL(); STAGE_B();
    WAITV(4); STAGE_A(); WAITV(4); pubv(vfull, 2); STAGE_B();
    WAITV(4); STAGE_A(); WAITV(4); pubv(vfull, 3); STAGE_B();
    {
      unsigned frm = min4u(peek4(W_FREE));
#pragma unroll 1
      for (int s = 1; s <= 6; ++s) {
        if (frm < (unsigned)s) { WAITV(0); pubv(vfull, s + 3); wait4(W_FREE, s); }
        if (s == 1 + (rt & 1) && rt < 10) wait4(W_AP, 1);
        WAITV(4); STAGE_A();
        WAITV(4); pubv(vfull, s + 3);
        STAGE_B();
        frm = min4u(peek4(W_FREE));
      }
    }
    WAITV(0); pubv(vfull, 10);
  } else if (w < 8) {
    const int cw = w - 4;
    RS_DECL();
    const int n = lane & 15, g = lane >> 4, q = (lane & 15) >> 2, pp = lane & 3;
    const int tok0 = isfull ? 64 * (cw & 1) : 32 * cw, fg = isfull ? cw >> 1 : 0;
    EPOCH_LD();
    __builtin_amdgcn_s_barrier(); FENCE();
    unsigned ax[2], aw[4][2];
#pragma unroll
    for (int u = 0; u < 2; ++u) ax[u] = (tok0 + n) * 128 + ((((4 * u + g) ^ n) & 7) << 4);
#pragma unroll
    for (int mt = 0; mt < 4; ++mt)
#pragma unroll
      for (int t = 0; t < 2; ++t)
        aw[mt][t] = 16384 + (8 * g + 4 * t + q) * 256 + (((2 * (4 * fg + mt) + (pp >> 1)) ^ ((q << 2) | ((2 * g + t) & 3))) << 4) + 8 * (pp & 1);
    f32x4 acc[4][4];
#pragma unroll
    for (int mt = 0; mt < 4; ++mt)
#pragma unroll
      for (int T = 0; T < 4; ++T) acc[mt][T] = f32x4{0.f, 0.f, 0.f, 0.f};
    v4u xf0[4], wf0[4], xf1[4], wf1[4];
    EPOCH_FIN();
    const unsigned vfree = pub_addr(W_FREE + 4 * cw, lane);
#define P1_READ(NT, XF, WF, U) do { \
      { v2u lo = ldstr(aw[0][0] + 8192 * U), hi = ldstr(aw[0][1] + 8192 * U); WF[0] = v4u{lo.x, lo.y, hi.x, hi.y}; } \
      _Pragma("unroll") for (int T = 0; T < NT; ++T) XF[T] = lds128(ax[U] + 2048 * T); \
      _Pragma("unroll") for (int mt = 1; mt < 4; ++mt) { v2u lo = ldstr(aw[mt][0] + 8192 * U), hi = ldstr(aw[mt][1] + 8192 * U); WF[mt] = v4u{lo.x, lo.y, hi.x, hi.y}; } \
    } while (0)
#define P1_MMA(NT, XF, WF) do { \
      _Pragma("unroll") for (int mt = 0; mt < 4; ++mt) _Pragma("unroll") for (int T = 0; T < NT; ++T) acc[mt][T] = mfma(WF[mt], XF[T], acc[mt][T]); \
    } while (0)
#define P1_ILV(NT) do { _Pragma("unroll") for (int i = 0; i < 4 * NT; ++i) { SGB(0x8, 1); SGB(0x100, NT == 4 ? 1 : 2); } SB0(); } while (0)
#define P1_ILV2(NT) do { _Pragma("unroll") for (int i = 0; i < 4 * NT; ++i) { SGB(0x8, 1); SGB(0x180, NT == 4 ? 1 : 2); SGB(0x2, NT == 4 ? 1 : 2); } SB0(); } while (0)
#define P1_LOOP(NT) do { \
      wait4(W_FULL, 1); \
      P1_READ(NT, xf0, wf0, 0); SB0(); \
      _Pragma("unroll 1") for (int s = 0; s < 9; ++s) { \
        const v4u fl = peek4(W_FULL); \
        P1_READ(NT, xf1, wf1, 1); \
        FENCE(); \
        pubv(vfree, s + 1); \
        const unsigned dx = (s & 1) ? 65536u : 32768u; \
        ax[0] ^= dx; ax[1] ^= dx; \
        _Pragma("unroll") for (int mt = 0; mt < 4; ++mt) { aw[mt][0] ^= dx; aw[mt][1] ^= dx; } \
        const unsigned flm = min4u(fl); \
        P1_MMA(NT, xf0, wf0); P1_ILV2(NT); \
        if (__builtin_expect(flm < (unsigned)s + 2, 0)) wait4(W_FULL, s + 2); \
        FENCE(); SB0(); \
        P1_READ(NT, xf0, wf0, 0); P1_MMA(NT, xf1, wf1); P1_ILV(NT); \
      } \
      apk = lds64v(W_AP + 8); \
      P1_READ(NT, xf1, wf1, 1); \
      FENCE(); \
      pubv(vfree, 10); \
      P1_MMA(NT, xf0, wf0); P1_ILV2(NT); \
    } while (0)
    PRIO(2);
    WAITV(0);
    v2u apk;
    if (isfull) P1_LOOP(4); else P1_LOOP(2);
    if ((unsigned)__builtin_amdgcn_readfirstlane(min(apk.x, apk.y)) < 5u) wait2(W_AP + 8, 5);
    FENCE();
    f32x4 cs[4]; f32x2 st[4];
#pragma unroll
    for (int mt = 0; mt < 4; ++mt) cs[mt] = __builtin_bit_cast(f32x4, lds128(lcs + (fg * 64 + mt * 16 + g * 4) * 4));
#pragma unroll
    for (int T = 0; T < 4; ++T) st[T] = __builtin_bit_cast(f32x2, lds64(L_STATS + ((tok0 + 16 * T + n) & 127) * 8));
    const unsigned ho = WS_HF + ((8 * rt + (tok0 >> 4)) * 54 + 2 * (u0 + fg)) * 1024 + l16;
    SB0();
#pragma unroll
    for (int T = 0; T < 2; ++T)
#pragma unroll
      for (int mt = 0; mt < 4; ++mt) acc[mt][T] = mfma(wf1[mt], xf1[T], acc[mt][T]);
    if (isfull) {
#pragma unroll
      for (int T = 2; T < 4; ++T)
#pragma unroll
        for (int mt = 0; mt < 4; ++mt) acc[mt][T] = mfma(wf1[mt], xf1[T], acc[mt][T]);
    }
    SB0();
#pragma unroll
    for (int T = 0; T < 4; ++T) {
      if (T < 2 || isfull) {
        f32x2 z[8], e[8];
#pragma unroll
        for (int j = 0; j < 8; ++j) z[j] = f32x2{cs[j >> 1][2 * (j & 1)], cs[j >> 1][2 * (j & 1) + 1]} * st[T][1];
        SB0();
#pragma unroll
        for (int j = 0; j < 8; ++j) z[j] = f32x2{acc[j >> 1][T][2 * (j & 1)], acc[j >> 1][T][2 * (j & 1) + 1]} * st[T][0] + z[j];
        SB0();
#pragma unroll
        for (int j = 0; j < 8; ++j) e[j] = f32x2{__builtin_amdgcn_exp2f(z[j][0]), __builtin_amdgcn_exp2f(z[j][1])};
        SB0();
#pragma unroll
        for (int j = 0; j < 8; ++j) e[j] = e[j] * -LOG2E - LOG2E;
        SB0();
#pragma unroll
        for (int j = 0; j < 8; ++j) e[j] = f32x2{__builtin_amdgcn_rcpf(e[j][0]), __builtin_amdgcn_rcpf(e[j][1])};
        SB0();
#pragma unroll
        for (int j = 0; j < 8; ++j) z[j] = z[j] * e[j];
        SB0();
        const v4u o0 = {pk_bf(z[0]), pk_bf(z[1]), pk_bf(z[2]), pk_bf(z[3])}, o1 = {pk_bf(z[4]), pk_bf(z[5]), pk_bf(z[6]), pk_bf(z[7])};
        __builtin_amdgcn_raw_buffer_store_b128(o0, rs, ho, T * (54 * 1024), AUX_SC1);
        __builtin_amdgcn_raw_buffer_store_b128(o1, rs, ho, T * (54 * 1024) + 1024, AUX_SC1);
        SB0();
      }
    }
    pubv(vfree, 11);
    hfo = ((4 * rt + (tok0 >> 5)) * 32 + u0 + fg) * 4;
    if (isfull || rt * 5 + half * 2 + cti - nfull >= 72 || min4u(peek4(W_FULL)) < 12) {
      WAITV(0);
      if (lane < (isfull ? 2 : 1)) flag_set(p.ws + WS_FH, hfo + lane * 128, epoch);
      hfo = ~0u;
    }
  } else {
    const int aw = w - 8;
    PRIO(3);
    RS_DECL();
    if (aw == 0 && lane < 28) {
      const int hn = rt * 5 + half * 2 + cti - nfull;
      const bool cvon = lane < 12 || lane >= 20 || (isfull ? lane >= 16 : hn < 72);
      sts32(L_SYNC + lane * 4, cvon ? 0u : 99u);
    }
    const int srow = 128 * rt + 8 * (half * 8 + cti) + 2 * aw;
    cptr xr = (cptr)p.x + (size_t)srow * 1280;
    const v4u xa = *(const GAS v4u*)(xr + l16), xc = *(const GAS v4u*)(xr + 1280 + l16);
    const unsigned xb = *(const GAS unsigned*)(xr + 1024 + lane * 4), xd = *(const GAS unsigned*)(xr + 2304 + lane * 4);
    const int tg8 = (lane & 7) + 8 * (aw & 1), cpl = (lane >> 3) + 8 * (aw >> 1);
    unsigned halo[2][3] = {{0u, 0u, 0u}, {0u, 0u, 0u}};
    v4u shk4[2] = {{0u, 0u, 0u, 0u}, {0u, 0u, 0u, 0u}};
    unsigned vlast[2] = {0u, 0u};
    if (kvrole) {
      if (tg8 == 15) {
#pragma unroll
        for (int it = 0; it < 2; ++it) vlast[it] = *(const GAS unsigned*)((cptr)p.v + (size_t)(128 * kt + 127) * 1280 + 128 * kc + 4 * cpl + 64 * it);
      }
#pragma unroll
      for (int it = 0; it < 2; ++it) shk4[it] = *(const GAS v4u*)((cptr)p.shk + (size_t)(64 * kc + 2 * cpl + 32 * it) * 8);
      if (tg8 == 0 && kt > 0) {
#pragma unroll
        for (int it = 0; it < 2; ++it)
#pragma unroll
          for (int i = 0; i < 3; ++i) halo[it][i] = *(const GAS unsigned*)((cptr)p.k + (size_t)(128 * kt - 3 + i) * 1280 + 128 * kc + 4 * cpl + 64 * it);
      }
    }
    SB0();
    BAR0();
    EPOCH_LD();
    asm volatile("" ::"v"(xd));
    EPOCH_FIN();
    const unsigned myap = W_AP + 4 * aw;
    const unsigned ka = L_P0 + tg8 * 1024 + ((((cpl >> 2) ^ tg8) & 7) << 4) + (cpl & 3) * 4;
    const unsigned kp = tg8 ? L_P0 + (tg8 - 1) * 1024 + ((((cpl >> 2) ^ (tg8 - 1)) & 7) << 4) + (cpl & 3) * 4 : ka;
    {
      f32x2 a[5] = {bf2(xa.x), bf2(xa.y), bf2(xa.z), bf2(xa.w), bf2(xb)}, c[5] = {bf2(xc.x), bf2(xc.y), bf2(xc.z), bf2(xc.w), bf2(xd)};
      f32x2 sa = (a[0] + a[1]) + (a[2] + a[3]) + a[4], sc = (c[0] + c[1]) + (c[2] + c[3]) + c[4];
      const float ma = wave_sum(sa[0] + sa[1]) * (1.0f / D), mc = wave_sum(sc[0] + sc[1]) * (1.0f / D);
      f32x2 qa = {0.f, 0.f}, qc = {0.f, 0.f};
#pragma unroll
      for (int i = 0; i < 5; ++i) { f32x2 e = a[i] - ma; qa += e * e; f32x2 f = c[i] - mc; qc += f * f; }
      const float va = wave_sum(qa[0] + qa[1]) * (1.0f / D), vc = wave_sum(qc[0] + qc[1]) * (1.0f / D);
      const unsigned vb = pk_bf(bf2(pk_bf(va, vc)) + 1e-5f);
      const v4u o = {pk_bf(ma, __builtin_amdgcn_sqrtf(bf_lo(vb))), epoch, pk_bf(mc, __builtin_amdgcn_sqrtf(bf_hi(vb))), epoch};
      if (lane < 2) __builtin_amdgcn_raw_buffer_store_b64(lane ? v2u{o.z, o.w} : v2u{o.x, o.y}, rs, WS_STATS + (srow + lane) * 8, 0, AUX_SC1);
    }
#define COLSUM() do { \
      wait4(W_FULL, 1 + (rt & 1)); \
      const int g = lane >> 4, q = (lane & 15) >> 2, pp = lane & 3; \
      float c0 = 0.f, c1 = 0.f, c2 = 0.f, c3 = 0.f; \
_Pragma("unroll") \
      for (int t = 0; t < 2; ++t) { \
        const unsigned rowb = (rt & 1) * 32768 + 16384 + (8 * g + 4 * t + q) * 256 + 8 * (pp & 1), swz = (q << 2) | ((2 * g + t) & 3); \
        const unsigned a0 = rowb + (((4 * aw + (pp >> 1)) ^ swz) << 4), a1 = rowb + (((4 * aw + 2 + (pp >> 1)) ^ swz) << 4); \
_Pragma("unroll") \
        for (int u = 0; u < 2; ++u) { \
          v2u r0 = ldstr(a0 + 8192 * u), r1 = ldstr(a1 + 8192 * u); \
          c0 = dot2ones(r0.x, c0); c1 = dot2ones(r1.x, c1); c2 = dot2ones(r0.y, c2); c3 = dot2ones(r1.y, c3); \
        } \
      } \
      FENCE(); \
      pub(myap, 1, lane); \
      c0 = xsum16_32(c0 + c2); c1 = xsum16_32(c1 + c3); \
      if (lane < 32) \
        __builtin_amdgcn_raw_buffer_store_b64(v2u{asu(lane < 16 ? c0 : c1), epoch}, rs, (isfull || aw < 2) ? WS_CSP + (rt * 1792 + 64 * u0 + 32 * aw + lane) * 8 : 0xfffffff0u, 0, AUX_SC1); \
      SB0(); \
    } while (0)
    if (rt < 10 && !(rt & 1)) COLSUM();
    if (!kvrole) {
      wait4(W_PL, 1);
      if (wn < 90) {
      const int g = lane >> 4, q = (lane & 15) >> 2, pp = lane & 3;
#pragma unroll
      for (int it = 0; it < 2; ++it) {
        const int dt = 8 * (wn % 5) + aw + 4 * it;
#pragma unroll
        for (int u = 0; u < 3; ++u) {
          v2u h2[2];
#pragma unroll
          for (int t = 0; t < 2; ++t)
            h2[t] = ldstr(L_P0 + (32 * u + 16 * t + 4 * g + q) * 256 + (((2 * (aw + 4 * it) + (pp >> 1)) ^ ((q << 2) | (g & 3))) << 4) + 8 * (pp & 1));
          __builtin_amdgcn_raw_buffer_store_b128(v4u{h2[0].x, h2[0].y, h2[1].x, h2[1].y}, rs, WS_W2T + (((dt / 5) * 54 + 3 * (wn / 5) + u) * 5 + dt % 5) * 1024 + l16, 0, AUX_SC1);
        }
      }
    }
      SB0();
    }
    wait4(W_PL, 1);
    if (kvrole) {
      unsigned kk[2][11], vv[2][8];
#pragma unroll
      for (int it = 0; it < 2; ++it) {
#pragma unroll
        for (int i = 0; i < 3; ++i) kk[it][i] = lds32((kp ^ (64 * it)) + (5 + i) * 128);
#pragma unroll
        for (int i = 0; i < 8; ++i) { kk[it][3 + i] = lds32((ka ^ (64 * it)) + i * 128); vv[it][i] = lds32((ka ^ (64 * it)) + 16384 + i * 128); }
      }
      const unsigned kvo = WS_KVR + (64 * kc + 2 * cpl) * KVR_ROW + (2040 - 128 * kt - 8 * tg8) * 2;
      v4u ra[2], rb[2];
#pragma unroll
      for (int it = 0; it < 2; ++it) {
        const v4u sh = shk4[it];
        const f32x2 s0 = {bf_lo(sh.x), bf_lo(sh.z)}, s1 = {bf_hi(sh.x), bf_hi(sh.z)}, s2 = {bf_lo(sh.y), bf_lo(sh.w)}, s3 = {bf_hi(sh.y), bf_hi(sh.w)};
        f32x2 kf[11], o[8];
#pragma unroll
        for (int i = 0; i < 11; ++i) kf[i] = bf2(i < 3 ? (tg8 ? kk[it][i] : halo[it][i]) : kk[it][i]);
#pragma unroll
        for (int i = 0; i < 8; ++i) {
          f32x2 s = kf[i] * s3 + kf[i + 1] * s2 + kf[i + 2] * s1 + kf[i + 3] * s0;
          o[i] = s * bf2(i == 7 ? (tg8 == 15 ? vlast[it] : vv[it][i]) : vv[it][i]);
        }
        ra[it] = v4u{pk_bf(o[7][0], o[6][0]), pk_bf(o[5][0], o[4][0]), pk_bf(o[3][0], o[2][0]), pk_bf(o[1][0], o[0][0])};
        rb[it] = v4u{pk_bf(o[7][1], o[6][1]), pk_bf(o[5][1], o[4][1]), pk_bf(o[3][1], o[2][1]), pk_bf(o[1][1], o[0][1])};
        USE(ra[it]); USE(rb[it]);
      }
#pragma unroll
      for (int it = 0; it < 2; ++it) {
        const unsigned o2 = kvo + it * (32 * KVR_ROW);
        __builtin_amdgcn_raw_buffer_store_b128(ra[it], rs, o2, 0, AUX_SC1);
        __builtin_amdgcn_raw_buffer_store_b128(rb[it], rs, o2 + KVR_ROW, 0, AUX_SC1);
        if (kt == 0 && tg8 == 0) {
          __builtin_amdgcn_raw_buffer_store_b128(v4u{0u, 0u, 0u, 0u}, rs, o2 + 16, 0, AUX_SC1);
          __builtin_amdgcn_raw_buffer_store_b128(v4u{0u, 0u, 0u, 0u}, rs, o2 + KVR_ROW + 16, 0, AUX_SC1);
        }
      }
    }
    SB0();
    if (rt < 10 && (rt & 1)) { COLSUM(); WAITV(1); } else WAITV(0);
    pub(myap, 3, lane);
    wait4(W_AP, 3);
    if (aw == 0) {
      wait4(W_FREE, 1);
      if (lane == 32 || (kvrole && lane < 32))
        flag_set(p.ws, lane == 32 ? WS_FP0 + (kvrole ? kvn : 160 + wn) * 4 : WS_MBKV + (kc * 32 + lane) * 128 + kt * 4, epoch);
    }
    PRIO(1);
    LATE_CONSTS();
    v4u gr[10];
    if (aw >= 2) {
      wait4(W_FREE, 5);
      const unsigned gb = aw == 2 ? WS_STATS + 128 * rt * 8 + l16 : WS_CSP + 64 * u0 * 8 + (isfull ? l16 : (l16 & 511));
#pragma unroll 1
      for (unsigned spins = 0; spins < SPIN_CAP; ++spins) {
        gr[0] = __builtin_amdgcn_raw_buffer_load_b128(rs, gb, 0, AUX_SC1);
        unsigned bad = 0;
        if (aw == 3) {
#pragma unroll
          for (int i = 1; i < 10; ++i) gr[i] = __builtin_amdgcn_raw_buffer_load_b128(rs, gb, i * (1792 * 8), AUX_SC1);
          float c0 = asf(gr[0].x), c1 = asf(gr[0].z);
#pragma unroll
          for (int i = 1; i < 10; ++i) { bad |= (gr[i].y ^ epoch) | (gr[i].w ^ epoch); c0 += asf(gr[i].x); c1 += asf(gr[i].z); }
          gr[0].x = asu(c0); gr[0].z = asu(c1);
        }
        bad |= (gr[0].y ^ epoch) | (gr[0].w ^ epoch);
        if (__all(bad == 0u)) break;
        __builtin_amdgcn_s_sleep(8);
      }
    }
    H_DMA(); CONV_PADS();
    if (aw == 1) {
      if (wn >= 0 && wn % 12 == 11) {
        wait4(W_FREE, 3);
#pragma unroll 1
        for (unsigned spins = 0; spins < SPIN_CAP; ++spins) {
          __builtin_amdgcn_s_sleep(12);
          const v2u f = __builtin_amdgcn_raw_buffer_load_b64(rs, WS_FP0 + 640 + (lane < 48 ? lane : 0) * 8, 0, AUX_SC1);
          if (__all(f.x == epoch && f.y == epoch)) break;
        }
        if (lane < 4) flag_set(p.ws + WS_W2R, ((wn / 12) * 4 + lane) * 128, epoch);
      } else {
        wait4(W_FREE, 5);
        poll1(p.ws, WS_W2R + (xcd * 4 + (idx & 3)) * 128, epoch);
      }
      pub(myap, 4, lane);
    }
    bool rdone = false;
    if (aw < 2) {
      if (aw == 0) {
        wait4(W_FREE, isfull ? 6 : 5);
        poll1(p.ws, WS_MBKV + ((chb + 3) >> 1) * 128 + (lane & 15) * 4, epoch);
        pub(myap, 4, lane);
        wait4(W_FREE, 7);
      }
    } else {
      stq = v2u{gr[0].x, gr[0].z};
      rdone = (unsigned)__builtin_amdgcn_readfirstlane(*(const volatile LAS unsigned*)(size_t)W_AP) >= 4;
      FENCE();
      if (rdone) R_DMA();
      wait4(W_FREE, 8);
      if (aw == 2) {
        const float r0 = __builtin_amdgcn_rcpf(bf_hi(gr[0].x)) * LOG2E, r1 = __builtin_amdgcn_rcpf(bf_hi(gr[0].z)) * LOG2E;
        sts128(L_STATS + l16, __builtin_bit_cast(v4u, f32x4{-r0, bf_lo(gr[0].x) * r0, -r1, bf_lo(gr[0].z) * r1}));
      } else if (isfull || lane < 32) sts64(lcs + lane * 8, v2u{gr[0].x, gr[0].z});
      FENCE();
      pub(myap, 5, lane);
    }
    if (!rdone) { wait1(W_AP, 4); R_DMA(); }
    WAITV(0);
  }
  epoch = __builtin_amdgcn_readfirstlane(epoch);
  RS_DECL();
  LATE_CONSTS();
  if (convw) {
    const int rh = w & 1;
    const unsigned cvp = W_CV + area * 8, mycv = cvp + rh * 4;
    pub(mycv, 1, lane);
    const int i16 = lane & 15, kg = lane >> 4;
    const unsigned rbase = cb + ((i16 & 1) ? CH_R1 : CH_R0);
    unsigned va = rbase + 4032 - 3584 - 64 * (i16 >> 1) + 16 * kg + 256 * rh;
    const unsigned vamax = rbase + 4096;
    const unsigned hbase = cb + ((i16 & 1) ? CH_H0 : CH_H1);
    unsigned vb = hbase + 4 * i16 - ((i16 & 1) ? 60 : 64) + 16 * kg + 256 * rh;
    const unsigned vbmin = hbase - 16;
    USE(va); USE(vb);
    f32x4 acc[8];
#pragma unroll
    for (int i = 0; i < 8; ++i) { acc[i] = f32x4{0.f, 0.f, 0.f, 0.f}; USE(acc[i]); }
    const float dD = bf_lo(*(kptr)((cptr)p.sD + (cch >> 1) * 4) >> ((cch & 1) * 16));
    if (w >= 8) { wait2(cvp, 1); if (isfull) wait4(W_FREE, 11); } else wait4(W_FULL, 12);
    if (isfull || w < 8) PRIO(3);
#define CONV_HOOK() do { if (hfo != ~0u) { WAITV(0); if (lane == 0) flag_set(p.ws + WS_FH, hfo, epoch); } } while (0)
    v4u A0a, B0a, A0b, B0b, A1a, B1a, A1b, B1b, A2a, B2a, A2b, B2b, A3, B3, A4, B4, A5, B5, A6, B6, A7, B7;
    v2u L0a, H0a, L0b, H0b, L1a, H1a, L1b, H1b, L2a, H2a, L2b, H2b, L3, H3, L4, H4, L5, H5, L6, H6, L7, H7;
    A0a = lds128v(min(va + 3584, vamax));
    L0a = lds64v(max(vb, vbmin)); H0a = lds64v(max(vb + 8, vbmin)); B0a = v4u{L0a.x, L0a.y, H0a.x, H0a.y};
    A1a = lds128v(va + 3072);
    L1a = lds64v(vb + 512);
    H1a = lds64v(vb + 520); B1a = v4u{L1a.x, L1a.y, H1a.x, H1a.y};
    A2a = lds128v(va + 2560);
    L2a = lds64v(vb + 1024);
    H2a = lds64v(vb + 1032); B2a = v4u{L2a.x, L2a.y, H2a.x, H2a.y};
    mfma_ip(acc[0], A0a, B0a);
    A3 = lds128v(va + 2048);
    mfma_ip(acc[1], A1a, B0a);
    L3 = lds64v(vb + 1536);
    mfma_ip(acc[2], A1a, B1a);
    H3 = lds64v(vb + 1544); B3 = v4u{L3.x, L3.y, H3.x, H3.y};
    mfma_ip(acc[3], A2a, B1a);
    A4 = lds128v(va + 1536);
    mfma_ip(acc[4], A2a, B2a);
    L4 = lds64v(vb + 2048);
    mfma_ip(acc[1], A0a, B1a);
    H4 = lds64v(vb + 2056); B4 = v4u{L4.x, L4.y, H4.x, H4.y};
    mfma_ip(acc[2], A2a, B0a);
    mfma_ip(acc[3], A1a, B2a);
    mfma_ip(acc[2], A0a, B2a);
    mfma_ip(acc[3], A3, B0a);
    A5 = lds128v(va + 1024);
    L5 = lds64v(vb + 2560);
    mfma_ip(acc[4], A3, B1a);
    H5 = lds64v(vb + 2568); B5 = v4u{L5.x, L5.y, H5.x, H5.y};
    A6 = lds128v(va + 512);
    mfma_ip(acc[5], A3, B2a);
    L6 = lds64v(vb + 3072);
    H6 = lds64v(vb + 3080); B6 = v4u{L6.x, L6.y, H6.x, H6.y};
    mfma_ip(acc[6], A3, B3);
    A7 = lds128v(va + 0);
    mfma_ip(acc[3], A0a, B3);
    L7 = lds64v(vb + 3584);
    mfma_ip(acc[4], A1a, B3);
    H7 = lds64v(vb + 3592); B7 = v4u{L7.x, L7.y, H7.x, H7.y};
    mfma_ip(acc[5], A2a, B3);
    A0b = lds128v(min(va + 3648, vamax));
    mfma_ip(acc[6], A4, B2a);
    L0b = lds64v(vb + 64);
    mfma_ip(acc[7], A4, B3);
    H0b = lds64v(vb + 72); B0b = v4u{L0b.x, L0b.y, H0b.x, H0b.y};
    mfma_ip(acc[4], A4, B0a);
    A1b = lds128v(va + 3136);
    mfma_ip(acc[5], A4, B1a);
    L1b = lds64v(vb + 576);
    mfma_ip(acc[6], A2a, B4);
    H1b = lds64v(vb + 584); B1b = v4u{L1b.x, L1b.y, H1b.x, H1b.y};
    mfma_ip(acc[7], A3, B4);
    A2b = lds128v(va + 2624);
    mfma_ip(acc[4], A0a, B4);
    L2b = lds64v(vb + 1088);
    mfma_ip(acc[5], A1a, B4);
    H2b = lds64v(vb + 1096); B2b = v4u{L2b.x, L2b.y, H2b.x, H2b.y};
    CONV_HOOK();
    mfma_ip(acc[5], A5, B0a);
    A3 = lds128v(va + 2112);
    mfma_ip(acc[0], A0b, B0b);
    L3 = lds64v(vb + 1600);
    mfma_ip(acc[6], A5, B1a);
    H3 = lds64v(vb + 1608); B3 = v4u{L3.x, L3.y, H3.x, H3.y};
    mfma_ip(acc[1], A1b, B0b);
    A4 = lds128v(va + 1600);
    mfma_ip(acc[7], A5, B2a);
    L4 = lds64v(vb + 2112);
    mfma_ip(acc[2], A1b, B1b);
    H4 = lds64v(vb + 2120); B4 = v4u{L4.x, L4.y, H4.x, H4.y};
    mfma_ip(acc[5], A0a, B5);
    mfma_ip(acc[1], A0b, B1b);
    mfma_ip(acc[6], A1a, B5);
    mfma_ip(acc[2], A2b, B0b);
    mfma_ip(acc[7], A2a, B5);
    mfma_ip(acc[3], A2b, B1b);
    mfma_ip(acc[6], A6, B0a);
    mfma_ip(acc[4], A2b, B2b);
    mfma_ip(acc[7], A6, B1a);
    mfma_ip(acc[2], A0b, B2b);
    mfma_ip(acc[6], A0a, B6);
    mfma_ip(acc[3], A1b, B2b);
    mfma_ip(acc[7], A1a, B6);
    mfma_ip(acc[7], A7, B0a);
    mfma_ip(acc[7], A0a, B7);
    mfma_ip(acc[3], A3, B0b);
    A5 = lds128v(va + 1088);
    L5 = lds64v(vb + 2624);
    mfma_ip(acc[4], A3, B1b);
    H5 = lds64v(vb + 2632); B5 = v4u{L5.x, L5.y, H5.x, H5.y};
    A6 = lds128v(va + 576);
    mfma_ip(acc[5], A3, B2b);
    L6 = lds64v(vb + 3136);
    H6 = lds64v(vb + 3144); B6 = v4u{L6.x, L6.y, H6.x, H6.y};
    mfma_ip(acc[6], A3, B3);
    A7 = lds128v(va + 64);
    mfma_ip(acc[3], A0b, B3);
    L7 = lds64v(vb + 3648);
    mfma_ip(acc[4], A1b, B3);
    H7 = lds64v(vb + 3656); B7 = v4u{L7.x, L7.y, H7.x, H7.y};
    mfma_ip(acc[5], A2b, B3);
    A0a = lds128v(min(va + 3712, vamax));
    mfma_ip(acc[6], A4, B2b);
    L0a = lds64v(vb + 128);
    mfma_ip(acc[7], A4, B3);
    H0a = lds64v(vb + 136); B0a = v4u{L0a.x, L0a.y, H0a.x, H0a.y};
    mfma_ip(acc[4], A4, B0b);
    A1a = lds128v(va + 3200);
    mfma_ip(acc[5], A4, B1b);
    L1a = lds64v(vb + 640);
    mfma_ip(acc[6], A2b, B4);
    H1a = lds64v(vb + 648); B1a = v4u{L1a.x, L1a.y, H1a.x, H1a.y};
    mfma_ip(acc[7], A3, B4);
    A2a = lds128v(va + 2688);
    mfma_ip(acc[4], A0b, B4);
    L2a = lds64v(vb + 1152);
    mfma_ip(acc[5], A1b, B4);
    H2a = lds64v(vb + 1160); B2a = v4u{L2a.x, L2a.y, H2a.x, H2a.y};
    mfma_ip(acc[5], A5, B0b);
    A3 = lds128v(va + 2176);
    mfma_ip(acc[0], A0a, B0a);
    L3 = lds64v(vb + 1664);
    mfma_ip(acc[6], A5, B1b);
    H3 = lds64v(vb + 1672); B3 = v4u{L3.x, L3.y, H3.x, H3.y};
    mfma_ip(acc[1], A1a, B0a);
    A4 = lds128v(va + 1664);
    mfma_ip(acc[7], A5, B2b);
    L4 = lds64v(vb + 2176);
    mfma_ip(acc[2], A1a, B1a);
    H4 = lds64v(vb + 2184); B4 = v4u{L4.x, L4.y, H4.x, H4.y};
    mfma_ip(acc[5], A0b, B5);
    mfma_ip(acc[1], A0a, B1a);
    mfma_ip(acc[6], A1b, B5);
    mfma_ip(acc[2], A2a, B0a);
    mfma_ip(acc[7], A2b, B5);
    mfma_ip(acc[3], A2a, B1a);
    mfma_ip(acc[6], A6, B0b);
    mfma_ip(acc[4], A2a, B2a);
    mfma_ip(acc[7], A6, B1b);
    mfma_ip(acc[2], A0a, B2a);
    mfma_ip(acc[6], A0b, B6);
    mfma_ip(acc[3], A1a, B2a);
    mfma_ip(acc[7], A1b, B6);
    mfma_ip(acc[7], A7, B0b);
    mfma_ip(acc[7], A0b, B7);
    mfma_ip(acc[3], A3, B0a);
    A5 = lds128v(va + 1152);
    L5 = lds64v(vb + 2688);
    mfma_ip(acc[4], A3, B1a);
    H5 = lds64v(vb + 2696); B5 = v4u{L5.x, L5.y, H5.x, H5.y};
    A6 = lds128v(va + 640);
    mfma_ip(acc[5], A3, B2a);
    L6 = lds64v(vb + 3200);
    H6 = lds64v(vb + 3208); B6 = v4u{L6.x, L6.y, H6.x, H6.y};
    mfma_ip(acc[6], A3, B3);
    A7 = lds128v(va + 128);
    mfma_ip(acc[3], A0a, B3);
    L7 = lds64v(vb + 3712);
    mfma_ip(acc[4], A1a, B3);
    H7 = lds64v(vb + 3720); B7 = v4u{L7.x, L7.y, H7.x, H7.y};
    mfma_ip(acc[5], A2a, B3);
    A0b = lds128v(min(va + 3776, vamax));
    mfma_ip(acc[6], A4, B2a);
    L0b = lds64v(vb + 192);
    mfma_ip(acc[7], A4, B3);
    H0b = lds64v(vb + 200); B0b = v4u{L0b.x, L0b.y, H0b.x, H0b.y};
    mfma_ip(acc[4], A4, B0a);
    A1b = lds128v(va + 3264);
    mfma_ip(acc[5], A4, B1a);
    L1b = lds64v(vb + 704);
    mfma_ip(acc[6], A2a, B4);
    H1b = lds64v(vb + 712); B1b = v4u{L1b.x, L1b.y, H1b.x, H1b.y};
    mfma_ip(acc[7], A3, B4);
    A2b = lds128v(va + 2752);
    mfma_ip(acc[4], A0a, B4);
    L2b = lds64v(vb + 1216);
    mfma_ip(acc[5], A1a, B4);
    H2b = lds64v(vb + 1224); B2b = v4u{L2b.x, L2b.y, H2b.x, H2b.y};
    mfma_ip(acc[5], A5, B0a);
    A3 = lds128v(va + 2240);
    mfma_ip(acc[0], A0b, B0b);
    L3 = lds64v(vb + 1728);
    mfma_ip(acc[6], A5, B1a);
    H3 = lds64v(vb + 1736); B3 = v4u{L3.x, L3.y, H3.x, H3.y};
    mfma_ip(acc[1], A1b, B0b);
    A4 = lds128v(va + 1728);
    mfma_ip(acc[7], A5, B2a);
    L4 = lds64v(vb + 2240);
    mfma_ip(acc[2], A1b, B1b);
    H4 = lds64v(vb + 2248); B4 = v4u{L4.x, L4.y, H4.x, H4.y};
    mfma_ip(acc[5], A0a, B5);
    mfma_ip(acc[1], A0b, B1b);
    mfma_ip(acc[6], A1a, B5);
    mfma_ip(acc[2], A2b, B0b);
    mfma_ip(acc[7], A2a, B5);
    mfma_ip(acc[3], A2b, B1b);
    mfma_ip(acc[6], A6, B0a);
    mfma_ip(acc[4], A2b, B2b);
    mfma_ip(acc[7], A6, B1a);
    mfma_ip(acc[2], A0b, B2b);
    mfma_ip(acc[6], A0a, B6);
    mfma_ip(acc[3], A1b, B2b);
    mfma_ip(acc[7], A1a, B6);
    mfma_ip(acc[7], A7, B0a);
    mfma_ip(acc[7], A0a, B7);
    mfma_ip(acc[3], A3, B0b);
    A5 = lds128v(va + 1216);
    mfma_ip(acc[4], A3, B1b);
    L5 = lds64v(vb + 2752);
    mfma_ip(acc[5], A3, B2b);
    H5 = lds64v(vb + 2760); B5 = v4u{L5.x, L5.y, H5.x, H5.y};
    mfma_ip(acc[6], A3, B3);
    A6 = lds128v(va + 704);
    mfma_ip(acc[3], A0b, B3);
    L6 = lds64v(vb + 3264);
    mfma_ip(acc[4], A1b, B3);
    H6 = lds64v(vb + 3272); B6 = v4u{L6.x, L6.y, H6.x, H6.y};
    mfma_ip(acc[5], A2b, B3);
    A7 = lds128v(va + 192);
    mfma_ip(acc[6], A4, B2b);
    L7 = lds64v(vb + 3776);
    mfma_ip(acc[7], A4, B3);
    H7 = lds64v(vb + 3784); B7 = v4u{L7.x, L7.y, H7.x, H7.y};
    mfma_ip(acc[4], A4, B0b);
    mfma_ip(acc[5], A4, B1b);
    mfma_ip(acc[6], A2b, B4);
    mfma_ip(acc[7], A3, B4);
    mfma_ip(acc[4], A0b, B4);
    mfma_ip(acc[5], A1b, B4);
    mfma_ip(acc[5], A5, B0b);
    mfma_ip(acc[6], A5, B1b);
    mfma_ip(acc[7], A5, B2b);
    mfma_ip(acc[5], A0b, B5);
    mfma_ip(acc[6], A1b, B5);
    mfma_ip(acc[7], A2b, B5);
    mfma_ip(acc[6], A6, B0b);
    mfma_ip(acc[7], A6, B1b);
    mfma_ip(acc[6], A0b, B6);
    mfma_ip(acc[7], A1b, B6);
    mfma_ip(acc[7], A7, B0b);
    mfma_ip(acc[7], A0b, B7);
    asm volatile("s_nop 7\n\ts_nop 7" ::: "memory");
    const int chunk = 128 * rh + lane;
    const v4u kva = lds128(cb + CH_R1 + (255 - chunk) * 16), kvb = lds128(cb + CH_R1 + (191 - chunk) * 16);
    PRIO(3);
    FENCE();
    pub(mycv, 2, lane); wait2(cvp, 2);
    {
      const unsigned a = cb + rh * 8192 + (64 * (lane >> 4) + 2 * (lane & 15)) * 4;
#pragma unroll
      for (int i = 0; i < 8; ++i) {
        sts64(a + 1024 * i, __builtin_bit_cast(v2u, f32x2{acc[i][0], acc[i][1]}));
        sts64(a + 1024 * i + 128, __builtin_bit_cast(v2u, f32x2{acc[i][2], acc[i][3]}));
      }
    }
    FENCE();
    pub(mycv, 3, lane); wait2(cvp, 3);
    if (w < 8) hpv = atom_rd(p.ws + WS_FH, ((2 * tt64 + (w & 1)) * 32 + 13 * ((w >> 1) & 1) + lane) * 4, 0x3fffull, epoch);
    conv_final(rs, cb, cch, chunk, kva, dD);
    conv_final(rs, cb, cch, chunk + 64, kvb, dD);
    FENCE();
    pub(mycv, 4, lane);
    if (w >= 8) { WAITV(0); if (lane == 0) flag_set(p.ws + WS_FY, (rh * 640 + cch) * 4, epoch); }
  }
  const int ftok = tid / 10, fc8 = tid % 10;
  unsigned foa = L_O + ftok * OROW + fc8 * 32, xa_ = L_X + tid * 16, sa_ = L_XN + (64 * (tt64 & 1) + ftok) * 4;
  unsigned ya = L_Y + fc8 * 1024 + ((((ftok >> 3) ^ fc8) & 7) << 4) + (ftok & 7) * 2, go_ = (64 * tt64 + ftok) * 1280 + c80 * 160 + fc8 * 16;
  USE(foa); USE(xa_); USE(sa_); USE(ya); USE(go_);
  if (w < 4) {
    const unsigned vfull = pub_addr(W_FULL + 4 * w, lane);
    if (!isfull) {
      PADS_LD();
      wait4(W_FREE, 7);
      H_DMA(); PADS_ST();
      wait1(W_AP, 4);
      if (w == 0) wait4(W_FREE, 8);
      R_DMA();
      WAITV(0); pubv(vfull, 12);
    }
    wait1(W_AP + 4, 4);
    v4u RG[4][8];
#define RG_LD(i, g) do { _Pragma("unroll") for (int j = 0; j < 8; ++j) { if ((j & 3) == 0) WAITV(8); \
      RG[i][j] = __builtin_amdgcn_raw_buffer_load_b128(rs, (unsigned)(w2s - p.ws) + l16, (g) * 15360 + j * 1024, AUX_SC1); } SB0(); } while (0)
#define RG_ST(i, g) do { wait4(W_FREE, 21 + (g) - 4); unsigned a_ = w2d + wslot(g) + l16; USE(a_); \
      _Pragma("unroll") for (int j = 0; j < 8; ++j) { sts128(a_ + j * 1024, RG[i][j]); R5AP(); } FENCE(); pubv(vfull, 21 + (g)); } while (0)
    wait4(W_FREE, 8);
    W2GROUP(2);
    if (isfull) W2GROUP(3);
    wait4(W_FREE, 9); W2GROUP(0);
    wait4(W_FREE, 10); W2GROUP(1);
    RG_LD(0, 4);
    WAITV(8); pubv(vfull, isfull ? 24 : 23);
    RG_LD(1, 5); RG_LD(2, 6); RG_LD(3, 7);
    if (!isfull) { wait4(W_CV, 4); W2GROUP(3); WAITV(0); pubv(vfull, 24); }
    PRIO(0);
    RG_ST(0, 4); RG_ST(1, 5); RG_ST(2, 6); RG_ST(3, 7);
    if (b == 0 && w == 3) {
      poll4(p.ws + WS_FP0, l16, epoch);
      if (lane == 0) __builtin_amdgcn_raw_buffer_store_b32(epoch, rs, WS_EPOCH, 0, AUX_SC1);
    }
    PRIO(0);
  } else if (w < 8) {
    const int cw = w - 4, tp = cw & 1, kh = cw >> 1;
    const unsigned vfree = pub_addr(W_FREE + 4 * cw, lane);
    const unsigned hoff = WS_HF + ((4 * tt64 + 2 * tp) * 54 + 27 * kh) * 1024 + l16;
    constexpr int HW = 8;
    v4u hf[27][2];
    f32x4 acc[2][5];
#pragma unroll
    for (int i = 0; i < 5; ++i) { acc[0][i] = acc[1][i] = f32x4{0.f, 0.f, 0.f, 0.f}; USE(acc[0][i]); USE(acc[1][i]); }
    unsigned wa[2] = {kh * 15360 + l16, kh * 15360 + l16 + 65536};
    unsigned oa = L_O + kh * (64 * OROW) + (32 * tp + (lane & 15)) * OROW + (lane >> 4) * 16;
    USE(wa[0]); USE(wa[1]); USE(oa);
    PRIO(2);
    bool yflag = convw;
    {
      const unsigned fho = ((2 * tt64 + tp) * 32 + 13 * kh + lane) * 4;
      bool ok = false;
      if (convw) ok = wait_eq1<2>(hpv, epoch);
      if (!ok) {
        const unsigned v0 = atom_rd(p.ws + WS_FH, fho, 0x3fffull, epoch);
        ok = wait_eq1<0>(v0, epoch);
        if (convw && lane == 0) flag_set(p.ws + WS_FY, ((w & 1) * 640 + cch) * 4, epoch);
        yflag = false;
        if (!ok) poll_atomic(p.ws + WS_FH, fho, 0x3fffull, epoch);
      }
    }
#define H_LD(i) do { hf[i][0] = __builtin_amdgcn_raw_buffer_load_b128(rs, hoff, (i) * 1024, AUX_SC1); \
      hf[i][1] = __builtin_amdgcn_raw_buffer_load_b128(rs, hoff, (i) * 1024 + 54 * 1024, AUX_SC1); } while (0)
#pragma unroll
    for (int i = 0; i < HW; ++i) H_LD(i);
    v4u a0[5], a1[5];
    v4u fl;
    unsigned flm = 0;
#define P3_AD(ks, i) (wa[wslot((ks) / 3) >> 16] + (wslot((ks) / 3) & 65535) + (((ks) % 3) * 5 + (i)) * 1024)
#define P3_STEP(A, N, ks) do { \
      if (((ks) + 1) % 3 == 0 && (ks) < 26) { if (__builtin_expect(flm < 21 + ((ks) + 1) / 3, 0)) wait4((ks) == 23 ? W_MK : W_FULL, 21 + ((ks) + 1) / 3); FENCE(); } \
      _Pragma("unroll") for (int i = 0; i < 5; ++i) { \
        mfma_ip(acc[0][i], A[i], hf[ks][0]); \
        if ((ks) < 26) N[i] = lds128v(P3_AD((ks) + 1, i)); \
        mfma_ip(acc[1][i], A[i], hf[ks][1]); \
        if (i == 1 && (ks) % 3 == 0 && (ks) < 24) fl = peek4((ks) == 21 ? W_MK : W_FULL); \
        if (i == 3 && (ks) % 3 == 1 && (ks) < 24) flm = min4u(fl); \
        if (i == 1 && (ks) == 25) fl = peek4(W_FREE); \
        if ((ks) >= 1 && (ks) + HW - 1 < 27 && (i == 0 || i == 2)) { SB0(); \
          hf[(ks) + HW - 1][i >> 1] = __builtin_amdgcn_raw_buffer_load_b128(rs, hoff, ((ks) + HW - 1) * 1024 + (i >> 1) * 54 * 1024, AUX_SC1); SB0(); } \
      } \
      if (((ks) + 1) % 3 == 2 && (ks) < 25) pubv(vfree, 21 + ((ks) + 1) / 3); \
      SB0(); \
    } while (0)
    wait4(W_FULL, 21);
#pragma unroll
    for (int i = 0; i < 5; ++i) a0[i] = lds128v(P3_AD(0, i));
    if (yflag) { asm volatile("s_waitcnt vmcnt(%0)" ::"i"(2 * HW) : "memory"); if (lane == 0) flag_set(p.ws + WS_FY, ((w & 1) * 640 + cch) * 4, epoch); }
    SB0();
#pragma unroll
    for (int ks = 0; ks < 26; ks += 2) {
      P3_STEP(a0, a1, ks);
      P3_STEP(a1, a0, ks + 1);
    }
    P3_STEP(a0, a1, 26);
    const v4u fr = fl;
    asm volatile("s_nop 7\n\ts_nop 7" ::: "memory");
    if (min4u(fr) < 27) wait4(W_FREE, 27);
    FENCE();
    {
#pragma unroll
      for (int i = 0; i < 5; ++i) { sts128(oa + i * 64, __builtin_bit_cast(v4u, acc[0][i])); sts128(oa + 16 * OROW + i * 64, __builtin_bit_cast(v4u, acc[1][i])); }
    }
    PRIO(0);
  } else {
    const int aw = w - 8;
    PRIO(1);
    wait4(W_CV + 16, 4);
    if (!convw) { pub(W_AP + 4 * aw, 6, lane); wait4(W_AP, 6); }
    const int npc = aw < 3 ? 3 : 1;
#pragma unroll 1
    for (int i = 0; i < npc; ++i) {
      const int it = (3 * aw + i) * 64 + lane;
      dma16((cptr)p.x + (size_t)(64 * tt64) * 1280 + c80 * 160, (it / 10) * 1280 + (it % 10) * 16, L_X + (3 * aw + i) * 1024);
    }
    if (aw == 2) sts64(L_XN + lane * 8, stq);
    wait4(W_FREE, 25); W2GROUP(8);
    WAITV(0); pub(W_MK + 4 * aw, 29, lane);
    poll_atomic(p.ws + WS_FY, ((tt64 >> 4) * 640 + c80 * 80 + aw * 24 + lane) * 4, aw < 3 ? 0xffffffull : 0xffull, epoch);
#pragma unroll 1
    for (int i = 0; i < npc; ++i) {
      const int pc = 3 * aw + i;
      dma16s(p.ws + WS_YT + (size_t)(c80 * 80 + 8 * pc) * 4096 + tt64 * 128, (lane >> 3) * 4096 + (((lane ^ pc) & 7) << 4), L_Y + pc * 1024);
    }
    WAITV(0);
    PRIO(0);
  }
  BAR0();
  if (tid < 640) {
    f32x4 o0 = __builtin_bit_cast(f32x4, lds128(foa)) + __builtin_bit_cast(f32x4, lds128(foa + 64 * OROW));
    f32x4 o1 = __builtin_bit_cast(f32x4, lds128(foa + 16)) + __builtin_bit_cast(f32x4, lds128(foa + 64 * OROW + 16));
    const v4u xv = lds128(xa_);
    const unsigned fs = lds32(sa_);
    const float fm = bf_lo(fs), fr = __builtin_amdgcn_rcpf(bf_hi(fs));
    f32x2 o[4] = {{o0[0], o0[1]}, {o0[2], o0[3]}, {o1[0], o1[1]}, {o1[2], o1[3]}};
    unsigned xw[4] = {xv.x, xv.y, xv.z, xv.w}, rw[4];
#pragma unroll
    for (int j = 0; j < 4; ++j) {
      const f32x2 y = {bf_lo(*(const LAS u16*)(size_t)(ya + 256 * j)), bf_lo(*(const LAS u16*)(size_t)(ya + 256 * j + 128))};
      const f32x2 gb = silu_z(o[j] * -LOG2E);
      const f32x2 xf = bf2(xw[j]);
      rw[j] = pk_bf(xf + ((xf - fm) * fr) * (y * gb));
    }
    { const __amdgpu_buffer_rsrc_t ro = __builtin_amdgcn_make_buffer_rsrc((void*)p.out, 0, S * D * 2, 0x00020000);
      __builtin_amdgcn_raw_buffer_store_b128(v4u{rw[0], rw[1], rw[2], rw[3]}, ro, go_, 0, AUX_NT); }
  }
}

extern "C" void kernel_launch(void* const* d_in, const int* in_sizes, int n_in, void* d_out, int out_size, void* d_ws, size_t ws_size,
                              hipStream_t stream) {
  Params p;
  p.x = (const u16*)d_in[0]; p.k = (const u16*)d_in[1]; p.v = (const u16*)d_in[2];
  p.shk = (const u16*)d_in[3]; p.sk = (const u16*)d_in[4]; p.sD = (const u16*)d_in[5];
  p.w1 = (const u16*)d_in[7]; p.w2 = (const u16*)d_in[8];
  p.out = (u16*)d_out; p.ws = (unsigned char*)d_ws;
  fused_kernel<<<NWG, NTHR, 0, stream>>>(p);
}
```
